```python
import torch

def _softmax(x, axis=-1):
    return torch.softmax(x, dim=axis)

def _gelu(x):
    return torch.nn.functional.gelu(x, approximate='tanh')

def _silu(x):
    return torch.nn.functional.silu(x)

def _sigmoid(x):
    return torch.sigmoid(x)

def _layernorm(x, axis=-1, eps=1e-05):
    m = x.mean(dim=axis, keepdim=True)
    v = x.var(dim=axis, keepdim=True, unbiased=False)
    return (x - m) / torch.sqrt(v + eps)

def _scatter_add(tgt, idx, src, axis=0):
    return tgt.clone().index_add_(axis, idx, src)

def _segment_sum(src, seg, n):
    out = torch.zeros((n,) + src.shape[1:], dtype=src.dtype)
    return out.index_add_(0, seg, src)

def _pad(x, pw):
    flat = tuple((v for pair in reversed(pw) for v in pair))
    return torch.nn.functional.pad(x, flat)
kernel_name = 'adaLN_cond__sliding_block_cached__geglu_ffn__decode'

def setup_inputs():

    _w = lambda a: (lambda t: t.to(torch.bfloat16) if t.is_floating_point() else t)(torch.as_tensor(a))

    def _A_setup():
        B, Sq, D = (4, 1, 512)
        Hq, Hkv, Sk, Dh = (32, 8, 16384, 128)
        assert Hq % Hkv == 0, (Hq, Hkv)
        return {'x': _w((lambda *s: torch.randn(*s, dtype=torch.bfloat16) * 0.05)(B, Sq, D)), 'k_cache': _w((lambda *s: torch.randn(*s, dtype=torch.bfloat16) * 1.6)(B, Hkv, Sk, Dh)), 'v_cache': _w((lambda *s: torch.randn(*s, dtype=torch.bfloat16) * 1.6)(B, Hkv, Sk, Dh)), 'wq': _w((lambda *s: torch.randn(*s, dtype=torch.bfloat16) * 1.6)(D, Hq * Dh) * D ** (-0.5)), 'wo': _w((lambda *s: torch.randn(*s, dtype=torch.bfloat16) * 1.6)(Hq * Dh, D) * (Hq * Dh) ** (-0.5))}
    _A_in = _A_setup()

    def _B_setup():
        B, S, D, F = (4, 1, 512, 1792)
        return {'x': _w((lambda *s: torch.randn(*s, dtype=torch.bfloat16) * 0.05)(B, S, D)), 'w_gate': _w((lambda *s: torch.randn(*s, dtype=torch.bfloat16) * 0.05)(D, F)), 'w_up': _w((lambda *s: torch.randn(*s, dtype=torch.bfloat16) * 0.05)(D, F)), 'w_down': _w((lambda *s: torch.randn(*s, dtype=torch.bfloat16) * 0.05)(F, D))}
    _B_in = _B_setup()
    return {'A_x': _A_in['x'], 'A_k_cache': _A_in['k_cache'], 'A_v_cache': _A_in['v_cache'], 'A_wq': _A_in['wq'], 'A_wo': _A_in['wo'], 'B_x': _B_in['x'], 'B_w_gate': _B_in['w_gate'], 'B_w_up': _B_in['w_up'], 'B_w_down': _B_in['w_down'], 'c': _w((lambda *s: torch.randn(*s, dtype=torch.bfloat16) * 0.2)(4, 512) * 8.0), 'w_mod': _w((lambda *s: torch.randn(*s, dtype=torch.bfloat16) * 0.2)(512, 6 * 512) * 512 ** (-0.5) * 8.0)}

def reference(A_x, A_k_cache, A_v_cache, A_wq, A_wo, B_x, B_w_gate, B_w_up, B_w_down, c, w_mod):

    def _A_ref(x, k_cache, v_cache, wq, wo):
        B, Sq, D = x.shape
        Hq, Dh = (32, 128)
        Hkv, Sk = (k_cache.shape[1], k_cache.shape[2])
        W = min(4096, max(256, Sk // 4))
        rep = Hq // Hkv
        q = (lambda x, *d: x.permute(*d))((x @ wq).reshape(B, Sq, Hq, Dh), 0, 2, 1, 3)
        k = (lambda x, n, axis: torch.repeat_interleave(x, n, dim=axis))(k_cache[:, :, Sk - W:, :], rep, axis=1)
        v = (lambda x, n, axis: torch.repeat_interleave(x, n, dim=axis))(v_cache[:, :, Sk - W:, :], rep, axis=1)
        scores = torch.einsum('bhqd,bhkd->bhqk', q, k) * Dh ** (-0.5)
        p = _softmax(scores, axis=-1)
        o = torch.einsum('bhqk,bhkd->bhqd', p, v)
        return (lambda x, *d: x.permute(*d))(o, 0, 2, 1, 3).reshape(B, Sq, Hq * Dh) @ wo

    def _B_ref(x, w_gate, w_up, w_down):
        return _gelu(x @ w_gate) * (x @ w_up) @ w_down
    x = A_x
    mod = (_silu(c) @ w_mod).reshape(c.shape[0], 6, 1, -1)
    sa, ca, ga, sb, cb, gb = (mod[:, 0].clone(), mod[:, 1].clone(), mod[:, 2].clone(), mod[:, 3].clone(), mod[:, 4].clone(), mod[:, 5].clone())
    h = _layernorm(x, axis=-1) * (1 + ca) + sa
    x = x + ga * _A_ref(h, A_k_cache, A_v_cache, A_wq, A_wo)
    h = _layernorm(x, axis=-1) * (1 + cb) + sb
    x = x + gb * _B_ref(h, B_w_gate, B_w_up, B_w_down)
    return x
```

```cpp
#include <hip/hip_runtime.h>
#include <hip/hip_bf16.h>
#include <stdint.h>

typedef unsigned short bf16_t;
typedef unsigned v4u __attribute__((ext_vector_type(4)));

constexpr int B = 4, D = 512, HQ = 32, HKV = 8, SK = 16384, DH = 128, W = 4096, F = 1792;
constexpr int NSPLIT = 8, KPS = W / NSPLIT;

__device__ __forceinline__ float bf2f(unsigned b) { return __uint_as_float(b << 16); }
__device__ __forceinline__ float rbf(float f) {
  unsigned u = __float_as_uint(f);
  u += 0x7FFFu + ((u >> 16) & 1u);
  return __uint_as_float(u & 0xFFFF0000u);
}
__device__ __forceinline__ float wsum(float v) {
  for (int o = 32; o > 0; o >>= 1) v += __shfl_xor(v, o);
  return v;
}
__device__ __forceinline__ float wmax(float v) {
  for (int o = 32; o > 0; o >>= 1) v = fmaxf(v, __shfl_xor(v, o));
  return v;
}

template <int K>
__device__ __forceinline__ void gemv64(const float* xin  , const bf16_t* w, int ldw, int n0, float* red  , float out[4]) {
  const int lane = threadIdx.x & 63, wv = threadIdx.x >> 6;
  float acc[4] = {0, 0, 0, 0};
  for (int k = wv; k < K; k += 4) {
    float wvv = bf2f(w[(size_t)k * ldw + n0 + lane]);
#pragma unroll
    for (int b = 0; b < 4; ++b) acc[b] = fmaf(xin[b * K + k], wvv, acc[b]);
  }
#pragma unroll
  for (int b = 0; b < 4; ++b) red[(wv * 4 + b) * 64 + lane] = acc[b];
  __syncthreads();
#pragma unroll
  for (int b = 0; b < 4; ++b) out[b] = red[(0 * 4 + b) * 64 + lane] + red[(1 * 4 + b) * 64 + lane] + red[(2 * 4 + b) * 64 + lane] + red[(3 * 4 + b) * 64 + lane];
  __syncthreads();
}

__global__ void k_mod(const bf16_t* c, const bf16_t* wmod, float* mod) {
  __shared__ float xin[4 * 512];
  __shared__ float red[16 * 64];
  for (int i = threadIdx.x; i < 2048; i += 256) { float v = bf2f(c[i]); xin[i] = rbf(v / (1.f + expf(-v))); }
  __syncthreads();
  float o[4];
  gemv64<512>(xin, wmod, 3072, blockIdx.x * 64, red, o);
  if (threadIdx.x < 64) for (int b = 0; b < 4; ++b) mod[b * 3072 + blockIdx.x * 64 + threadIdx.x] = rbf(o[b]);
}

__device__ __forceinline__ void ln_mod(const float xr[8], const float* shift, const float* scale, float* hout) {
  const int lane = threadIdx.x & 63;
  float s = 0;
  for (int i = 0; i < 8; ++i) s += xr[i];
  s = wsum(s);
  const float mean = s * (1.f / 512.f);
  float v = 0;
  for (int i = 0; i < 8; ++i) { float d = xr[i] - mean; v += d * d; }
  v = wsum(v) * (1.f / 512.f);
  const float mb = rbf(mean), vb = rbf(v);
  const float den = rbf(sqrtf(rbf(vb + 1e-5f)));
  for (int i = 0; i < 8; ++i) {
    int col = lane + 64 * i;
    float ln = rbf(rbf(xr[i] - mb) / den);
    hout[col] = rbf(rbf(ln * rbf(1.f + scale[col])) + shift[col]);
  }
}

__global__ void k_q(const bf16_t* x, const float* mod, const bf16_t* wq, float* q) {
  __shared__ float xin[4 * 512];
  __shared__ float red[16 * 64];
  const int lane = threadIdx.x & 63, b = threadIdx.x >> 6;
  float xr[8];
  for (int i = 0; i < 8; ++i) xr[i] = bf2f(x[b * 512 + lane + 64 * i]);
  ln_mod(xr, mod + b * 3072, mod + b * 3072 + 512, xin + b * 512);
  __syncthreads();
  float o[4];
  gemv64<512>(xin, wq, 4096, blockIdx.x * 64, red, o);
  if (threadIdx.x < 64) for (int bb = 0; bb < 4; ++bb) q[bb * 4096 + blockIdx.x * 64 + threadIdx.x] = rbf(o[bb]);
}

__global__ void __launch_bounds__(512) k_attn(const float* q, const bf16_t* kc, const bf16_t* vc, float* pm, float* pl, float* po) {
  __shared__ float qs[4 * 128];
  __shared__ float sc[4 * KPS];
  __shared__ float red[8 * 4];
  __shared__ float oacc[8 * 4 * 128];
  const int s = blockIdx.x % NSPLIT, kvh = (blockIdx.x / NSPLIT) % HKV, b = blockIdx.x / (NSPLIT * HKV);
  const int tid = threadIdx.x, lane = tid & 63, wv = tid >> 6;
  qs[tid] = q[b * 4096 + kvh * 512 + tid];
  __syncthreads();
  const size_t base = ((size_t)(b * HKV + kvh) * SK + (SK - W) + s * KPS) * DH;
  {
    const bf16_t* kr = kc + base + (size_t)tid * DH;
    float a[4] = {0, 0, 0, 0};
    for (int c = 0; c < 16; ++c) {
      v4u kv = *(const v4u*)(kr + c * 8);
      for (int j = 0; j < 4; ++j) {
        float k0 = bf2f(kv[j] & 0xffff), k1 = bf2f(kv[j] >> 16);
        for (int h = 0; h < 4; ++h) { a[h] = fmaf(qs[h * 128 + c * 8 + 2 * j], k0, a[h]); a[h] = fmaf(qs[h * 128 + c * 8 + 2 * j + 1], k1, a[h]); }
      }
    }
    float m[4];
    for (int h = 0; h < 4; ++h) { a[h] = rbf(rbf(a[h]) * 0.08838834764831845f); m[h] = wmax(a[h]); }
    if (lane == 0) for (int h = 0; h < 4; ++h) red[wv * 4 + h] = m[h];
    __syncthreads();
    for (int h = 0; h < 4; ++h) { float mm = red[h]; for (int w = 1; w < 8; ++w) mm = fmaxf(mm, red[w * 4 + h]); m[h] = mm; }
    __syncthreads();
    float l[4];
    for (int h = 0; h < 4; ++h) { float p = expf(a[h] - m[h]); sc[h * KPS + tid] = p; l[h] = wsum(p); }
    if (lane == 0) for (int h = 0; h < 4; ++h) red[wv * 4 + h] = l[h];
    __syncthreads();
    if (tid < 4) { float ll = 0; for (int w = 0; w < 8; ++w) ll += red[w * 4 + tid]; pm[blockIdx.x * 4 + tid] = m[tid]; pl[blockIdx.x * 4 + tid] = ll; }
  }
  {
    float acc[4][2] = {};
    for (int kk = wv; kk < KPS; kk += 8) {
      unsigned vv = *(const unsigned*)(vc + base + (size_t)kk * DH + lane * 2);
      float v0 = bf2f(vv & 0xffff), v1 = bf2f(vv >> 16);
      for (int h = 0; h < 4; ++h) { float p = sc[h * KPS + kk]; acc[h][0] = fmaf(p, v0, acc[h][0]); acc[h][1] = fmaf(p, v1, acc[h][1]); }
    }
    for (int h = 0; h < 4; ++h) { oacc[(wv * 4 + h) * 128 + lane * 2] = acc[h][0]; oacc[(wv * 4 + h) * 128 + lane * 2 + 1] = acc[h][1]; }
    __syncthreads();
    float o = 0;
    for (int w = 0; w < 8; ++w) o += oacc[w * 512 + tid];
    po[(size_t)blockIdx.x * 512 + tid] = o;
  }
}

__global__ void k_comb(const float* pm, const float* pl, const float* po, float* o) {
  const int g = blockIdx.x, tid = threadIdx.x, h = tid >> 7;
  float M = -INFINITY;
  for (int s = 0; s < 8; ++s) M = fmaxf(M, pm[(g * 8 + s) * 4 + h]);
  float L = 0, a = 0;
  for (int s = 0; s < 8; ++s) { float w = expf(pm[(g * 8 + s) * 4 + h] - M); L += w * pl[(g * 8 + s) * 4 + h]; a += w * po[(size_t)(g * 8 + s) * 512 + tid]; }
  o[g * 512 + tid] = rbf(a / L);
}

__global__ void k_wo(const float* o, const bf16_t* wo, const bf16_t* x, const float* mod, float* x1) {
  __shared__ float xin[4 * 4096];
  __shared__ float red[16 * 64];
  for (int i = threadIdx.x; i < 4 * 4096; i += 256) xin[i] = o[i];
  __syncthreads();
  float r[4];
  gemv64<4096>(xin, wo, 512, blockIdx.x * 64, red, r);
  if (threadIdx.x < 64) for (int b = 0; b < 4; ++b) {
    int n = blockIdx.x * 64 + threadIdx.x;
    x1[b * 512 + n] = rbf(bf2f(x[b * 512 + n]) + rbf(mod[b * 3072 + 1024 + n] * rbf(r[b])));
  }
}

__device__ __forceinline__ float gelu_t(float x) {
  float u = 0.7978845608028654f * (x + 0.044715f * x * x * x);
  return 0.5f * x * (1.f + tanhf(u));
}

__global__ void k_gu(const float* x1, const float* mod, const bf16_t* wg, const bf16_t* wu, float* act) {
  __shared__ float xin[4 * 512];
  __shared__ float red[16 * 64];
  const int lane = threadIdx.x & 63, b = threadIdx.x >> 6;
  float xr[8];
  for (int i = 0; i < 8; ++i) xr[i] = x1[b * 512 + lane + 64 * i];
  ln_mod(xr, mod + b * 3072 + 1536, mod + b * 3072 + 2048, xin + b * 512);
  __syncthreads();
  float g[4], u[4];
  gemv64<512>(xin, wg, F, blockIdx.x * 64, red, g);
  gemv64<512>(xin, wu, F, blockIdx.x * 64, red, u);
  if (threadIdx.x < 64) for (int bb = 0; bb < 4; ++bb) act[bb * F + blockIdx.x * 64 + threadIdx.x] = rbf(rbf(gelu_t(rbf(g[bb]))) * rbf(u[bb]));
}

__global__ void k_down(const float* act, const bf16_t* wd, const float* x1, const float* mod, bf16_t* out) {
  __shared__ float xin[4 * F];
  __shared__ float red[16 * 64];
  for (int i = threadIdx.x; i < 4 * F; i += 256) xin[i] = act[i];
  __syncthreads();
  float r[4];
  gemv64<F>(xin, wd, 512, blockIdx.x * 64, red, r);
  if (threadIdx.x < 64) for (int b = 0; b < 4; ++b) {
    int n = blockIdx.x * 64 + threadIdx.x;
    float v = rbf(x1[b * 512 + n] + rbf(mod[b * 3072 + 2560 + n] * rbf(r[b])));
    out[b * 512 + n] = (bf16_t)(__float_as_uint(v) >> 16);
  }
}

extern "C" void kernel_launch(void* const* d_in, const int* in_sizes, int n_in, void* d_out, int out_size, void* d_ws, size_t ws_size, hipStream_t stream) {
  const bf16_t* x = (const bf16_t*)d_in[0];
  const bf16_t* kc = (const bf16_t*)d_in[1];
  const bf16_t* vc = (const bf16_t*)d_in[2];
  const bf16_t* wq = (const bf16_t*)d_in[3];
  const bf16_t* wo = (const bf16_t*)d_in[4];
  const bf16_t* wg = (const bf16_t*)d_in[6];
  const bf16_t* wu = (const bf16_t*)d_in[7];
  const bf16_t* wd = (const bf16_t*)d_in[8];
  const bf16_t* c = (const bf16_t*)d_in[9];
  const bf16_t* wmod = (const bf16_t*)d_in[10];
  float* ws = (float*)d_ws;
  float* mod = ws;
  float* q = mod + 12288;
  float* pm = q + 16384;
  float* pl = pm + 1024;
  float* po = pl + 1024;
  float* o = po + 131072;
  float* x1 = o + 16384;
  float* act = x1 + 2048;
  k_mod<<<48, 256, 0, stream>>>(c, wmod, mod);
  k_q<<<64, 256, 0, stream>>>(x, mod, wq, q);
  k_attn<<<256, 512, 0, stream>>>(q, kc, vc, pm, pl, po);
  k_comb<<<32, 512, 0, stream>>>(pm, pl, po, o);
  k_wo<<<8, 256, 0, stream>>>(o, wo, x, mod, x1);
  k_gu<<<28, 256, 0, stream>>>(x1, mod, wg, wu, act);
  k_down<<<8, 256, 0, stream>>>(act, wd, x1, mod, (bf16_t*)d_out);
}
```

```cpp
#include <hip/hip_runtime.h>
#include <hip/hip_bf16.h>
#include <stdint.h>

#define GAS __attribute__((address_space(1)))
#define LAS __attribute__((address_space(3)))
typedef unsigned short bf16_t;
typedef unsigned v4u __attribute__((ext_vector_type(4)));
typedef unsigned v2u __attribute__((ext_vector_type(2)));
typedef float f32x4 __attribute__((ext_vector_type(4)));
typedef float f32x2 __attribute__((ext_vector_type(2)));
typedef __bf16 bf16x8 __attribute__((ext_vector_type(8)));
typedef __bf16 bf16x2 __attribute__((ext_vector_type(2)));
typedef short s16x4 __attribute__((ext_vector_type(4)));
typedef __amdgpu_buffer_rsrc_t rsrc_t;

#define PACE 4
#define AUX_W 2
#define DLY_H4 0

#define DLY_MOD 24
#define DLY_Q 24
#define DLY_ST 24
#define DLY_H2 0

namespace {
constexpr int SK = 16384, WIN = 4096, FF = 1792;
constexpr int AUX_SC1 = 16, AUX_NT = 2;
constexpr int WS_EPOCH = 0;
constexpr int WS_X1 = 4096;
constexpr int WS_SB = WS_X1 + 8192;
constexpr int WS_CB = WS_SB + 8192;
constexpr int WS_ST = WS_CB + 8192;
constexpr int WS_MOD = WS_ST + 32768;
constexpr int WS_QP = WS_MOD + 196608;
constexpr int WS_H1 = WS_QP + 262144;
constexpr int WS_H2 = WS_H1 + 524288;
constexpr int WS_H4 = WS_H2 + 1048576;
constexpr int WS_END = WS_H4 + 917504;
constexpr int WS_STAMP = 8 << 20;
constexpr int L_K = 0;
constexpr int L_FB = 0;
constexpr int L_SC = 32768;
constexpr int L_P = 131072;
constexpr int L_QB = L_P + 4096;
constexpr int L_HQ = L_QB + 1280;
constexpr int L_ZERO = L_HQ + 1024;
constexpr int L_FLAG = L_ZERO + 1024;
constexpr int L_GG = L_FLAG + 256;
constexpr int L_OA = L_GG + 384;
constexpr int L_F = L_OA + 256;
constexpr int L_ACT = L_F + 64;
constexpr int L_RED = L_ACT + 128;
constexpr int L_MRG = L_RED + 2048;
constexpr int L_HB = L_MRG + 8192;
constexpr int L_END = L_HB + 4096 + 64;
enum { F_QPUB = 0, F_HCNT = 1, F_QCNT = 2, F_WQ = 3, F_K = 4, F_P = 8, F_F = 12 };
constexpr unsigned SPIN_G = 1u << 16, SPIN_L = 1u << 20;

__device__ __forceinline__ float bflo(unsigned u) { return __uint_as_float(u << 16); }
__device__ __forceinline__ float bfhi(unsigned u) { return __uint_as_float(u & 0xFFFF0000u); }
__device__ __forceinline__ unsigned pack2(float lo, float hi) {
  f32x2 v = {lo, hi};
  return __builtin_bit_cast(unsigned, __builtin_convertvector(v, bf16x2));
}
__device__ __forceinline__ float rbf(float x) { return bflo(pack2(x, x) & 0xFFFFu); }
__device__ __forceinline__ float fmx(float a, float b) { return __builtin_amdgcn_fmed3f(a, b, __builtin_inff()); }
template <int CTRL> __device__ __forceinline__ float dpp(float x) {
  return __builtin_bit_cast(float, __builtin_amdgcn_mov_dpp(__builtin_bit_cast(int, x), CTRL, 0xf, 0xf, true));
}
constexpr int XOR1 = 0xB1, XOR2 = 0x4E, HMIR = 0x141, ROR2 = 0x122, ROR4 = 0x124, ROR8 = 0x128;
__device__ __forceinline__ float xrow_sum(float x) {
  auto s = __builtin_amdgcn_permlane16_swap(__float_as_uint(x), __float_as_uint(x), false, false);
  x = __uint_as_float(s[0]) + __uint_as_float(s[1]);
  auto t = __builtin_amdgcn_permlane32_swap(__float_as_uint(x), __float_as_uint(x), false, false);
  return __uint_as_float(t[0]) + __uint_as_float(t[1]);
}
__device__ __forceinline__ float xrow_max(float x) {
  auto s = __builtin_amdgcn_permlane16_swap(__float_as_uint(x), __float_as_uint(x), false, false);
  x = fmx(__uint_as_float(s[0]), __uint_as_float(s[1]));
  auto t = __builtin_amdgcn_permlane32_swap(__float_as_uint(x), __float_as_uint(x), false, false);
  return fmx(__uint_as_float(t[0]), __uint_as_float(t[1]));
}
__device__ __forceinline__ float wave_sum(float x) {
  x += dpp<XOR1>(x); x += dpp<XOR2>(x); x += dpp<ROR4>(x); x += dpp<ROR8>(x);
  return xrow_sum(x);
}
__device__ __forceinline__ float wave_max(float x) {
  x = fmx(x, dpp<XOR1>(x)); x = fmx(x, dpp<XOR2>(x)); x = fmx(x, dpp<ROR4>(x)); x = fmx(x, dpp<ROR8>(x));
  return xrow_max(x);
}
__device__ __forceinline__ f32x4 mfma32(v4u a, v4u b, f32x4 c) {
  return __builtin_amdgcn_mfma_f32_16x16x32_bf16(__builtin_bit_cast(bf16x8, a), __builtin_bit_cast(bf16x8, b), c, 0, 0, 0);
}
__device__ __forceinline__ f32x4 mfma16(v2u a, v2u b, f32x4 c) {
  return __builtin_amdgcn_mfma_f32_16x16x16bf16_1k(__builtin_bit_cast(s16x4, a), __builtin_bit_cast(s16x4, b), c, 0, 0, 0);
}
template <int E> __device__ __forceinline__ v4u tcol8(const v4u* r) {
  constexpr unsigned sel = (E & 1) ? 0x07060302u : 0x05040100u;
  v4u o;
  o.x = __builtin_amdgcn_perm(r[1][E >> 1], r[0][E >> 1], sel);
  o.y = __builtin_amdgcn_perm(r[3][E >> 1], r[2][E >> 1], sel);
  o.z = __builtin_amdgcn_perm(r[5][E >> 1], r[4][E >> 1], sel);
  o.w = __builtin_amdgcn_perm(r[7][E >> 1], r[6][E >> 1], sel);
  return o;
}
template <int E> __device__ __forceinline__ v2u tcol4(const v4u* r) {
  constexpr unsigned sel = (E & 1) ? 0x07060302u : 0x05040100u;
  v2u o;
  o.x = __builtin_amdgcn_perm(r[1][E >> 1], r[0][E >> 1], sel);
  o.y = __builtin_amdgcn_perm(r[3][E >> 1], r[2][E >> 1], sel);
  return o;
}
#define FOR8(X) X(0) X(1) X(2) X(3) X(4) X(5) X(6) X(7)
__device__ __forceinline__ rsrc_t mkrsrc(const void* p, int bytes) { return __builtin_amdgcn_make_buffer_rsrc((void*)p, 0, bytes, 0x00020000); }
template <int AUX> __device__ __forceinline__ v4u bld(rsrc_t r, int voff, int soff) { return __builtin_amdgcn_raw_buffer_load_b128(r, voff, soff, AUX); }
__device__ __forceinline__ v4u put4(f32x4 v, unsigned tag) {
  v4u o;
  o.x = __builtin_amdgcn_perm(__float_as_uint(v.x), tag, 0x07060500u);
  o.y = __builtin_amdgcn_perm(__float_as_uint(v.y), tag, 0x07060501u);
  o.z = __builtin_amdgcn_perm(__float_as_uint(v.z), tag, 0x07060502u);
  o.w = __builtin_amdgcn_perm(__float_as_uint(v.w), tag, 0x07060503u);
  return o;
}
__device__ __forceinline__ unsigned tagof(v4u v) { return __builtin_amdgcn_perm(v.y, v.x, 0x0c0c0400u) | __builtin_amdgcn_perm(v.w, v.z, 0x04000c0cu); }
__device__ __forceinline__ f32x4 get4(v4u v) { return __builtin_bit_cast(f32x4, v & 0xFFFFFF00u); }
__device__ __forceinline__ void st16(rsrc_t r, int off, v4u v) { __builtin_amdgcn_raw_buffer_store_b128(v, r, off, 0, AUX_SC1); }
#define CBAR() asm volatile("" ::: "memory")
#define LGKM0() asm volatile("s_waitcnt lgkmcnt(0)" ::: "memory")
#define SB() __builtin_amdgcn_sched_barrier(0)
#define PIN(x) asm volatile("" : "+v"(x))
__device__ __forceinline__ void lds_wait(volatile LAS unsigned* f, unsigned want = 1u) {
#pragma unroll 1
  for (unsigned i = 0; i < SPIN_L; ++i) { if (*f >= want) break; __builtin_amdgcn_s_sleep(1); }
  CBAR();
}
__device__ __forceinline__ void lds_set(volatile LAS unsigned* f) { LGKM0(); *f = 1u; }
__device__ __forceinline__ void lds_arrive(LAS unsigned* f, int lane) {
  LGKM0();
  if (lane == 0) __hip_atomic_fetch_add(f, 1u, __ATOMIC_RELAXED, __HIP_MEMORY_SCOPE_WORKGROUP);
}
__device__ __forceinline__ void wg_barrier() { LGKM0(); __builtin_amdgcn_s_barrier(); CBAR(); }
template <int N, int SLEEP> __device__ __forceinline__ void sweep(rsrc_t rs, const int (&off)[N], f32x4 (&o)[N], unsigned tag) {
  v4u v[N];
#pragma unroll 1
  for (unsigned spin = 0;; ++spin) {
    unsigned bad = 0;
#pragma unroll
    for (int i = 0; i < N; ++i) v[i] = bld<AUX_SC1>(rs, off[i], 0);
#pragma unroll
    for (int i = 0; i < N; ++i) bad |= tagof(v[i]) ^ tag;
    if (__all(bad == 0u) || spin >= SPIN_G) break;
    if (SLEEP) __builtin_amdgcn_s_sleep(SLEEP);
    CBAR();
  }
#pragma unroll
  for (int i = 0; i < N; ++i) o[i] = get4(v[i]);
}
__device__ __forceinline__ float fexp(float x) { return __builtin_amdgcn_exp2f(x * 1.4426950408889634f); }
__device__ __forceinline__ float silu_f(float x) { return x * __builtin_amdgcn_rcpf(1.f + fexp(-x)); }
__device__ __forceinline__ float gelu_f(float x) {
  const float u = 0.7978845608028654f * (x + 0.044715f * x * x * x);
  return x * __builtin_amdgcn_rcpf(1.f + fexp(-2.f * u));
}
__device__ __forceinline__ f32x4 gemv128x64(v4u a, const v4u* bt, int n) {
  const bool hi = n >= 8;
  float val[8];
#pragma unroll
  for (int e = 0; e < 8; ++e) {
    const f32x4 c0 = mfma32(a, bt[e], f32x4{0.f, 0.f, 0.f, 0.f}), c1 = mfma32(a, bt[8 + e], f32x4{0.f, 0.f, 0.f, 0.f});
    float v_ = hi ? c0[1] + c1[3] : c0[0] + c1[2];
    v_ += dpp<ROR8>(v_);
    val[e] = v_;
  }
  return f32x4{hi ? val[4] : val[0], hi ? val[5] : val[1], hi ? val[6] : val[2], hi ? val[7] : val[3]};
}
__device__ __forceinline__ float sel4(bool b0, bool b1, float c0, float c1, float c2, float c3) {
  const float lo = b0 ? c1 : c0, hi = b0 ? c3 : c2;
  return b1 ? hi : lo;
}
__device__ __forceinline__ f32x2 rbf2(f32x2 v) {
  const unsigned p = pack2(v.x, v.y);
  return f32x2{bflo(p), bfhi(p)};
}
template <bool MAX> __device__ __forceinline__ float red2(float a, float b) { return MAX ? fmx(a, b) : a + b; }
template <bool MAX> __device__ __forceinline__ f32x4 wave_red4(f32x4 s, int lane) {
  const bool o1 = lane & 1, o2 = lane & 2;
  const float a = red2<MAX>(o1 ? s[1] : s[0], dpp<XOR1>(o1 ? s[0] : s[1])), c = red2<MAX>(o1 ? s[3] : s[2], dpp<XOR1>(o1 ? s[2] : s[3]));
  float m = red2<MAX>(o2 ? c : a, dpp<XOR2>(o2 ? a : c));
  m = red2<MAX>(m, dpp<ROR4>(m)); m = red2<MAX>(m, dpp<ROR8>(m));
  m = MAX ? xrow_max(m) : xrow_sum(m);
  return f32x4{dpp<0x00>(m), dpp<0x55>(m), dpp<0xAA>(m), dpp<0xFF>(m)};
}
#define STAMP(i) do {} while (0)
#define STAMP_OUT() do {} while (0)
}

__global__ void __launch_bounds__(512, 2)
adaln_block_kernel(const bf16_t* __restrict__ x, const bf16_t* __restrict__ kc, const bf16_t* __restrict__ vc,
                   const bf16_t* __restrict__ wq, const bf16_t* __restrict__ wo, const bf16_t* __restrict__ wg,
                   const bf16_t* __restrict__ wu, const bf16_t* __restrict__ wd, const bf16_t* __restrict__ cc,
                   const bf16_t* __restrict__ wmod, bf16_t* __restrict__ out, unsigned char* ws) {
  __shared__ __attribute__((aligned(16))) unsigned char lds_raw[L_END];
  LAS unsigned char* L = (LAS unsigned char*)lds_raw;
  volatile LAS unsigned* FL = (volatile LAS unsigned*)(L + L_FLAG);
  const int tid = threadIdx.x, lane = tid & 63, wv = __builtin_amdgcn_readfirstlane(tid >> 6);
  const int g = blockIdx.x, b = g >> 6, kvh = (g >> 3) & 7, s = g & 7;
  const int n = lane & 15, kg = lane >> 4;
  const rsrc_t rws = mkrsrc(ws, WS_END);
  STAMP(0);
  const unsigned epoch = __builtin_amdgcn_raw_buffer_load_b32(rws, WS_EPOCH, 0, AUX_SC1);
  const int ru = ((g >> 5) << 3) + (g & 7);
  const int jsl = 4 * ru + ((g >> 3) & 3);
  const bool roleG = (g < 128) && (ru < 28), roleR = g >= 192;
  unsigned tag;

  if (__builtin_expect(wv >= 4, 1)) {
    const int w = wv - 4;
    const size_t kvbase = ((size_t)(b * 8 + kvh) * SK + (SK - WIN) + s * 512) * 128;
    const int rot = ((g >> 3) * 4 + (g & 3)) & 127;
    const unsigned char* kbase = (const unsigned char*)(kc + kvbase);
    const unsigned ldsk = (unsigned)(uintptr_t)(L + L_K);
    const int klane = kg * 256 + ((n ^ kg) << 4);
    wg_barrier();
#pragma unroll 4
    for (int i = 0; i < 32; ++i) {
      const int phys = (4 * i + w + rot) & 127;
      const unsigned char* sb_ = kbase + phys * 1024;
      const int vo = klane ^ ((i & 3) << 6);
      const unsigned dst = ldsk + (4 * i + w) * 1024;
      unsigned keep;
      asm volatile("s_mov_b32 %0, m0\n\ts_mov_b32 m0, %3\n\ts_nop 0\n\tglobal_load_lds_dwordx4 %1, %2 nt\n\ts_mov_b32 m0, %0\n\ts_waitcnt vmcnt(%4)"
                   : "=&s"(keep) : "v"(vo), "s"(sb_), "s"(dst), "i"(PACE) : "memory");
    }
    STAMP(1);
    const rsrc_t rv = mkrsrc(vc + kvbase, 131072);
    v4u V[32];
#pragma unroll
    for (int i = 0; i < 32; ++i) {
      V[i] = bld<AUX_NT>(rv, lane * 16, ((4 * i + w + rot) & 127) * 1024);
      if (i < 31) asm volatile("s_waitcnt vmcnt(%0)" :: "i"(PACE) : "memory");
      if (i == PACE - 1) { FL[F_K + w] = 1u; STAMP(2); }
      SB();
    }
    tag = __builtin_amdgcn_readfirstlane(epoch) + 0x01010101u;
    STAMP(3);
    lds_wait(FL + F_P + w);
    STAMP(4);
    f32x4 acc[2] = {f32x4{0.f, 0.f, 0.f, 0.f}, f32x4{0.f, 0.f, 0.f, 0.f}};
    int paddr[4];
    f32x4 f;
#pragma unroll
    for (int ep = 0; ep < 4; ++ep) paddr[ep] = (n >> 2) == ep ? L_P + w * 1024 + (((n & 3) * 4 + kg) << 4) : L_ZERO;
#pragma unroll
    for (int G = 0; G < 4; ++G) {
      v4u pa[4];
#pragma unroll
      for (int ep = 0; ep < 4; ++ep) pa[ep] = *(const LAS v4u*)(L + paddr[ep] + G * 256);
      if (G == 3) {
        lds_wait(FL + F_F + w);
        f = *(const LAS f32x4*)(L + L_F + w * 16);
        PIN(pa[0]); PIN(pa[1]); PIN(pa[2]); PIN(pa[3]); PIN(f); SB(); STAMP(5);
      }
#define PV(E) acc[E & 1] = mfma32(pa[E >> 1], tcol8<E>(V + 8 * G), acc[E & 1]);
      FOR8(PV)
#undef PV
      SB();
    }
    STAMP(6);
#pragma unroll
    for (int h = 0; h < 4; ++h) *(LAS f32x2*)(L + L_MRG + (w * 512 + h * 128 + 8 * n + 2 * kg) * 4) = f32x2{acc[0][h], acc[1][h]} * f[h];
  } else {
    const int cw = wv;
    __builtin_amdgcn_s_setprio(2);
    const int ks = g >> 6, ns = g & 63, u5 = lane & 31, h5 = lane >> 5;
    const int mkq = g < 64 ? g >> 4 : (g - 64) >> 5, mcb = g < 64 ? g & 15 : 16 + ((g - 64) & 31);
    v4u wr[16], cf;
    if (cw == 0 && g < 192) {
      const rsrc_t rm = mkrsrc(wmod + (size_t)mkq * 128 * 3072, 128 * 6144);
      const int off = (32 * (n >> 3) + 8 * kg) * 6144 + mcb * 128 + (n & 7) * 16;
      cf = *(const v4u*)(cc + (n >> 2) * 512 + 128 * mkq + 32 * (n & 3) + 8 * kg);
#pragma unroll
      for (int j = 0; j < 16; ++j) wr[j] = bld<AUX_NT>(rm, off, ((j >> 3) * 64 + (j & 7)) * 6144);
    }
    const v4u xs = *(const v4u*)(x + cw * 512 + lane * 8);
    const v2u xo = *(const v2u*)(x + cw * 512 + 128 * ks + 4 * u5);
    if (cw == 1) {
      const rsrc_t rq = mkrsrc(wq + (size_t)ks * 128 * 4096, 128 * 8192);
      const int off = (32 * (n >> 3) + 8 * kg) * 8192 + ns * 128 + (n & 7) * 16;
#pragma unroll
      for (int j = 0; j < 16; ++j) wr[j] = bld<AUX_NT>(rq, off, ((j >> 3) * 64 + (j & 7)) * 8192);
    }
    if (cw == 2) FL[lane] = 0u;
    if (cw == 3) *(LAS v4u*)(L + L_ZERO + lane * 16) = v4u{0u, 0u, 0u, 0u};
    wg_barrier();
    STAMP(1);
    v4u bt[16];
#define TQ(E) bt[E] = tcol8<E>(wr); bt[8 + E] = tcol8<E>(wr + 8); PIN(bt[E]); PIN(bt[8 + E]);
    if (cw == 0 && g < 192) {
      v4u a;
#pragma unroll
      for (int i = 0; i < 4; ++i) a[i] = pack2(silu_f(bflo(cf[i])), silu_f(bfhi(cf[i])));
      PIN(a);
      FOR8(TQ)
      st16(rws, WS_MOD + ((mkq * 4 + kg) * 3072 + 64 * mcb + 8 * (n & 7) + 4 * (n >> 3)) * 4, put4(gemv128x64(a, bt, n), epoch + 0x01010101u));
    }
    tag = __builtin_amdgcn_readfirstlane(epoch) + 0x01010101u;
    float ln[4];
    {
      float sum = 0.f;
#pragma unroll
      for (int i = 0; i < 4; ++i) sum += bflo(xs[i]) + bfhi(xs[i]);
      const float mean = wave_sum(sum) * (1.f / 512.f);
      float sq = 0.f;
#pragma unroll
      for (int i = 0; i < 4; ++i) { const float d0 = bflo(xs[i]) - mean, d1 = bfhi(xs[i]) - mean; sq = fmaf(d0, d0, sq); sq = fmaf(d1, d1, sq); }
      sq = wave_sum(sq);
      const float mb = rbf(mean), rden = __builtin_amdgcn_rcpf(rbf(__builtin_amdgcn_sqrtf(rbf(rbf(sq * (1.f / 512.f)) + 1e-5f))));
#pragma unroll
      for (int i = 0; i < 2; ++i) { ln[2 * i] = rbf(rbf(bflo(xo[i]) - mb) * rden); ln[2 * i + 1] = rbf(rbf(bfhi(xo[i]) - mb) * rden); }
    }
    if (cw == 1) { FOR8(TQ) }
#undef TQ
    if (cw == 1) FL[F_WQ] = 1u;
    lds_wait(FL + F_WQ);
    STAMP(2);
    __builtin_amdgcn_s_sleep(DLY_MOD);
    {
      int off[4]; f32x4 v[4];
#pragma unroll
      for (int i = 0; i < 4; ++i) off[i] = WS_MOD + ((i * 4 + cw) * 3072 + h5 * 512 + 128 * ks + 4 * u5) * 4;
      sweep<4, 4>(rws, off, v, tag);
      const f32x4 t = (v[0] + v[1]) + (v[2] + v[3]);
      float h[4];
#pragma unroll
      for (int e = 0; e < 4; ++e) {
        auto p = __builtin_amdgcn_permlane32_swap(__float_as_uint(t[e]), __float_as_uint(t[e]), false, false);
        h[e] = rbf(ln[e] * rbf(1.f + rbf(__uint_as_float(p[1])))) + rbf(__uint_as_float(p[0]));
      }
      if (lane < 32) *(LAS v2u*)(L + L_HQ + cw * 256 + u5 * 8) = v2u{pack2(h[0], h[1]), pack2(h[2], h[3])};
      lds_arrive((LAS unsigned*)(L + L_FLAG) + F_HCNT, lane);
    }
    STAMP(3);
    if (cw == 1) {
      lds_wait(FL + F_HCNT, 4u);
      const v4u a = *(const LAS v4u*)(L + L_HQ + (n >> 2) * 256 + (32 * (n & 3) + 8 * kg) * 2);
      st16(rws, WS_QP + ((kg * 4 + ks) * 4096 + 64 * ns + 8 * (n & 7) + 4 * (n >> 3)) * 4, put4(gemv128x64(a, bt, n), tag));
      FL[F_QPUB] = 1u;
    } else if (cw == 2 && roleR) {
      const int rr = g - 192;
      const int mo = WS_MOD + ((((lane >> 3) & 3) * 4 + (rr >> 4)) * 3072 + 1024 + h5 * 512 + 32 * (rr & 15) + 4 * (lane & 7)) * 4;
      const int off[2] = {mo, mo + 4096};
      f32x4 v[2];
      const unsigned short xin = x[rr * 32 + u5];
      sweep<2, 4>(rws, off, v, tag);
#pragma unroll
      for (int i = 0; i < 2; ++i)
#pragma unroll
        for (int e = 0; e < 4; ++e) {
          float u = v[i][e]; u += dpp<ROR8>(u);
          auto p = __builtin_amdgcn_permlane16_swap(__float_as_uint(u), __float_as_uint(u), false, false);
          v[i][e] = rbf(__uint_as_float(p[0]) + __uint_as_float(p[1]));
        }
      *(LAS float*)(L + L_GG + 256 + u5 * 4) = bflo(xin);
      if (u5 < 8) {
        if (lane < 32) {
          *(LAS f32x4*)(L + L_GG + lane * 16) = v[0];
          st16(rws, WS_CB + rr * 128 + lane * 16, put4(f32x4{rbf(1.f + v[1][0]), rbf(1.f + v[1][1]), rbf(1.f + v[1][2]), rbf(1.f + v[1][3])}, tag));
        } else {
          *(LAS f32x4*)(L + L_GG + 128 + u5 * 16) = v[1];
          st16(rws, WS_SB + rr * 128 + u5 * 16, put4(v[0], tag));
        }
      }
    }
    lds_wait(FL + F_QPUB);
    STAMP(4);
    {
      __builtin_amdgcn_s_sleep(DLY_Q);
      const int qo = WS_QP + ((b * 4 + h5) * 4096 + kvh * 512 + cw * 128 + 4 * u5) * 4;
      const int off[2] = {qo, qo + 32768};
      f32x4 v[2];
      sweep<2, 4>(rws, off, v, tag);
      const f32x4 t = v[0] + v[1];
      float q[4];
#pragma unroll
      for (int e = 0; e < 4; ++e) {
        auto p = __builtin_amdgcn_permlane32_swap(__float_as_uint(t[e]), __float_as_uint(t[e]), false, false);
        q[e] = __uint_as_float(p[0]) + __uint_as_float(p[1]);
      }
      if (lane < 32) *(LAS v2u*)(L + L_QB + cw * 320 + u5 * 8) = v2u{pack2(q[0], q[1]), pack2(q[2], q[3])};
      lds_arrive((LAS unsigned*)(L + L_FLAG) + F_QCNT, lane);
      lds_wait(FL + F_QCNT, 4u);
    }
    STAMP(5);
    v4u qf[4];
#pragma unroll
    for (int ds = 0; ds < 4; ++ds) qf[ds] = *(const LAS v4u*)(L + L_QB + (n & 3) * 320 + kg * 16 + ds * 64);
    lds_wait(FL + F_K + cw);
    STAMP(6);
    f32x4 sc[2];
    {
      v4u kf[32];
#pragma unroll
      for (int t = 0; t < 8; ++t)
#pragma unroll
        for (int ds = 0; ds < 4; ++ds)
          kf[4 * t + ds] = *(const LAS v4u*)(L + L_K + (16 * (4 * t + (n >> 2)) + 4 * cw + (n & 3)) * 256 + (((4 * ds + kg) ^ n) << 4));
      SB();
      f32x4 c[8];
#pragma unroll
      for (int t = 0; t < 8; ++t) {
        c[t] = f32x4{0.f, 0.f, 0.f, 0.f};
#pragma unroll
        for (int ds = 0; ds < 4; ++ds) c[t] = mfma32(qf[ds], kf[4 * t + ds], c[t]);
      }
#pragma unroll
      for (int sg = 0; sg < 2; ++sg)
#pragma unroll
        for (int h = 0; h < 4; ++h) sc[sg][h] = sel4(lane & 16, lane & 32, c[4 * sg][h], c[4 * sg + 1][h], c[4 * sg + 2][h], c[4 * sg + 3][h]);
    }
    f32x4 e0, e1, mw;
    {
      f32x4 mx;
#pragma unroll
      for (int h = 0; h < 4; ++h) {
        const f32x2 r = rbf2(rbf2(f32x2{sc[0][h], sc[1][h]}) * 0.08838834764831845f);
        e0[h] = r.x; e1[h] = r.y; mx[h] = fmx(r.x, r.y);
      }
      mw = wave_red4<true>(mx, lane);
#pragma unroll
      for (int h = 0; h < 4; ++h) { e0[h] = fexp(e0[h] - mw[h]); e1[h] = fexp(e1[h] - mw[h]); }
      const f32x4 lw = wave_red4<false>(e0 + e1, lane);
      if (lane < 2) st16(rws, WS_ST + g * 128 + cw * 32 + lane * 16, put4(lane == 0 ? mw : lw, tag));
#pragma unroll
      for (int h = 0; h < 4; ++h) {
        const unsigned pp = pack2(e0[h], e1[h]);
        LAS unsigned short* pd = (LAS unsigned short*)(L + L_P) + ((((cw * 4 + (kg >> 1)) * 4 + h) * 4 + (n & 3)) * 8 + 4 * (kg & 1) + (n >> 2));
        pd[0] = (unsigned short)(pp & 0xFFFFu);
        pd[2 * 128] = (unsigned short)(pp >> 16);
      }
      lds_set(FL + F_P + cw);
    }
    STAMP(7);
    {
      __builtin_amdgcn_s_sleep(DLY_ST);
      const int off[1] = {WS_ST + (g >> 3) * 1024 + lane * 16};
      f32x4 v[1];
      sweep<1, 4>(rws, off, v, tag);
      f32x4 m;
#pragma unroll
      for (int h = 0; h < 4; ++h) m[h] = dpp<0xA0>(v[0][h]);
      const f32x4 M = wave_red4<true>(m, lane);
      f32x4 t;
#pragma unroll
      for (int h = 0; h < 4; ++h) t[h] = (lane & 1) ? v[0][h] * fexp(m[h] - M[h]) : 0.f;
      const f32x4 Ls = wave_red4<false>(t, lane);
      f32x4 f;
#pragma unroll
      for (int h = 0; h < 4; ++h) f[h] = fexp(mw[h] - M[h]) * __builtin_amdgcn_rcpf(Ls[h]);
      if (lane == 0) *(LAS f32x4*)(L + L_F + cw * 16) = f;
      lds_set(FL + F_F + cw);
    }
    STAMP(8);
    if (roleG) {
      const int mo = WS_SB + cw * 2048 + lane * 16;
      const int off[4] = {mo, mo + 1024, mo + 8192, mo + 8192 + 1024};
      f32x4 pre[4];
      sweep<4, 4>(rws, off, pre, tag);
#pragma unroll
      for (int i = 0; i < 4; ++i) *(LAS f32x4*)(L + L_SC + (i >> 1) * 8192 + cw * 2048 + (i & 1) * 1024 + lane * 16) = pre[i];
    }
  }
  int h1o = WS_H1 + ((kvh * 16 + (tid >> 3)) * 32 + b * 8 + s) * 128 + (tid & 7) * 16;
  PIN(h1o);
  STAMP(9);
  wg_barrier();
  if (wv < 2) {
    const LAS f32x4* mp = (const LAS f32x4*)(L + L_MRG + tid * 16);
    st16(rws, h1o, put4((mp[0] + mp[128]) + (mp[256] + mp[384]), tag));
  }
  SB();
  __builtin_amdgcn_s_barrier();
  STAMP(10);
  if (g >= 128) {
    const int wj = g - 128;
    v2u wor[8];
    {
      const rsrc_t rwo = mkrsrc(wo + (size_t)wj * 32 * 512, 32768);
#pragma unroll
      for (int j = 0; j < 8; ++j) wor[j] = __builtin_amdgcn_raw_buffer_load_b64(rwo, 8 * kg * 1024 + wv * 128 + n * 8, j * 1024, AUX_NT);
    }
    v4u bo[4];
#pragma unroll
    for (int e = 0; e < 4; ++e) {
      const unsigned sel = (e & 1) ? 0x07060302u : 0x05040100u;
#pragma unroll
      for (int jp = 0; jp < 4; ++jp) bo[e][jp] = __builtin_amdgcn_perm(wor[2 * jp + 1][e >> 1], wor[2 * jp][e >> 1], sel);
      PIN(bo[e]);
    }
    int h2o = WS_H2 + ((kg * 16 + 2 * wv + (n >> 3)) * 128 + wj) * 128 + (n & 7) * 16;
    int oar = L_OA + (n >> 2) * 64 + kg * 16;
    PIN(h2o); PIN(oar);
    SB();
    if (wv < 4) {
      const int off[1] = {WS_H1 + wj * 4096 + wv * 1024 + (lane & 7) * 128 + (lane >> 3) * 16};
      f32x4 v[1];
      sweep<1, 0>(rws, off, v, tag);
#pragma unroll
      for (int e = 0; e < 4; ++e) { float u = v[0][e]; u += dpp<XOR1>(u); u += dpp<XOR2>(u); u += dpp<HMIR>(u); v[0][e] = u; }
      if ((lane & 7) == 0) *(LAS v2u*)(L + L_OA + wv * 64 + (lane >> 3) * 8) = v2u{pack2(v[0][0], v[0][1]), pack2(v[0][2], v[0][3])};
    }
    wg_barrier();
    STAMP(11);
    const v4u a = *(const LAS v4u*)(L + oar);
    f32x4 c[4];
#pragma unroll
    for (int e = 0; e < 4; ++e) c[e] = mfma32(a, bo[e], f32x4{0.f, 0.f, 0.f, 0.f});
    st16(rws, h2o, put4(f32x4{c[0][0], c[1][0], c[2][0], c[3][0]}, tag));
    SB();
    STAMP(12);
  }
  if (roleR) {
    const int rr = g - 192, u5 = lane & 31;
    const unsigned tsel = 0x07060500u + (lane & 3);
    const float ga = *(const LAS float*)(L + L_GG + u5 * 4), gb = *(const LAS float*)(L + L_GG + 128 + u5 * 4), xin = *(const LAS float*)(L + L_GG + 256 + u5 * 4);
    const int lo = (lane & 7) * 128 + (lane >> 3) * 16;
    float x1 = 0.f;
    {
      const int off[2] = {WS_H2 + rr * 16384 + wv * 2048 + lo, WS_H2 + rr * 16384 + wv * 2048 + 1024 + lo};
      f32x4 v[2];
      if (DLY_H2) __builtin_amdgcn_s_sleep(DLY_H2);
      sweep<2, 0>(rws, off, v, tag);
      v[0] += v[1];
#pragma unroll
      for (int e = 0; e < 4; ++e) { float u = v[0][e]; u += dpp<XOR1>(u); u += dpp<XOR2>(u); u += dpp<HMIR>(u); v[0][e] = u; }
      if ((lane & 7) == 0) *(LAS f32x4*)(L + L_RED + wv * 128 + (lane >> 3) * 16) = v[0];
    }
    wg_barrier();
    STAMP(13);
    if (wv == 0) {
      const LAS float* rp = (const LAS float*)(L + L_RED + u5 * 4);
      const float a = ((rp[0] + rp[32]) + (rp[64] + rp[96])) + ((rp[128] + rp[160]) + (rp[192] + rp[224]));
      x1 = rbf(xin + rbf(ga * rbf(a)));
      const float tw = __uint_as_float(__builtin_amdgcn_perm(__float_as_uint(x1), tag, tsel));
      const f32x4 o = {tw, dpp<0x55>(tw), dpp<0xAA>(tw), dpp<0xFF>(tw)};
      if (lane < 32 && (lane & 3) == 0) st16(rws, WS_X1 + rr * 128 + lane * 4, __builtin_bit_cast(v4u, o));
    }
    STAMP(14);
    if (wv < 7) {
      if (DLY_H4) __builtin_amdgcn_s_sleep(DLY_H4);
      const int off[2] = {WS_H4 + rr * 14336 + wv * 2048 + lo, WS_H4 + rr * 14336 + wv * 2048 + 1024 + lo};
      f32x4 v[2];
      sweep<2, 0>(rws, off, v, tag);
      v[0] += v[1];
#pragma unroll
      for (int e = 0; e < 4; ++e) { float u = v[0][e]; u += dpp<XOR1>(u); u += dpp<XOR2>(u); u += dpp<HMIR>(u); v[0][e] = u; }
      if ((lane & 7) == 0) *(LAS f32x4*)(L + L_RED + 1024 + wv * 128 + (lane >> 3) * 16) = v[0];
    }
    wg_barrier();
    if (wv == 0) {
      const LAS float* rp = (const LAS float*)(L + L_RED + 1024 + u5 * 4);
      const float a = ((rp[0] + rp[32]) + (rp[64] + rp[96])) + ((rp[128] + rp[160]) + rp[192]);
      const float o = x1 + rbf(gb * rbf(a));
      const unsigned pk = pack2(o, dpp<XOR1>(o));
      if (lane < 32 && (lane & 1) == 0) *(unsigned*)(out + rr * 32 + lane) = pk;
      if (g == 192 && lane == 0) __builtin_amdgcn_raw_buffer_store_b32(tag, rws, WS_EPOCH, 0, AUX_SC1);
    }
    STAMP(15);
  } else if (roleG) {
    v2u dnr[8];
    {
      v4u gur[8];
      const rsrc_t rg = mkrsrc(((wv & 1) ? wu : wg) + (size_t)(128 * (wv >> 1)) * FF, 128 * FF * 2);
      const int go = (32 * ((n >> 1) & 3) + 8 * kg) * (FF * 2) + 32 * jsl + 16 * (n & 1);
#pragma unroll
      for (int j = 0; j < 8; ++j) gur[j] = bld<AUX_W>(rg, n < 8 ? go : 0x7FFFFFF0, j * FF * 2);
      const rsrc_t rd = mkrsrc(wd + (size_t)(16 * jsl) * 512, 16384);
      const int dofs = (kg & 1) * 8192 + wv * 128 + n * 8;
#pragma unroll
      for (int j = 0; j < 8; ++j) dnr[j] = __builtin_amdgcn_raw_buffer_load_b64(rd, dofs, j * 1024, AUX_NT);
      LAS unsigned char* fb = L + L_FB + (((((wv & 1) * 2 + (n & 1)) * 8 * 4 + (wv >> 1)) * 4 + ((n >> 1) & 3)) * 4 + kg) * 16;
      if (n < 8) {
#define TG(E) *(LAS v4u*)(fb + E * 1024) = tcol8<E>(gur);
        FOR8(TG)
#undef TG
      }
    }
    wg_barrier();
    STAMP(12);
    v4u bg[4], bd[2];
#pragma unroll
    for (int t = 0; t < 4; ++t) { bg[t] = *(const LAS v4u*)(L + L_FB + ((((((n >> 1) & 1) * 16 + 2 * wv + (n & 1)) * 4 + t) * 4 + (n >> 2)) * 4 + kg) * 16); PIN(bg[t]); }
    const int rb = wv >> 1, hf = wv & 1;
    f32x4 shv = *(const LAS f32x4*)(L + L_SC + rb * 2048 + hf * 1024 + lane * 16), scv = *(const LAS f32x4*)(L + L_SC + 8192 + rb * 2048 + hf * 1024 + lane * 16);
    PIN(shv); PIN(scv);
    {
      const unsigned sel = (kg >> 1) ? 0x07060302u : 0x05040100u;
#pragma unroll
      for (int m2 = 0; m2 < 2; ++m2) {
#pragma unroll
        for (int jp = 0; jp < 4; ++jp) bd[m2][jp] = __builtin_amdgcn_perm(dnr[2 * jp + 1][m2], dnr[2 * jp][m2], sel);
        PIN(bd[m2]);
      }
    }
    int hbw = L_HB + rb * 1024 + (rb >> 1) * 32 + hf * 512 + lane * 8;
    int hbr = L_HB + (n >> 2) * 1024 + (n >> 3) * 32 + (32 * (n & 3) + 8 * kg) * 2;
    int acr = ((n & 1) == (kg >> 1)) ? L_ACT + (n >> 2) * 32 + (kg & 1) * 16 : L_ZERO;
    int h4o = WS_H4 + ((kg * 16 + 2 * wv + (n >> 3)) * 112 + jsl) * 128 + (n & 7) * 16;
    PIN(hbw); PIN(hbr); PIN(acr); PIN(h4o);
    SB();
    {
      const int off[2] = {WS_X1 + rb * 2048 + hf * 1024 + lane * 16, WS_X1 + rb * 2048 + (hf ^ 1) * 1024 + lane * 16};
      f32x4 v[2];
      sweep<2, 0>(rws, off, v, tag);
      STAMP(13);
      const float x0 = __uint_as_float(__builtin_amdgcn_readfirstlane(__float_as_uint(0.5f * (v[0][0] + v[1][0]))));
      const f32x4 d0 = v[0] - x0, d1 = v[1] - x0;
      const f32x4 sa = d0 + d1, sb2 = d0 * d0 + d1 * d1;
      float s1 = (sa[0] + sa[1]) + (sa[2] + sa[3]), s2 = (sb2[0] + sb2[1]) + (sb2[2] + sb2[3]);
      s1 = wave_sum(s1); s2 = wave_sum(s2);
      const float md = s1 * (1.f / 512.f), var = fmx(s2 * (1.f / 512.f) - md * md, 0.f);
      const float mb = rbf(x0 + md), rden = __builtin_amdgcn_rcpf(rbf(__builtin_amdgcn_sqrtf(rbf(rbf(var) + 1e-5f))));
      const f32x2 ha = rbf2(rbf2(rbf2(f32x2{v[0][0], v[0][1]} - mb) * rden) * f32x2{scv[0], scv[1]}) + f32x2{shv[0], shv[1]};
      const f32x2 hb = rbf2(rbf2(rbf2(f32x2{v[0][2], v[0][3]} - mb) * rden) * f32x2{scv[2], scv[3]}) + f32x2{shv[2], shv[3]};
      *(LAS v2u*)(L + hbw) = v2u{pack2(ha.x, ha.y), pack2(hb.x, hb.y)};
    }
    wg_barrier();
    {
      f32x4 c = {0.f, 0.f, 0.f, 0.f};
#pragma unroll
      for (int t = 0; t < 4; ++t) c = mfma32(*(const LAS v4u*)(L + hbr + t * 256), bg[t], c);
      float gs = sel4(lane & 4, lane & 8, c[0], c[1], c[2], c[3]);
      gs += dpp<ROR4>(gs); gs += dpp<ROR8>(gs);
      gs = rbf(gs);
      const float act = rbf(gelu_f(gs)) * dpp<XOR2>(gs);
      const unsigned pk = pack2(act, dpp<XOR1>(act));
      if (n == 0) *(LAS unsigned*)(L + L_ACT + kg * 32 + wv * 4) = pk;
    }
    wg_barrier();
    STAMP(14);
    {
      const v4u a = *(const LAS v4u*)(L + acr);
      const f32x4 c0 = mfma32(a, bd[0], f32x4{0.f, 0.f, 0.f, 0.f}), c1 = mfma32(a, bd[1], f32x4{0.f, 0.f, 0.f, 0.f});
      st16(rws, h4o, put4(f32x4{c0[0], c0[1], c1[0], c1[1]}, tag));
    }
    STAMP(15);
  }
  STAMP_OUT();
}

extern "C" void kernel_launch(void* const* d_in, const int* in_sizes, int n_in, void* d_out, int out_size, void* d_ws, size_t ws_size, hipStream_t stream) {
  adaln_block_kernel<<<256, 512, 0, stream>>>((const bf16_t*)d_in[0], (const bf16_t*)d_in[1], (const bf16_t*)d_in[2], (const bf16_t*)d_in[3],
                                              (const bf16_t*)d_in[4], (const bf16_t*)d_in[6], (const bf16_t*)d_in[7], (const bf16_t*)d_in[8],
                                              (const bf16_t*)d_in[9], (const bf16_t*)d_in[10], (bf16_t*)d_out, (unsigned char*)d_ws);
}
```

```cpp
#define AUX_V 0
#include <hip/hip_runtime.h>
#include <hip/hip_bf16.h>
#include <stdint.h>

#define GAS __attribute__((address_space(1)))
#define LAS __attribute__((address_space(3)))
typedef unsigned short bf16_t;
typedef unsigned v4u __attribute__((ext_vector_type(4)));
typedef unsigned v2u __attribute__((ext_vector_type(2)));
typedef float f32x4 __attribute__((ext_vector_type(4)));
typedef float f32x2 __attribute__((ext_vector_type(2)));
typedef __bf16 bf16x8 __attribute__((ext_vector_type(8)));
typedef __bf16 bf16x2 __attribute__((ext_vector_type(2)));
typedef short s16x4 __attribute__((ext_vector_type(4)));
typedef __amdgpu_buffer_rsrc_t rsrc_t;

#define CODE_KB 19
#define MAPX 1
#define MAPQ 0
#define ROTM 1
#define NOTR 0
#define AUX_XC 0
#define NOXC 0
#define NOST 0
#define PRIO 2
#define KPOL "nt"
#define ABL 0
#define NOWO 0
#define PACE 4
#define PACEV 8
#define NLATE 12
#define AUX_W 2
#define T_MOD 280
#define T_Q 600
#define GU_A 2
#define GU_B 1
#define DLY_H2 0
#define DLY_H4 0

namespace {
constexpr int SK = 16384, WIN = 4096, FF = 1792;
constexpr int AUX_SC1 = 16, AUX_NT = 2;
constexpr int WS_EPOCH = 0;
constexpr int WS_X1 = 4096;
constexpr int WS_SB = WS_X1 + 8192;
constexpr int WS_CB = WS_SB + 8192;
constexpr int WS_ST = WS_CB + 8192;
constexpr int WS_MOD = WS_ST + 32768;
constexpr int WS_QP = WS_MOD + 196608;
constexpr int WS_H1 = WS_QP + 524288;
constexpr int WS_H2 = WS_H1 + 524288;
constexpr int WS_H4 = WS_H2 + 1048576;
constexpr int WS_END = WS_H4 + 917504;
constexpr int WS_STAMP = 8 << 20;
constexpr int L_K = 0;
constexpr int L_FB = 0;
constexpr int L_SC = 32768;
constexpr int L_P = 131072;
constexpr int L_QB = L_P + 4096;
constexpr int L_HQ = L_QB + 1280;
constexpr int L_ZERO = L_HQ + 1024;
constexpr int L_FLAG = L_ZERO + 1024;
constexpr int L_GG = L_FLAG + 256;
constexpr int L_OA = L_GG + 384;
constexpr int L_F = L_OA + 256;
constexpr int L_ACT = L_F + 128;
constexpr int L_RED = L_ACT + 128;
constexpr int L_MRG = L_RED + 2048;
constexpr int L_HB = L_MRG + 8192;
constexpr int L_END = L_HB + 4096 + 64;
enum { F_MCNT = 0, F_HCNT = 1, F_QCNT = 2, F_K = 4, F_PCNT = 8, F_G = 10, F_VI = 16 };
constexpr unsigned SPIN_G = 1u << 16, SPIN_L = 1u << 20;

__device__ __forceinline__ float bflo(unsigned u) { return __uint_as_float(u << 16); }
__device__ __forceinline__ float bfhi(unsigned u) { return __uint_as_float(u & 0xFFFF0000u); }
__device__ __forceinline__ unsigned pack2(float lo, float hi) {
  f32x2 v = {lo, hi};
  return __builtin_bit_cast(unsigned, __builtin_convertvector(v, bf16x2));
}
__device__ __forceinline__ float rbf(float x) { return bflo(pack2(x, x) & 0xFFFFu); }
__device__ __forceinline__ float fmx(float a, float b) { return __builtin_amdgcn_fmed3f(a, b, __builtin_inff()); }
template <int CTRL> __device__ __forceinline__ float dpp(float x) {
  return __builtin_bit_cast(float, __builtin_amdgcn_mov_dpp(__builtin_bit_cast(int, x), CTRL, 0xf, 0xf, true));
}
constexpr int XOR1 = 0xB1, XOR2 = 0x4E, HMIR = 0x141, ROR2 = 0x122, ROR4 = 0x124, ROR8 = 0x128;
__device__ __forceinline__ float xrow_sum(float x) {
  auto s = __builtin_amdgcn_permlane16_swap(__float_as_uint(x), __float_as_uint(x), false, false);
  x = __uint_as_float(s[0]) + __uint_as_float(s[1]);
  auto t = __builtin_amdgcn_permlane32_swap(__float_as_uint(x), __float_as_uint(x), false, false);
  return __uint_as_float(t[0]) + __uint_as_float(t[1]);
}
__device__ __forceinline__ float xrow_max(float x) {
  auto s = __builtin_amdgcn_permlane16_swap(__float_as_uint(x), __float_as_uint(x), false, false);
  x = fmx(__uint_as_float(s[0]), __uint_as_float(s[1]));
  auto t = __builtin_amdgcn_permlane32_swap(__float_as_uint(x), __float_as_uint(x), false, false);
  return fmx(__uint_as_float(t[0]), __uint_as_float(t[1]));
}
__device__ __forceinline__ float wave_sum(float x) {
  x += dpp<XOR1>(x); x += dpp<XOR2>(x); x += dpp<ROR4>(x); x += dpp<ROR8>(x);
  return xrow_sum(x);
}
__device__ __forceinline__ float wave_max(float x) {
  x = fmx(x, dpp<XOR1>(x)); x = fmx(x, dpp<XOR2>(x)); x = fmx(x, dpp<ROR4>(x)); x = fmx(x, dpp<ROR8>(x));
  return xrow_max(x);
}
__device__ __forceinline__ f32x4 mfma32(v4u a, v4u b, f32x4 c) {
  return __builtin_amdgcn_mfma_f32_16x16x32_bf16(__builtin_bit_cast(bf16x8, a), __builtin_bit_cast(bf16x8, b), c, 0, 0, 0);
}
__device__ __forceinline__ f32x4 mfma16(v2u a, v2u b, f32x4 c) {
  return __builtin_amdgcn_mfma_f32_16x16x16bf16_1k(__builtin_bit_cast(s16x4, a), __builtin_bit_cast(s16x4, b), c, 0, 0, 0);
}
template <int E> __device__ __forceinline__ v4u tcol8(const v4u* r) {
  constexpr unsigned sel = (E & 1) ? 0x07060302u : 0x05040100u;
  v4u o;
  o.x = __builtin_amdgcn_perm(r[1][E >> 1], r[0][E >> 1], sel);
  o.y = __builtin_amdgcn_perm(r[3][E >> 1], r[2][E >> 1], sel);
  o.z = __builtin_amdgcn_perm(r[5][E >> 1], r[4][E >> 1], sel);
  o.w = __builtin_amdgcn_perm(r[7][E >> 1], r[6][E >> 1], sel);
  return o;
}
__device__ __forceinline__ v4u tcol8r(const v4u* r, int e) {
  const unsigned sel = (e & 1) ? 0x07060302u : 0x05040100u;
  v4u o;
  o.x = __builtin_amdgcn_perm(r[1][e >> 1], r[0][e >> 1], sel);
  o.y = __builtin_amdgcn_perm(r[3][e >> 1], r[2][e >> 1], sel);
  o.z = __builtin_amdgcn_perm(r[5][e >> 1], r[4][e >> 1], sel);
  o.w = __builtin_amdgcn_perm(r[7][e >> 1], r[6][e >> 1], sel);
  return o;
}
template <int E> __device__ __forceinline__ v2u tcol4(const v4u* r) {
  constexpr unsigned sel = (E & 1) ? 0x07060302u : 0x05040100u;
  v2u o;
  o.x = __builtin_amdgcn_perm(r[1][E >> 1], r[0][E >> 1], sel);
  o.y = __builtin_amdgcn_perm(r[3][E >> 1], r[2][E >> 1], sel);
  return o;
}
#define FOR8(X) X(0) X(1) X(2) X(3) X(4) X(5) X(6) X(7)
__device__ __forceinline__ rsrc_t mkrsrc(const void* p, int bytes) { return __builtin_amdgcn_make_buffer_rsrc((void*)p, 0, bytes, 0x00020000); }
template <int AUX> __device__ __forceinline__ v4u bld(rsrc_t r, int voff, int soff) { return __builtin_amdgcn_raw_buffer_load_b128(r, voff, soff, AUX); }
__device__ __forceinline__ v4u put4(f32x4 v, unsigned tag) {
  v4u o;
  o.x = __builtin_amdgcn_perm(__float_as_uint(v.x), tag, 0x07060500u);
  o.y = __builtin_amdgcn_perm(__float_as_uint(v.y), tag, 0x07060501u);
  o.z = __builtin_amdgcn_perm(__float_as_uint(v.z), tag, 0x07060502u);
  o.w = __builtin_amdgcn_perm(__float_as_uint(v.w), tag, 0x07060503u);
  return o;
}
__device__ __forceinline__ unsigned tagof(v4u v) { return __builtin_amdgcn_perm(v.y, v.x, 0x0c0c0400u) | __builtin_amdgcn_perm(v.w, v.z, 0x04000c0cu); }
__device__ __forceinline__ f32x4 get4(v4u v) { return __builtin_bit_cast(f32x4, v & 0xFFFFFF00u); }
__device__ __forceinline__ void st16(rsrc_t r, int off, v4u v) { __builtin_amdgcn_raw_buffer_store_b128(v, r, off, 0, AUX_SC1); }
#define CBAR() asm volatile("" ::: "memory")
#define LGKM0() asm volatile("s_waitcnt lgkmcnt(0)" ::: "memory")
#define SB() __builtin_amdgcn_sched_barrier(0)
#define PIN(x) asm volatile("" : "+v"(x))
__device__ __forceinline__ void lds_wait(volatile LAS unsigned* f, unsigned want = 1u) {
#pragma unroll 1
  for (unsigned i = 0; i < SPIN_L; ++i) { if (*f >= want) break; __builtin_amdgcn_s_sleep(1); }
  CBAR();
}
__device__ __forceinline__ void lds_set(volatile LAS unsigned* f) { LGKM0(); *f = 1u; }
__device__ __forceinline__ void lds_arrive(LAS unsigned* f, int lane) {
  LGKM0();
  if (lane == 0) __hip_atomic_fetch_add(f, 1u, __ATOMIC_RELAXED, __HIP_MEMORY_SCOPE_WORKGROUP);
}
__device__ __forceinline__ void wg_barrier() { LGKM0(); __builtin_amdgcn_s_barrier(); CBAR(); }
template <int N, int SLEEP> __device__ __forceinline__ void sweep(rsrc_t rs, const int (&off)[N], f32x4 (&o)[N], unsigned tag) {
  v4u v[N];
#pragma unroll 1
  for (unsigned spin = 0;; ++spin) {
    unsigned bad = 0;
#pragma unroll
    for (int i = 0; i < N; ++i) v[i] = bld<AUX_SC1>(rs, off[i], 0);
#pragma unroll
    for (int i = 0; i < N; ++i) bad |= tagof(v[i]) ^ tag;
    if (__all(bad == 0u) || spin >= SPIN_G) break;
    if (SLEEP) __builtin_amdgcn_s_sleep(SLEEP);
    CBAR();
  }
#pragma unroll
  for (int i = 0; i < N; ++i) o[i] = get4(v[i]);
}
__device__ __forceinline__ float fexp(float x) { return __builtin_amdgcn_exp2f(x * 1.4426950408889634f); }
__device__ __forceinline__ float silu_f(float x) { return x * __builtin_amdgcn_rcpf(1.f + fexp(-x)); }
__device__ __forceinline__ float gelu_f(float x) {
  const float u = 0.7978845608028654f * (x + 0.044715f * x * x * x);
  return x * __builtin_amdgcn_rcpf(1.f + fexp(-2.f * u));
}
__device__ __forceinline__ void tcol4x8(const v2u* r, v4u* o) {
#pragma unroll
  for (int e = 0; e < 4; ++e) {
    const unsigned sel = (e & 1) ? 0x07060302u : 0x05040100u;
#pragma unroll
    for (int jp = 0; jp < 4; ++jp) o[e][jp] = __builtin_amdgcn_perm(r[2 * jp + 1][e >> 1], r[2 * jp][e >> 1], sel);
    PIN(o[e]);
  }
}
__device__ __forceinline__ f32x4 gemv32x64(v4u a, const v4u* bt) {
  f32x4 c[4];
#pragma unroll
  for (int e = 0; e < 4; ++e) c[e] = mfma32(a, bt[e], f32x4{0.f, 0.f, 0.f, 0.f});
  return f32x4{c[0][0], c[1][0], c[2][0], c[3][0]};
}
__device__ __forceinline__ void wait_until(unsigned long long t0, unsigned ticks) {
#pragma unroll 1
  for (int i = 0; i < 4096 && __builtin_amdgcn_s_memrealtime() - t0 < ticks; ++i) __builtin_amdgcn_s_sleep(1);
}
__device__ __forceinline__ float sel4(bool b0, bool b1, float c0, float c1, float c2, float c3) {
  const float lo = b0 ? c1 : c0, hi = b0 ? c3 : c2;
  return b1 ? hi : lo;
}
__device__ __forceinline__ f32x2 rbf2(f32x2 v) {
  const unsigned p = pack2(v.x, v.y);
  return f32x2{bflo(p), bfhi(p)};
}
template <bool MAX> __device__ __forceinline__ float red2(float a, float b) { return MAX ? fmx(a, b) : a + b; }
template <bool MAX> __device__ __forceinline__ f32x4 wave_red4(f32x4 s, int lane) {
  const bool o1 = lane & 1, o2 = lane & 2;
  const float a = red2<MAX>(o1 ? s[1] : s[0], dpp<XOR1>(o1 ? s[0] : s[1])), c = red2<MAX>(o1 ? s[3] : s[2], dpp<XOR1>(o1 ? s[2] : s[3]));
  float m = red2<MAX>(o2 ? c : a, dpp<XOR2>(o2 ? a : c));
  m = red2<MAX>(m, dpp<ROR4>(m)); m = red2<MAX>(m, dpp<ROR8>(m));
  m = MAX ? xrow_max(m) : xrow_sum(m);
  return f32x4{dpp<0x00>(m), dpp<0x55>(m), dpp<0xAA>(m), dpp<0xFF>(m)};
}
#define STAMP(i) do {} while (0)
#define STAMP_OUT() do {} while (0)
}

__global__ void __launch_bounds__(512, 2)
adaln_block_kernel(const bf16_t* __restrict__ x, const bf16_t* __restrict__ kc, const bf16_t* __restrict__ vc,
                   const bf16_t* __restrict__ wq, const bf16_t* __restrict__ wo, const bf16_t* __restrict__ wg,
                   const bf16_t* __restrict__ wu, const bf16_t* __restrict__ wd, const bf16_t* __restrict__ cc,
                   const bf16_t* __restrict__ wmod, bf16_t* __restrict__ out, unsigned char* ws) {
  __shared__ __attribute__((aligned(16))) unsigned char lds_raw[L_END];
  LAS unsigned char* L = (LAS unsigned char*)lds_raw;
  volatile LAS unsigned* FL = (volatile LAS unsigned*)(L + L_FLAG);
  LAS unsigned* FC = (LAS unsigned*)(L + L_FLAG);
  const int tid = threadIdx.x, lane = tid & 63, wv = __builtin_amdgcn_readfirstlane(tid >> 6);
  const int g = blockIdx.x, grp = MAPX ? (g & 7) * 4 + (g >> 6) : g >> 3, b = grp >> 3, kvh = grp & 7, s = MAPX ? (g >> 3) & 7 : g & 7;
  const int n = lane & 15, kg = lane >> 4;
  const rsrc_t rws = mkrsrc(ws, WS_END);
  STAMP(0);
  v4u cpf = {0u, 0u, 0u, 0u};
  if (CODE_KB && wv == 3) {
    const unsigned long long pc = __builtin_amdgcn_s_getpc() & ~255ull;
    const rsrc_t rc = mkrsrc((const void*)pc, CODE_KB * 1024);
    cpf = bld<0>(rc, (g >> 3) * 1024 + lane * 16, 0);
  }
  if (tid < 320) *(LAS unsigned*)(L + L_ZERO + tid * 4) = 0u;
  wg_barrier();
  const unsigned epoch = __builtin_amdgcn_raw_buffer_load_b32(rws, WS_EPOCH, 0, AUX_SC1);
  const int ru = ((g >> 5) << 3) + (g & 7);
  const int jsl = 4 * ru + ((g >> 3) & 3);
  const bool roleG = (g < 128) && (ru < 28), roleR = g >= 192;
  unsigned tag;
  v4u gur[8], stv[2];
  v2u wor[8];
  const int wj = g - 128;
  const rsrc_t rwo = mkrsrc(wo + (size_t)(wj & 127) * 32 * 512, (g >= 128 && !NOWO) ? 32768 : 0);
  const int sto = WS_ST + ((wv & 3) * 64 + ((wj >> 4) & 7) * 8 + (lane & 7)) * 128;
#define WO_LD() _Pragma("unroll") for (int j = 0; j < 8; ++j) wor[j] = __builtin_amdgcn_raw_buffer_load_b64(rwo, 8 * kg * 1024 + wv * 128 + n * 8, j * 1024, AUX_NT)
  const bf16_t* gup = ((n & 2) ? wu : wg) + (size_t)(128 * (wv & 3) + 32 * (n >> 2) + 8 * kg) * FF + 16 * jsl + 8 * (n & 1);
#define GU_LD(j) gur[j] = AUX_W ? __builtin_nontemporal_load((const v4u*)(gup + (j) * FF)) : *(const v4u*)(gup + (j) * FF)

  if (__builtin_expect(wv >= 4, 1)) {
    const int w = wv - 4;
    const size_t kvbase = ((size_t)(b * 8 + kvh) * SK + (SK - WIN) + s * 512) * 128;
    const int rot = ROTM == 0 ? (((grp * 8 + s) >> 3) * 4 + (s & 3)) & 127 : ROTM == 1 ? (g * 37) & 127 : ((g >> 3) * 4 + (g & 3)) & 127;
    const unsigned char* kbase = (const unsigned char*)(kc + kvbase);
    const unsigned ldsk = (unsigned)(uintptr_t)(L + L_K);
    const int klane = kg * 256 + ((n ^ kg) << 4);
    STAMP(7);
#pragma unroll 4
    for (int i = 0; i < 32; ++i) {
      const int phys = (4 * i + w + rot) & 127;
      const unsigned char* sb_ = kbase + phys * 1024;
      const int vo = klane ^ ((i & 3) << 6);
      const unsigned dst = ldsk + (4 * i + w) * 1024;
      unsigned keep;
      asm volatile("s_mov_b32 %0, m0\n\ts_mov_b32 m0, %3\n\ts_nop 0\n\tglobal_load_lds_dwordx4 %1, %2 " KPOL "\n\ts_mov_b32 m0, %0\n\ts_waitcnt vmcnt(%4)"
                   : "=&s"(keep) : "v"(vo), "s"(sb_), "s"(dst), "i"(PACE) : "memory");
    }
    STAMP(1);
    const rsrc_t rv = mkrsrc(vc + kvbase, 131072);
    v4u V[32], VT[24];
    constexpr int TR0 = 8 + (PACEV > PACE && NLATE > 0 ? PACEV : PACE);
#pragma unroll
    for (int i = 0; i < 32; ++i) {
      V[i] = bld<AUX_V>(rv, lane * 16, ((4 * i + w + rot) & 127) * 1024);
      if (i < 31) asm volatile("s_waitcnt vmcnt(%0)" :: "i"(i < 32 - NLATE ? PACE : PACEV) : "memory");
      if (i == PACE - 1) { FL[F_K + w] = 1u; STAMP(2); }
      if (!NOTR && i >= TR0) { const int k = i - TR0; VT[k] = tcol8r(V + 8 * (k >> 3), k & 7); PIN(VT[k]); }
      SB();
    }
    FL[F_VI + w] = 1u;
    WO_LD();
    SB();
    tag = __builtin_amdgcn_readfirstlane(epoch) + 0x01010101u;
    STAMP(3);
#pragma unroll
    for (int k = NOTR ? 0 : 32 - TR0; k < 24; ++k) { VT[k] = tcol8r(V + 8 * (k >> 3), k & 7); PIN(VT[k]); }
    lds_wait(FL + F_PCNT, 4u);
    STAMP(4);
    f32x4 f;
    {
      const LAS f32x4* sp = (const LAS f32x4*)(L + L_F);
      const f32x4 m0 = sp[0], m1 = sp[2], m2 = sp[4], m3 = sp[6], mo = sp[2 * w];
#pragma unroll
      for (int h = 0; h < 4; ++h) f[h] = fexp(mo[h] - fmx(fmx(m0[h], m1[h]), fmx(m2[h], m3[h])));
    }
    f32x4 acc[2] = {f32x4{0.f, 0.f, 0.f, 0.f}, f32x4{0.f, 0.f, 0.f, 0.f}};
    int paddr[4];
#pragma unroll
    for (int ep = 0; ep < 4; ++ep) paddr[ep] = (n >> 2) == ep ? L_P + w * 1024 + (((n & 3) * 4 + kg) << 4) : L_ZERO;
#pragma unroll
    for (int G = 0; G < 4; ++G) {
      v4u pa[4];
#pragma unroll
      for (int ep = 0; ep < 4; ++ep) pa[ep] = *(const LAS v4u*)(L + paddr[ep] + G * 256);
      if (G == 3) { PIN(pa[0]); PIN(pa[1]); PIN(pa[2]); PIN(pa[3]); PIN(f); SB(); STAMP(5); }
#define PV(E) acc[E & 1] = mfma32(pa[E >> 1], G < 3 ? VT[8 * G + E] : tcol8<E>(V + 24), acc[E & 1]);
      FOR8(PV)
#undef PV
      SB();
    }
    STAMP(6);
#pragma unroll
    for (int h = 0; h < 4; ++h) *(LAS f32x2*)(L + L_MRG + (w * 512 + h * 128 + 8 * n + 2 * kg) * 4) = f32x2{acc[0][h], acc[1][h]} * f[h];
  } else {
    const int cw = wv;
    if (ABL == 6) { lds_arrive(FC + F_PCNT, lane); tag = 0; goto abl_end2; }
    {
    __builtin_amdgcn_s_setprio(PRIO);
    const unsigned long long tstart = __builtin_amdgcn_s_memrealtime();
    const int ks = MAPQ ? g & 7 : g >> 5, ns = MAPQ ? g >> 3 : g & 31, u5 = lane & 31, h5 = lane >> 5;
    const int mkq = g < 64 ? g >> 4 : (g - 64) >> 5, mcb = g < 64 ? g & 15 : 16 + ((g - 64) & 31);
    v2u wm[8], wr[8]; v4u cf;
    const rsrc_t rm = mkrsrc(wmod + (size_t)(mkq * 128 + cw * 32) * 3072, (g < 192 && ABL != 1) ? 32 * 6144 : 0);
    const rsrc_t rq = mkrsrc(wq + (size_t)(ks * 64 + (cw & 1) * 32) * 4096, ABL != 1 ? 32 * 8192 : 0);
    const rsrc_t rcx = mkrsrc(cc, NOXC ? 0 : 4096), rxx = mkrsrc(x, NOXC ? 0 : 4096);
    cf = bld<AUX_XC>(rcx, ((n >> 2) * 512 + ((128 * mkq) & 511) + 32 * cw + 8 * kg) * 2, 0);
#pragma unroll
    for (int j = 0; j < 8; ++j) wm[j] = __builtin_amdgcn_raw_buffer_load_b64(rm, 8 * kg * 6144 + mcb * 128 + n * 8, j * 6144, AUX_NT);
    const v4u xs = bld<AUX_XC>(rxx, (cw * 512 + lane * 8) * 2, 0);
    const v2u xo = __builtin_amdgcn_raw_buffer_load_b64(rxx, (cw * 512 + 64 * ks + 4 * n) * 2, 0, AUX_XC);
#pragma unroll
    for (int j = 0; j < 8; ++j) wr[j] = __builtin_amdgcn_raw_buffer_load_b64(rq, 8 * kg * 8192 + ns * 256 + (cw >> 1) * 128 + n * 8, j * 8192, AUX_NT);
    SB();
    STAMP(1);
    tag = __builtin_amdgcn_readfirstlane(epoch) + 0x01010101u;
    if (g < 192) {
      v4u a, bt[4];
#pragma unroll
      for (int i = 0; i < 4; ++i) a[i] = pack2(silu_f(bflo(cf[i])), silu_f(bfhi(cf[i])));
      PIN(a);
      tcol4x8(wm, bt);
      *(LAS f32x4*)(L + L_MRG + cw * 1024 + lane * 16) = gemv32x64(a, bt);
      lds_arrive(FC + F_MCNT, lane);
      if (cw == 0) {
        lds_wait(FL + F_MCNT, 4u);
        const LAS f32x4* mp = (const LAS f32x4*)(L + L_MRG + lane * 16);
        st16(rws, NOST ? 0x7FFFFFF0 : WS_MOD + ((mkq * 4 + kg) * 3072 + 64 * mcb + 4 * n) * 4, put4((mp[0] + mp[64]) + (mp[128] + mp[192]), tag));
        STAMP(11);
      }
    }
    float ln[4];
    {
      float sum = 0.f;
#pragma unroll
      for (int i = 0; i < 4; ++i) sum += bflo(xs[i]) + bfhi(xs[i]);
      const float mean = wave_sum(sum) * (1.f / 512.f);
      float sq = 0.f;
#pragma unroll
      for (int i = 0; i < 4; ++i) { const float d0 = bflo(xs[i]) - mean, d1 = bfhi(xs[i]) - mean; sq = fmaf(d0, d0, sq); sq = fmaf(d1, d1, sq); }
      sq = wave_sum(sq);
      const float mb = rbf(mean), rden = __builtin_amdgcn_rcpf(rbf(__builtin_amdgcn_sqrtf(rbf(rbf(sq * (1.f / 512.f)) + 1e-5f))));
#pragma unroll
      for (int i = 0; i < 2; ++i) { ln[2 * i] = rbf(rbf(bflo(xo[i]) - mb) * rden); ln[2 * i + 1] = rbf(rbf(bfhi(xo[i]) - mb) * rden); }
    }
    v4u bq[4];
    tcol4x8(wr, bq);
    asm volatile("" :: "v"(cpf));
    STAMP(2);
    if (ABL == 1 || ABL == 2) { asm volatile("" :: "v"(bq[0]), "v"(bq[1]), "v"(bq[2]), "v"(bq[3]), "v"(ln[0])); lds_arrive(FC + F_PCNT, lane); goto abl_end; }
    wait_until(tstart, T_MOD);
    {
      const int mo = WS_MOD + ((h5 * 4 + cw) * 3072 + (kg & 1) * 512 + 64 * ks + 4 * n) * 4;
      const int off[2] = {mo, mo + 2 * 4 * 3072 * 4};
      f32x4 v[2];
      sweep<2, 8>(rws, off, v, tag);
      STAMP(3);
      const f32x4 t = v[0] + v[1];
      float h[4];
#pragma unroll
      for (int e = 0; e < 4; ++e) {
        auto p = __builtin_amdgcn_permlane32_swap(__float_as_uint(t[e]), __float_as_uint(t[e]), false, false);
        const float u = rbf(__uint_as_float(p[0]) + __uint_as_float(p[1]));
        auto r = __builtin_amdgcn_permlane16_swap(__float_as_uint(u), __float_as_uint(u), false, false);
        h[e] = rbf(ln[e] * rbf(1.f + __uint_as_float(r[1]))) + __uint_as_float(r[0]);
      }
      if (lane < 16) *(LAS v2u*)(L + L_HQ + cw * 128 + n * 8) = v2u{pack2(h[0], h[1]), pack2(h[2], h[3])};
      lds_arrive(FC + F_HCNT, lane);
      lds_wait(FL + F_HCNT, 4u);
      const v4u a = *(const LAS v4u*)(L + L_HQ + (n >> 2) * 128 + (cw & 1) * 64 + kg * 16);
      const f32x4 qp = gemv32x64(a, bq);
      if (cw & 1) {
        *(LAS f32x4*)(L + L_MRG + 4096 + (cw >> 1) * 1024 + lane * 16) = qp;
        lds_set(FL + F_G + (cw >> 1));
      } else {
        lds_wait(FL + F_G + (cw >> 1));
        st16(rws, WS_QP + ((kg * 8 + ks) * 4096 + 128 * ns + 64 * (cw >> 1) + 4 * n) * 4, put4(qp + *(const LAS f32x4*)(L + L_MRG + 4096 + (cw >> 1) * 1024 + lane * 16), tag));
      }
    }
    STAMP(4);
    if (cw == 3 && roleR) {
      const int rr = g - 192;
      const int mo = WS_MOD + ((((lane >> 3) & 3) * 4 + (rr >> 4)) * 3072 + 1024 + h5 * 512 + 32 * (rr & 15) + 4 * (lane & 7)) * 4;
      const int off[2] = {mo, mo + 4096};
      f32x4 v[2];
      const unsigned short xin = x[rr * 32 + u5];
      sweep<2, 8>(rws, off, v, tag);
#pragma unroll
      for (int i = 0; i < 2; ++i)
#pragma unroll
        for (int e = 0; e < 4; ++e) {
          float u = v[i][e]; u += dpp<ROR8>(u);
          auto p = __builtin_amdgcn_permlane16_swap(__float_as_uint(u), __float_as_uint(u), false, false);
          v[i][e] = rbf(__uint_as_float(p[0]) + __uint_as_float(p[1]));
        }
      *(LAS float*)(L + L_GG + 256 + u5 * 4) = bflo(xin);
      if (u5 < 8) {
        if (lane < 32) {
          *(LAS f32x4*)(L + L_GG + lane * 16) = v[0];
          st16(rws, WS_CB + rr * 128 + lane * 16, put4(f32x4{rbf(1.f + v[1][0]), rbf(1.f + v[1][1]), rbf(1.f + v[1][2]), rbf(1.f + v[1][3])}, tag));
        } else {
          *(LAS f32x4*)(L + L_GG + 128 + u5 * 16) = v[1];
          st16(rws, WS_SB + rr * 128 + u5 * 16, put4(v[0], tag));
        }
      }
    }
    if (ABL == 3) { lds_arrive(FC + F_PCNT, lane); goto abl_end; }
    {
    const int qo = WS_QP + ((b * 8 + h5) * 4096 + kvh * 512 + cw * 128 + 4 * u5) * 4;
    wait_until(tstart, T_Q);
    v4u qv[4];
#pragma unroll
    for (int i = 0; i < 4; ++i) qv[i] = bld<AUX_SC1>(rws, qo, i * 32768);
    SB();
    lds_wait(FL + F_K + cw);
    STAMP(5);
    v4u kf[32];
#pragma unroll
    for (int t = 0; t < 8; ++t)
#pragma unroll
      for (int ds = 0; ds < 4; ++ds)
        kf[4 * t + ds] = *(const LAS v4u*)(L + L_K + (16 * (4 * t + (n >> 2)) + 4 * cw + (n & 3)) * 256 + (((4 * ds + kg) ^ n) << 4));
    SB();
    {
#pragma unroll 1
      for (unsigned spin = 0; spin < SPIN_G; ++spin) {
        if (__all(((tagof(qv[0]) ^ tag) | (tagof(qv[1]) ^ tag) | (tagof(qv[2]) ^ tag) | (tagof(qv[3]) ^ tag)) == 0u)) break;
        __builtin_amdgcn_s_sleep(4);
#pragma unroll
        for (int i = 0; i < 4; ++i) qv[i] = bld<AUX_SC1>(rws, qo, i * 32768);
      }
      const f32x4 t = (get4(qv[0]) + get4(qv[1])) + (get4(qv[2]) + get4(qv[3]));
      float q[4];
#pragma unroll
      for (int e = 0; e < 4; ++e) {
        auto p = __builtin_amdgcn_permlane32_swap(__float_as_uint(t[e]), __float_as_uint(t[e]), false, false);
        q[e] = __uint_as_float(p[0]) + __uint_as_float(p[1]);
      }
      if (lane < 32) *(LAS v2u*)(L + L_QB + cw * 320 + u5 * 8) = v2u{pack2(q[0], q[1]), pack2(q[2], q[3])};
      lds_arrive(FC + F_QCNT, lane);
      lds_wait(FL + F_QCNT, 4u);
    }
    STAMP(6);
    {
      f32x4 sc[2];
      v4u qf[4];
#pragma unroll
      for (int ds = 0; ds < 4; ++ds) qf[ds] = *(const LAS v4u*)(L + L_QB + (n & 3) * 320 + kg * 16 + ds * 64);
      f32x4 c[8];
#pragma unroll
      for (int t = 0; t < 8; ++t) {
        c[t] = f32x4{0.f, 0.f, 0.f, 0.f};
#pragma unroll
        for (int ds = 0; ds < 4; ++ds) c[t] = mfma32(qf[ds], kf[4 * t + ds], c[t]);
      }
#pragma unroll
      for (int sg = 0; sg < 2; ++sg)
#pragma unroll
        for (int h = 0; h < 4; ++h) sc[sg][h] = sel4(lane & 16, lane & 32, c[4 * sg][h], c[4 * sg + 1][h], c[4 * sg + 2][h], c[4 * sg + 3][h]);
      f32x4 e0, e1, mx;
#pragma unroll
      for (int h = 0; h < 4; ++h) {
        const f32x2 r = rbf2(rbf2(f32x2{sc[0][h], sc[1][h]}) * 0.08838834764831845f);
        e0[h] = r.x; e1[h] = r.y; mx[h] = fmx(r.x, r.y);
      }
      const f32x4 mw = wave_red4<true>(mx, lane);
#pragma unroll
      for (int h = 0; h < 4; ++h) {
        e0[h] = fexp(e0[h] - mw[h]); e1[h] = fexp(e1[h] - mw[h]);
        const unsigned pp = pack2(e0[h], e1[h]);
        LAS unsigned short* pd = (LAS unsigned short*)(L + L_P) + ((((cw * 4 + (kg >> 1)) * 4 + h) * 4 + (n & 3)) * 8 + 4 * (kg & 1) + (n >> 2));
        pd[0] = (unsigned short)(pp & 0xFFFFu);
        pd[2 * 128] = (unsigned short)(pp >> 16);
      }
      const f32x4 lw = wave_red4<false>(e0 + e1, lane);
      if (lane < 2) *(LAS f32x4*)(L + L_F + cw * 32 + lane * 16) = lane == 0 ? mw : lw;
      lds_arrive(FC + F_PCNT, lane);
    }
    STAMP(7);
    lds_wait(FL + F_PCNT, 4u);
    if (cw == 3) {
      const LAS f32x4* sp = (const LAS f32x4*)(L + L_F);
      f32x4 M, Ls;
#pragma unroll
      for (int h = 0; h < 4; ++h) {
        M[h] = fmx(fmx(sp[0][h], sp[2][h]), fmx(sp[4][h], sp[6][h]));
        Ls[h] = (sp[1][h] * fexp(sp[0][h] - M[h]) + sp[3][h] * fexp(sp[2][h] - M[h])) + (sp[5][h] * fexp(sp[4][h] - M[h]) + sp[7][h] * fexp(sp[6][h] - M[h]));
      }
      if (lane < 2) st16(rws, WS_ST + (grp * 8 + s) * 128 + lane * 16, put4(lane == 0 ? M : Ls, tag));
    }
    if (roleG) {
      const int mo = WS_SB + cw * 2048 + lane * 16;
      const int off[4] = {mo, mo + 1024, mo + 8192, mo + 8192 + 1024};
      f32x4 pre[4];
      sweep<4, 8>(rws, off, pre, tag);
#pragma unroll
      for (int i = 0; i < 4; ++i) *(LAS f32x4*)(L + L_SC + (i >> 1) * 8192 + cw * 2048 + (i & 1) * 1024 + lane * 16) = pre[i];
#pragma unroll
      for (int j = 0; j < GU_A; ++j) { GU_LD(j); if (j + 1 < GU_A) asm volatile("s_waitcnt vmcnt(0)" ::: "memory"); SB(); }
      STAMP(8);
      if (GU_B) {
        lds_wait(FL + F_VI + cw);
#pragma unroll
        for (int j = GU_A; j < GU_A + GU_B; ++j) GU_LD(j);
        SB();
      }
    }
    if (g >= 128) {
      lds_wait(FL + F_VI + cw);
      WO_LD();
      stv[0] = bld<AUX_SC1>(rws, sto, 0); stv[1] = bld<AUX_SC1>(rws, sto + 16, 0);
      SB();
    }
    }
abl_end:;
    }
abl_end2:;
  }
  int h1o = WS_H1 + ((kvh * 16 + (tid >> 3)) * 32 + b * 8 + s) * 128 + (tid & 7) * 16;
  PIN(h1o);
  STAMP(9);
  wg_barrier();
  if (wv < 2) {
    const LAS f32x4* mp = (const LAS f32x4*)(L + L_MRG + tid * 16);
    st16(rws, h1o, put4((mp[0] + mp[128]) + (mp[256] + mp[384]), tag));
  }
  SB();
  __builtin_amdgcn_s_barrier();
  STAMP(10);
  if (ABL) { STAMP_OUT(); return; }
  if (g >= 128) {
    float cs = 0.f;
    if (wv < 4) {
#pragma unroll 1
      for (unsigned spin = 0; spin < SPIN_G; ++spin) {
        if (__all(((tagof(stv[0]) ^ tag) | (tagof(stv[1]) ^ tag)) == 0u)) break;
        CBAR();
        stv[0] = bld<AUX_SC1>(rws, sto, 0); stv[1] = bld<AUX_SC1>(rws, sto + 16, 0);
      }
      const f32x4 v[2] = {get4(stv[0]), get4(stv[1])};
      const bool h0 = wj & 4, h1 = wj & 8;
      const float m = sel4(h0, h1, v[0][0], v[0][1], v[0][2], v[0][3]), l = sel4(h0, h1, v[1][0], v[1][1], v[1][2], v[1][3]);
      float M = fmx(m, dpp<XOR1>(m)); M = fmx(M, dpp<XOR2>(M)); M = fmx(M, dpp<HMIR>(M));
      const float e = fexp(m - M);
      float Ls = l * e; Ls += dpp<XOR1>(Ls); Ls += dpp<XOR2>(Ls); Ls += dpp<HMIR>(Ls);
      cs = e * __builtin_amdgcn_rcpf(Ls);
      PIN(cs);
    }
    v4u bo[4];
    tcol4x8(wor, bo);
    int h2o = WS_H2 + ((kg * 16 + 2 * wv + (n >> 3)) * 128 + wj) * 128 + (n & 7) * 16;
    int oar = L_OA + (n >> 2) * 64 + kg * 16;
    PIN(h2o); PIN(oar);
    SB();
    if (wv < 4) {
      const int off[1] = {WS_H1 + wj * 4096 + wv * 1024 + (lane & 7) * 128 + (lane >> 3) * 16};
      f32x4 v[1];
      sweep<1, 0>(rws, off, v, tag);
#pragma unroll
      for (int e = 0; e < 4; ++e) { float u = v[0][e] * cs; u += dpp<XOR1>(u); u += dpp<XOR2>(u); u += dpp<HMIR>(u); v[0][e] = u; }
      if ((lane & 7) == 0) *(LAS v2u*)(L + L_OA + wv * 64 + (lane >> 3) * 8) = v2u{pack2(v[0][0], v[0][1]), pack2(v[0][2], v[0][3])};
    }
    wg_barrier();
    STAMP(11);
    const v4u a = *(const LAS v4u*)(L + oar);
    st16(rws, h2o, put4(gemv32x64(a, bo), tag));
    SB();
    STAMP(12);
  }
  if (roleR) {
    const int rr = g - 192, u5 = lane & 31;
    const unsigned tsel = 0x07060500u + (lane & 3);
    const float ga = *(const LAS float*)(L + L_GG + u5 * 4), gb = *(const LAS float*)(L + L_GG + 128 + u5 * 4), xin = *(const LAS float*)(L + L_GG + 256 + u5 * 4);
    const int lo = (lane & 7) * 128 + (lane >> 3) * 16;
    float x1 = 0.f;
    {
      const int off[2] = {WS_H2 + rr * 16384 + wv * 2048 + lo, WS_H2 + rr * 16384 + wv * 2048 + 1024 + lo};
      f32x4 v[2];
      if (DLY_H2) __builtin_amdgcn_s_sleep(DLY_H2);
      sweep<2, 0>(rws, off, v, tag);
      v[0] += v[1];
#pragma unroll
      for (int e = 0; e < 4; ++e) { float u = v[0][e]; u += dpp<XOR1>(u); u += dpp<XOR2>(u); u += dpp<HMIR>(u); v[0][e] = u; }
      if ((lane & 7) == 0) *(LAS f32x4*)(L + L_RED + wv * 128 + (lane >> 3) * 16) = v[0];
    }
    wg_barrier();
    STAMP(13);
    if (wv == 0) {
      const LAS float* rp = (const LAS float*)(L + L_RED + u5 * 4);
      const float a = ((rp[0] + rp[32]) + (rp[64] + rp[96])) + ((rp[128] + rp[160]) + (rp[192] + rp[224]));
      x1 = rbf(xin + rbf(ga * rbf(a)));
      const float tw = __uint_as_float(__builtin_amdgcn_perm(__float_as_uint(x1), tag, tsel));
      const f32x4 o = {tw, dpp<0x55>(tw), dpp<0xAA>(tw), dpp<0xFF>(tw)};
      if (lane < 32 && (lane & 3) == 0) st16(rws, WS_X1 + rr * 128 + lane * 4, __builtin_bit_cast(v4u, o));
    }
    STAMP(14);
    if (wv < 7) {
      if (DLY_H4) __builtin_amdgcn_s_sleep(DLY_H4);
      const int off[2] = {WS_H4 + rr * 14336 + wv * 2048 + lo, WS_H4 + rr * 14336 + wv * 2048 + 1024 + lo};
      f32x4 v[2];
      sweep<2, 0>(rws, off, v, tag);
      v[0] += v[1];
#pragma unroll
      for (int e = 0; e < 4; ++e) { float u = v[0][e]; u += dpp<XOR1>(u); u += dpp<XOR2>(u); u += dpp<HMIR>(u); v[0][e] = u; }
      if ((lane & 7) == 0) *(LAS f32x4*)(L + L_RED + 1024 + wv * 128 + (lane >> 3) * 16) = v[0];
    }
    wg_barrier();
    if (wv == 0) {
      const LAS float* rp = (const LAS float*)(L + L_RED + 1024 + u5 * 4);
      const float a = ((rp[0] + rp[32]) + (rp[64] + rp[96])) + ((rp[128] + rp[160]) + rp[192]);
      const float o = x1 + rbf(gb * rbf(a));
      const unsigned pk = pack2(o, dpp<XOR1>(o));
      if (lane < 32 && (lane & 1) == 0) *(unsigned*)(out + rr * 32 + lane) = pk;
      if (g == 192 && lane == 0) __builtin_amdgcn_raw_buffer_store_b32(tag, rws, WS_EPOCH, 0, AUX_SC1);
    }
    STAMP(15);
  } else if (roleG) {
    v2u dnr[8];
    if (wv < 4) {
#pragma unroll
      for (int j = GU_A + GU_B; j < 8; ++j) GU_LD(j);
    }
    {
      const rsrc_t rd = mkrsrc(wd + (size_t)(16 * jsl) * 512, 16384);
      const int dofs = (kg & 1) * 8192 + wv * 128 + n * 8;
#pragma unroll
      for (int j = 0; j < 8; ++j) dnr[j] = __builtin_amdgcn_raw_buffer_load_b64(rd, dofs, j * 1024, AUX_NT);
    }
    if (wv < 4) {
      LAS unsigned char* fb = L + L_FB + ((((n & 3) * 8 * 4 + wv) * 4 + (n >> 2)) * 4 + kg) * 16;
#define TG(E) *(LAS v4u*)(fb + E * 1024) = tcol8<E>(gur);
      FOR8(TG)
#undef TG
    }
    wg_barrier();
    STAMP(12);
    v4u bg[4], bd[2];
#pragma unroll
    for (int t = 0; t < 4; ++t) { bg[t] = *(const LAS v4u*)(L + L_FB + ((((((n >> 1) & 1) * 16 + 2 * wv + (n & 1)) * 4 + t) * 4 + (n >> 2)) * 4 + kg) * 16); PIN(bg[t]); }
    const int rb = wv >> 1, hf = wv & 1;
    f32x4 shv = *(const LAS f32x4*)(L + L_SC + rb * 2048 + hf * 1024 + lane * 16), scv = *(const LAS f32x4*)(L + L_SC + 8192 + rb * 2048 + hf * 1024 + lane * 16);
    PIN(shv); PIN(scv);
    {
      const unsigned sel = (kg >> 1) ? 0x07060302u : 0x05040100u;
#pragma unroll
      for (int m2 = 0; m2 < 2; ++m2) {
#pragma unroll
        for (int jp = 0; jp < 4; ++jp) bd[m2][jp] = __builtin_amdgcn_perm(dnr[2 * jp + 1][m2], dnr[2 * jp][m2], sel);
        PIN(bd[m2]);
      }
    }
    int hbw = L_HB + rb * 1024 + (rb >> 1) * 32 + hf * 512 + lane * 8;
    int hbr = L_HB + (n >> 2) * 1024 + (n >> 3) * 32 + (32 * (n & 3) + 8 * kg) * 2;
    int acr = ((n & 1) == (kg >> 1)) ? L_ACT + (n >> 2) * 32 + (kg & 1) * 16 : L_ZERO;
    int h4o = WS_H4 + ((kg * 16 + 2 * wv + (n >> 3)) * 112 + jsl) * 128 + (n & 7) * 16;
    PIN(hbw); PIN(hbr); PIN(acr); PIN(h4o);
    SB();
    {
      const int off[2] = {WS_X1 + rb * 2048 + hf * 1024 + lane * 16, WS_X1 + rb * 2048 + (hf ^ 1) * 1024 + lane * 16};
      f32x4 v[2];
      sweep<2, 0>(rws, off, v, tag);
      STAMP(13);
      const float x0 = __uint_as_float(__builtin_amdgcn_readfirstlane(__float_as_uint(0.5f * (v[0][0] + v[1][0]))));
      const f32x4 d0 = v[0] - x0, d1 = v[1] - x0;
      const f32x4 sa = d0 + d1, sb2 = d0 * d0 + d1 * d1;
      float s1 = (sa[0] + sa[1]) + (sa[2] + sa[3]), s2 = (sb2[0] + sb2[1]) + (sb2[2] + sb2[3]);
      s1 = wave_sum(s1); s2 = wave_sum(s2);
      const float md = s1 * (1.f / 512.f), var = fmx(s2 * (1.f / 512.f) - md * md, 0.f);
      const float mb = rbf(x0 + md), rden = __builtin_amdgcn_rcpf(rbf(__builtin_amdgcn_sqrtf(rbf(rbf(var) + 1e-5f))));
      const f32x2 ha = rbf2(rbf2(rbf2(f32x2{v[0][0], v[0][1]} - mb) * rden) * f32x2{scv[0], scv[1]}) + f32x2{shv[0], shv[1]};
      const f32x2 hb = rbf2(rbf2(rbf2(f32x2{v[0][2], v[0][3]} - mb) * rden) * f32x2{scv[2], scv[3]}) + f32x2{shv[2], shv[3]};
      *(LAS v2u*)(L + hbw) = v2u{pack2(ha.x, ha.y), pack2(hb.x, hb.y)};
    }
    wg_barrier();
    {
      f32x4 c = {0.f, 0.f, 0.f, 0.f};
#pragma unroll
      for (int t = 0; t < 4; ++t) c = mfma32(*(const LAS v4u*)(L + hbr + t * 256), bg[t], c);
      float gs = sel4(lane & 4, lane & 8, c[0], c[1], c[2], c[3]);
      gs += dpp<ROR4>(gs); gs += dpp<ROR8>(gs);
      gs = rbf(gs);
      const float act = rbf(gelu_f(gs)) * dpp<XOR2>(gs);
      const unsigned pk = pack2(act, dpp<XOR1>(act));
      if (n == 0) *(LAS unsigned*)(L + L_ACT + kg * 32 + wv * 4) = pk;
    }
    wg_barrier();
    STAMP(14);
    {
      const v4u a = *(const LAS v4u*)(L + acr);
      const f32x4 c0 = mfma32(a, bd[0], f32x4{0.f, 0.f, 0.f, 0.f}), c1 = mfma32(a, bd[1], f32x4{0.f, 0.f, 0.f, 0.f});
      st16(rws, h4o, put4(f32x4{c0[0], c0[1], c1[0], c1[1]}, tag));
    }
    STAMP(15);
  }
  STAMP_OUT();
}

extern "C" void kernel_launch(void* const* d_in, const int* in_sizes, int n_in, void* d_out, int out_size, void* d_ws, size_t ws_size, hipStream_t stream) {
  adaln_block_kernel<<<256, 512, 0, stream>>>((const bf16_t*)d_in[0], (const bf16_t*)d_in[1], (const bf16_t*)d_in[2], (const bf16_t*)d_in[3],
                                              (const bf16_t*)d_in[4], (const bf16_t*)d_in[6], (const bf16_t*)d_in[7], (const bf16_t*)d_in[8],
                                              (const bf16_t*)d_in[9], (const bf16_t*)d_in[10], (bf16_t*)d_out, (unsigned char*)d_ws);
}
```

```cpp
#include <hip/hip_runtime.h>
#include <hip/hip_bf16.h>
#include <stdint.h>

#define GAS __attribute__((address_space(1)))
#define LAS __attribute__((address_space(3)))
typedef unsigned short bf16_t;
typedef unsigned v4u __attribute__((ext_vector_type(4)));
typedef unsigned v2u __attribute__((ext_vector_type(2)));
typedef float f32x4 __attribute__((ext_vector_type(4)));
typedef float f32x2 __attribute__((ext_vector_type(2)));
typedef __bf16 bf16x8 __attribute__((ext_vector_type(8)));
typedef __bf16 bf16x2 __attribute__((ext_vector_type(2)));
typedef short s16x4 __attribute__((ext_vector_type(4)));
typedef __amdgpu_buffer_rsrc_t rsrc_t;

#define PLO 4
#define PHI 8
#define SCH_A 110
#define SCH_T16 250
#define AUX_W 2
#define T_MOD 380
#define T_Q 640
#define GU_A 0
#define GU_B 3
#define DLY_H4 34
#define VE_AT 26
#define XREP 4
#define ABL 0

namespace {
constexpr int SK = 16384, WIN = 4096, FF = 1792;
constexpr int AUX_SC1 = 16, AUX_NT = 2;
constexpr int WS_EPOCH = 0;
constexpr int WS_X1 = 4096;
constexpr int WS_SB = WS_X1 + 32768;
constexpr int WS_CB = WS_SB + 8192;
constexpr int WS_ST = WS_CB + 8192;
constexpr int WS_MOD = WS_ST + 32768;
constexpr int WS_QP = WS_MOD + 196608;
constexpr int WS_H1 = WS_QP + 524288;
constexpr int WS_H2 = WS_H1 + 524288;
constexpr int WS_H4 = WS_H2 + 262144;
constexpr int WS_END = WS_H4 + 917504;
constexpr int WS_STAMP = 8 << 20;
constexpr int L_K = 0;
constexpr int L_FB = 0;
constexpr int L_WO = 0;
constexpr int L_SC = 32768;
constexpr int L_P = 131072;
constexpr int L_QB = L_P + 4096;
constexpr int L_HQ = L_QB + 1280;
constexpr int L_ZERO = L_HQ + 1024;
constexpr int L_FLAG = L_ZERO + 1024;
constexpr int L_GG = L_FLAG + 256;
constexpr int L_OA = L_GG + 384;
constexpr int L_CS = L_OA + 1280;
constexpr int L_F = L_CS + 128;
constexpr int L_ACT = L_F + 128;
constexpr int L_RED = L_ACT + 128;
constexpr int L_MRG = L_RED + 2048;
constexpr int L_HB = L_MRG + 8192;
constexpr int L_END = L_HB + 4096 + 64;
enum { F_MCNT = 0, F_HCNT = 1, F_QCNT = 2, F_K = 4, F_PCNT = 8, F_G = 10, F_H4 = 12, F_WCNT = 13, F_VI = 16, F_VE = 20, F_CS = 24 };
constexpr unsigned SPIN_G = 1u << 16, SPIN_L = 1u << 20;

__device__ __forceinline__ float bflo(unsigned u) { return __uint_as_float(u << 16); }
__device__ __forceinline__ float bfhi(unsigned u) { return __uint_as_float(u & 0xFFFF0000u); }
__device__ __forceinline__ unsigned pack2(float lo, float hi) {
  f32x2 v = {lo, hi};
  return __builtin_bit_cast(unsigned, __builtin_convertvector(v, bf16x2));
}
__device__ __forceinline__ float rbf(float x) { return bflo(pack2(x, x) & 0xFFFFu); }
__device__ __forceinline__ float fmx(float a, float b) { return __builtin_amdgcn_fmed3f(a, b, __builtin_inff()); }
template <int CTRL> __device__ __forceinline__ float dpp(float x) {
  return __builtin_bit_cast(float, __builtin_amdgcn_mov_dpp(__builtin_bit_cast(int, x), CTRL, 0xf, 0xf, true));
}
constexpr int XOR1 = 0xB1, XOR2 = 0x4E, HMIR = 0x141, ROR2 = 0x122, ROR4 = 0x124, ROR8 = 0x128;
__device__ __forceinline__ float xrow_sum(float x) {
  auto s = __builtin_amdgcn_permlane16_swap(__float_as_uint(x), __float_as_uint(x), false, false);
  x = __uint_as_float(s[0]) + __uint_as_float(s[1]);
  auto t = __builtin_amdgcn_permlane32_swap(__float_as_uint(x), __float_as_uint(x), false, false);
  return __uint_as_float(t[0]) + __uint_as_float(t[1]);
}
__device__ __forceinline__ float xrow_max(float x) {
  auto s = __builtin_amdgcn_permlane16_swap(__float_as_uint(x), __float_as_uint(x), false, false);
  x = fmx(__uint_as_float(s[0]), __uint_as_float(s[1]));
  auto t = __builtin_amdgcn_permlane32_swap(__float_as_uint(x), __float_as_uint(x), false, false);
  return fmx(__uint_as_float(t[0]), __uint_as_float(t[1]));
}
__device__ __forceinline__ float wave_sum(float x) {
  x += dpp<XOR1>(x); x += dpp<XOR2>(x); x += dpp<ROR4>(x); x += dpp<ROR8>(x);
  return xrow_sum(x);
}
__device__ __forceinline__ f32x4 mfma32(v4u a, v4u b, f32x4 c) {
  return __builtin_amdgcn_mfma_f32_16x16x32_bf16(__builtin_bit_cast(bf16x8, a), __builtin_bit_cast(bf16x8, b), c, 0, 0, 0);
}
template <int E> __device__ __forceinline__ v4u tcol8(const v4u* r) {
  constexpr unsigned sel = (E & 1) ? 0x07060302u : 0x05040100u;
  v4u o;
  o.x = __builtin_amdgcn_perm(r[1][E >> 1], r[0][E >> 1], sel);
  o.y = __builtin_amdgcn_perm(r[3][E >> 1], r[2][E >> 1], sel);
  o.z = __builtin_amdgcn_perm(r[5][E >> 1], r[4][E >> 1], sel);
  o.w = __builtin_amdgcn_perm(r[7][E >> 1], r[6][E >> 1], sel);
  return o;
}
__device__ __forceinline__ v4u tcol8r(const v4u* r, int e) {
  const unsigned sel = (e & 1) ? 0x07060302u : 0x05040100u;
  v4u o;
  o.x = __builtin_amdgcn_perm(r[1][e >> 1], r[0][e >> 1], sel);
  o.y = __builtin_amdgcn_perm(r[3][e >> 1], r[2][e >> 1], sel);
  o.z = __builtin_amdgcn_perm(r[5][e >> 1], r[4][e >> 1], sel);
  o.w = __builtin_amdgcn_perm(r[7][e >> 1], r[6][e >> 1], sel);
  return o;
}
#define FOR8(X) X(0) X(1) X(2) X(3) X(4) X(5) X(6) X(7)
__device__ __forceinline__ rsrc_t mkrsrc(const void* p, int bytes) { return __builtin_amdgcn_make_buffer_rsrc((void*)p, 0, bytes, 0x00020000); }
template <int AUX> __device__ __forceinline__ v4u bld(rsrc_t r, int voff, int soff) { return __builtin_amdgcn_raw_buffer_load_b128(r, voff, soff, AUX); }
__device__ __forceinline__ unsigned put1(float v, unsigned t) { return (__float_as_uint(v) & ~15u) | (t & 15u); }
__device__ __forceinline__ v4u put4(f32x4 v, unsigned tag) { return v4u{put1(v.x, tag), put1(v.y, tag >> 8), put1(v.z, tag >> 16), put1(v.w, tag >> 24)}; }
__device__ __forceinline__ unsigned tagdiff(v4u v, unsigned tag) {
  return (__builtin_amdgcn_perm(v.y, v.x, 0x0c0c0400u) | __builtin_amdgcn_perm(v.w, v.z, 0x04000c0cu)) ^ tag;
}
__device__ __forceinline__ bool all_ok(unsigned diff) { return __all((diff & 0x0F0F0F0Fu) == 0u); }
__device__ __forceinline__ f32x4 get4(v4u v) { return __builtin_bit_cast(f32x4, v & ~15u); }
__device__ __forceinline__ void st16(rsrc_t r, int off, v4u v) { __builtin_amdgcn_raw_buffer_store_b128(v, r, off, 0, AUX_SC1); }
#define CBAR() asm volatile("" ::: "memory")
#define LGKM0() asm volatile("s_waitcnt lgkmcnt(0)" ::: "memory")
#define SB() __builtin_amdgcn_sched_barrier(0)
#define PIN(x) asm volatile("" : "+v"(x))
__device__ __forceinline__ void lds_wait(volatile LAS unsigned* f, unsigned want = 1u) {
#pragma unroll 1
  for (unsigned i = 0; i < SPIN_L; ++i) { if (*f >= want) break; __builtin_amdgcn_s_sleep(1); }
  CBAR();
}
__device__ __forceinline__ void lds_set(volatile LAS unsigned* f) { LGKM0(); *f = 1u; }
__device__ __forceinline__ void lds_arrive(LAS unsigned* f, int lane) {
  LGKM0();
  if (lane == 0) __hip_atomic_fetch_add(f, 1u, __ATOMIC_RELAXED, __HIP_MEMORY_SCOPE_WORKGROUP);
}
__device__ __forceinline__ void wg_barrier() { LGKM0(); __builtin_amdgcn_s_barrier(); CBAR(); }
template <int N, int SLEEP> __device__ __forceinline__ void sweep(rsrc_t rs, const int (&off)[N], f32x4 (&o)[N], unsigned tag) {
  v4u v[N];
#pragma unroll 1
  for (unsigned spin = 0;; ++spin) {
    unsigned bad = 0;
#pragma unroll
    for (int i = 0; i < N; ++i) v[i] = bld<AUX_SC1>(rs, off[i], 0);
#pragma unroll
    for (int i = 0; i < N; ++i) bad |= tagdiff(v[i], tag);
    if (all_ok(bad) || spin >= SPIN_G) break;
    if (SLEEP) __builtin_amdgcn_s_sleep(SLEEP);
    CBAR();
  }
#pragma unroll
  for (int i = 0; i < N; ++i) o[i] = get4(v[i]);
}
__device__ __forceinline__ float fexp(float x) { return __builtin_amdgcn_exp2f(x * 1.4426950408889634f); }
__device__ __forceinline__ float silu_f(float x) { return x * __builtin_amdgcn_rcpf(1.f + fexp(-x)); }
__device__ __forceinline__ float gelu_f(float x) {
  const float u = 0.7978845608028654f * (x + 0.044715f * x * x * x);
  return x * __builtin_amdgcn_rcpf(1.f + fexp(-2.f * u));
}
__device__ __forceinline__ void tcol4x8(const v2u* r, v4u* o) {
#pragma unroll
  for (int e = 0; e < 4; ++e) {
    const unsigned sel = (e & 1) ? 0x07060302u : 0x05040100u;
#pragma unroll
    for (int jp = 0; jp < 4; ++jp) o[e][jp] = __builtin_amdgcn_perm(r[2 * jp + 1][e >> 1], r[2 * jp][e >> 1], sel);
    PIN(o[e]);
  }
}
__device__ __forceinline__ f32x4 gemv32x64(v4u a, const v4u* bt) {
  f32x4 c[4];
#pragma unroll
  for (int e = 0; e < 4; ++e) c[e] = mfma32(a, bt[e], f32x4{0.f, 0.f, 0.f, 0.f});
  return f32x4{c[0][0], c[1][0], c[2][0], c[3][0]};
}
__device__ __forceinline__ void wait_until(unsigned long long t0, unsigned ticks) {
#pragma unroll 1
  for (int i = 0; i < 4096 && __builtin_amdgcn_s_memrealtime() - t0 < ticks; ++i) __builtin_amdgcn_s_sleep(1);
}
__device__ __forceinline__ float sel4(bool b0, bool b1, float c0, float c1, float c2, float c3) {
  const float lo = b0 ? c1 : c0, hi = b0 ? c3 : c2;
  return b1 ? hi : lo;
}
__device__ __forceinline__ float part_sum(f32x4 v) {
  auto p = __builtin_amdgcn_permlane32_swap(__float_as_uint(v[0]), __float_as_uint(v[2]), false, false);
  auto q = __builtin_amdgcn_permlane32_swap(__float_as_uint(v[1]), __float_as_uint(v[3]), false, false);
  const float A = __uint_as_float(p[0]) + __uint_as_float(p[1]), B = __uint_as_float(q[0]) + __uint_as_float(q[1]);
  auto r = __builtin_amdgcn_permlane16_swap(__float_as_uint(A), __float_as_uint(B), false, false);
  const float t = __uint_as_float(r[0]) + __uint_as_float(r[1]);
  return t + dpp<ROR8>(t);
}
__device__ __forceinline__ float split_weight(f32x4 m4, f32x4 l4, int hh) {
  const bool h0 = hh & 1, h1 = hh & 2;
  const float m = sel4(h0, h1, m4[0], m4[1], m4[2], m4[3]), l = sel4(h0, h1, l4[0], l4[1], l4[2], l4[3]);
  float M = fmx(m, dpp<XOR1>(m)); M = fmx(M, dpp<XOR2>(M)); M = fmx(M, dpp<HMIR>(M));
  const float e = fexp(m - M);
  float Ls = l * e; Ls += dpp<XOR1>(Ls); Ls += dpp<XOR2>(Ls); Ls += dpp<HMIR>(Ls);
  return e * __builtin_amdgcn_rcpf(Ls);
}
__device__ __forceinline__ f32x2 rbf2(f32x2 v) {
  const unsigned p = pack2(v.x, v.y);
  return f32x2{bflo(p), bfhi(p)};
}
template <bool MAX> __device__ __forceinline__ float red2(float a, float b) { return MAX ? fmx(a, b) : a + b; }
template <bool MAX> __device__ __forceinline__ f32x4 wave_red4(f32x4 s, int lane) {
  const bool o1 = lane & 1, o2 = lane & 2;
  const float a = red2<MAX>(o1 ? s[1] : s[0], dpp<XOR1>(o1 ? s[0] : s[1])), c = red2<MAX>(o1 ? s[3] : s[2], dpp<XOR1>(o1 ? s[2] : s[3]));
  float m = red2<MAX>(o2 ? c : a, dpp<XOR2>(o2 ? a : c));
  m = red2<MAX>(m, dpp<ROR4>(m)); m = red2<MAX>(m, dpp<ROR8>(m));
  m = MAX ? xrow_max(m) : xrow_sum(m);
  return f32x4{dpp<0x00>(m), dpp<0x55>(m), dpp<0xAA>(m), dpp<0xFF>(m)};
}
#define STAMP(i) do {} while (0)
#define STAMP_OUT() do {} while (0)
}

__global__ void __launch_bounds__(512, 2)
adaln_block_kernel(const bf16_t* __restrict__ x, const bf16_t* __restrict__ kc, const bf16_t* __restrict__ vc,
                   const bf16_t* __restrict__ wq, const bf16_t* __restrict__ wo, const bf16_t* __restrict__ wg,
                   const bf16_t* __restrict__ wu, const bf16_t* __restrict__ wd, const bf16_t* __restrict__ cc,
                   const bf16_t* __restrict__ wmod, bf16_t* __restrict__ out, unsigned char* ws) {
  __shared__ __attribute__((aligned(16))) unsigned char lds_raw[L_END];
  LAS unsigned char* L = (LAS unsigned char*)lds_raw;
  volatile LAS unsigned* FL = (volatile LAS unsigned*)(L + L_FLAG);
  LAS unsigned* FC = (LAS unsigned*)(L + L_FLAG);
  const unsigned long long tstart = __builtin_amdgcn_s_memrealtime();
  const int tid = threadIdx.x, lane = tid & 63, wv = __builtin_amdgcn_readfirstlane(tid >> 6);
  const int g = blockIdx.x, grp = (g & 7) * 4 + (g >> 6), b = grp >> 3, kvh = grp & 7, s = (g >> 3) & 7;
  const int n = lane & 15, kg = lane >> 4;
  if (tid < 320) *(LAS unsigned*)(L + L_ZERO + tid * 4) = 0u;
  asm volatile("" :: "s"(x), "s"(kc), "s"(vc), "s"(wq), "s"(cc), "s"(wmod), "s"(ws));
  wg_barrier();
  const rsrc_t rws = mkrsrc(ws, WS_END);
  unsigned tag;
  v4u gur[8];

  if (__builtin_expect(wv >= 4, 1)) {
    const int w = wv - 4;
    const size_t kvbase = ((size_t)grp * SK + (SK - WIN) + s * 512) * 128;
    const int rot = (g * 37) & 127;
    const unsigned char* kbase = (const unsigned char*)(kc + kvbase);
    const unsigned ldsk = (unsigned)(uintptr_t)(L + L_K);
    const int klane = kg * 256 + ((n ^ kg) << 4);
    const unsigned due0 = (unsigned)tstart + SCH_A;
#define PACE_WAIT(now, i) do { \
      asm volatile("s_waitcnt vmcnt(%0)" :: "i"(PHI) : "memory"); \
      if (PHI > PLO && (int)((now) - due0) < (((i) * SCH_T16) >> 4)) asm volatile("s_waitcnt vmcnt(%0)" :: "i"(PLO) : "memory"); } while (0)
    STAMP(7);
#pragma unroll 4
    for (int i = 0; i < 32; ++i) {
      const int phys = (4 * i + w + rot) & 127;
      const unsigned char* sb_ = kbase + phys * 1024;
      const int vo = klane ^ ((i & 3) << 6);
      const unsigned dst = ldsk + (4 * i + w) * 1024;
      unsigned keep;
      asm volatile("s_mov_b32 %0, m0\n\ts_mov_b32 m0, %3\n\ts_nop 0\n\tglobal_load_lds_dwordx4 %1, %2 nt\n\ts_mov_b32 m0, %0"
                   : "=&s"(keep) : "v"(vo), "s"(sb_), "s"(dst) : "memory");
      const unsigned now = (unsigned)__builtin_amdgcn_s_memrealtime();
      PACE_WAIT(now, i);
    }
    STAMP(1);
    const unsigned epoch = __builtin_amdgcn_raw_buffer_load_b32(rws, WS_EPOCH, 0, AUX_SC1);
    const rsrc_t rv = mkrsrc(vc + kvbase, 131072);
    v4u V[32], VT[24];
    constexpr int TR0 = 8 + PHI;
#pragma unroll
    for (int i = 0; i < 32; ++i) {
      V[i] = bld<AUX_NT>(rv, lane * 16, ((4 * i + w + rot) & 127) * 1024);
      const unsigned now = (unsigned)__builtin_amdgcn_s_memrealtime();
      if (i >= TR0) { const int k = i - TR0; VT[k] = tcol8r(V + 8 * (k >> 3), k & 7); PIN(VT[k]); }
      if (i == PLO - 1) { asm volatile("s_waitcnt vmcnt(%0)" :: "i"(PLO) : "memory"); FL[F_K + w] = 1u; STAMP(2); }
      else if (i < 31) PACE_WAIT(now, 32 + i);
      if (i == VE_AT) FL[F_VE + w] = 1u;
      SB();
    }
    FL[F_VI + w] = 1u;
    tag = __builtin_amdgcn_readfirstlane(epoch) + 0x01010101u;
    STAMP(3);
#pragma unroll
    for (int k = 32 - TR0; k < 24; ++k) { VT[k] = tcol8r(V + 8 * (k >> 3), k & 7); PIN(VT[k]); }
    lds_wait(FL + F_PCNT, 4u);
    STAMP(4);
    f32x4 f;
    {
      const LAS f32x4* sp = (const LAS f32x4*)(L + L_F);
      const f32x4 m0 = sp[0], m1 = sp[2], m2 = sp[4], m3 = sp[6], mo = sp[2 * w];
#pragma unroll
      for (int h = 0; h < 4; ++h) f[h] = fexp(mo[h] - fmx(fmx(m0[h], m1[h]), fmx(m2[h], m3[h])));
    }
    f32x4 acc[2] = {f32x4{0.f, 0.f, 0.f, 0.f}, f32x4{0.f, 0.f, 0.f, 0.f}};
    int paddr[4];
#pragma unroll
    for (int ep = 0; ep < 4; ++ep) paddr[ep] = (n >> 2) == ep ? L_P + w * 1024 + (((n & 3) * 4 + kg) << 4) : L_ZERO;
    int mra = L_MRG + (w * 512 + 8 * n + 2 * kg) * 4;
#pragma unroll
    for (int G = 0; G < 4; ++G) {
      v4u pa[4];
#pragma unroll
      for (int ep = 0; ep < 4; ++ep) pa[ep] = *(const LAS v4u*)(L + paddr[ep] + G * 256);
      if (G == 3) { PIN(pa[0]); PIN(pa[1]); PIN(pa[2]); PIN(pa[3]); PIN(f); PIN(mra); PIN(acc[0]); PIN(acc[1]); STAMP(5); PIN(V[24]); SB(); }
#define PV(E) acc[E & 1] = mfma32(pa[E >> 1], G < 3 ? VT[8 * G + E] : tcol8<E>(V + 24), acc[E & 1]);
      FOR8(PV)
#undef PV
      SB();
    }
    STAMP(6);
#pragma unroll
    for (int h = 0; h < 4; ++h) *(LAS f32x2*)(L + mra + h * 512) = f32x2{acc[0][h], acc[1][h]} * f[h];
  } else if (ABL == 6) {
    lds_arrive(FC + F_PCNT, lane); tag = 0;
  } else {
    const int cw = wv;
    __builtin_amdgcn_s_setprio(2);
    const int ks = g >> 5, ns = g & 31, u5 = lane & 31, h5 = lane >> 5;
    const int mkq = g < 64 ? g >> 4 : (g - 64) >> 5, mcb = g < 64 ? g & 15 : 16 + ((g - 64) & 31);
    v2u wm[8], wr[8];
    const rsrc_t rm = mkrsrc(wmod + (size_t)(mkq * 128 + cw * 32) * 3072, g < 192 ? 32 * 6144 : 0);
    const rsrc_t rq = mkrsrc(wq + (size_t)(ks * 64 + (cw & 1) * 32) * 4096, 32 * 8192);
    const v4u cf = *(const v4u*)(cc + (n >> 2) * 512 + ((128 * mkq) & 511) + 32 * cw + 8 * kg);
#pragma unroll
    for (int j = 0; j < 8; ++j) wm[j] = __builtin_amdgcn_raw_buffer_load_b64(rm, 8 * kg * 6144 + mcb * 128 + n * 8, j * 6144, AUX_NT);
    const unsigned epoch = __builtin_amdgcn_raw_buffer_load_b32(rws, WS_EPOCH, 0, AUX_SC1);
    const v4u xs = *(const v4u*)(x + cw * 512 + lane * 8);
    const v2u xo = *(const v2u*)(x + cw * 512 + 64 * ks + 4 * n);
#pragma unroll
    for (int j = 0; j < 8; ++j) wr[j] = __builtin_amdgcn_raw_buffer_load_b64(rq, 8 * kg * 8192 + ns * 256 + (cw >> 1) * 128 + n * 8, j * 8192, AUX_NT);
    SB();
    STAMP(1);
    v4u bq[4];
    {
      v4u a, bt[4];
#pragma unroll
      for (int i = 0; i < 4; ++i) a[i] = pack2(silu_f(bflo(cf[i])), silu_f(bfhi(cf[i])));
      PIN(a);
      tcol4x8(wm, bt);
      *(LAS f32x4*)(L + L_MRG + cw * 1024 + lane * 16) = gemv32x64(a, bt);
      lds_arrive(FC + F_MCNT, lane);
      tag = __builtin_amdgcn_readfirstlane(epoch) + 0x01010101u;
      if (cw == 0 && g < 192) {
        lds_wait(FL + F_MCNT, 4u);
        const LAS f32x4* mp = (const LAS f32x4*)(L + L_MRG + lane * 16);
        st16(rws, WS_MOD + ((mkq * 4 + kg) * 3072 + 64 * mcb + 4 * n) * 4, put4((mp[0] + mp[64]) + (mp[128] + mp[192]), tag));
        STAMP(11);
      }
    }
    float ln[4];
    {
      float sum = 0.f;
#pragma unroll
      for (int i = 0; i < 4; ++i) sum += bflo(xs[i]) + bfhi(xs[i]);
      const float mean = wave_sum(sum) * (1.f / 512.f);
      float sq = 0.f;
#pragma unroll
      for (int i = 0; i < 4; ++i) { const float d0 = bflo(xs[i]) - mean, d1 = bfhi(xs[i]) - mean; sq = fmaf(d0, d0, sq); sq = fmaf(d1, d1, sq); }
      sq = wave_sum(sq);
      const float mb = rbf(mean), den = rbf(sqrtf(rbf(rbf(sq * (1.f / 512.f)) + 1e-5f)));
#pragma unroll
      for (int i = 0; i < 2; ++i) { ln[2 * i] = rbf(rbf(bflo(xo[i]) - mb) / den); ln[2 * i + 1] = rbf(rbf(bfhi(xo[i]) - mb) / den); }
    }
    tcol4x8(wr, bq);
    STAMP(2);
    wait_until(tstart, T_MOD);
    {
      const int mo = WS_MOD + ((h5 * 4 + cw) * 3072 + (kg & 1) * 512 + 64 * ks + 4 * n) * 4;
      const int off[2] = {mo, mo + 2 * 4 * 3072 * 4};
      f32x4 v[2];
      sweep<2, 8>(rws, off, v, tag);
      STAMP(3);
      const f32x4 t = v[0] + v[1];
      float h[4];
#pragma unroll
      for (int e = 0; e < 4; ++e) {
        auto p = __builtin_amdgcn_permlane32_swap(__float_as_uint(t[e]), __float_as_uint(t[e]), false, false);
        const float u = rbf(__uint_as_float(p[0]) + __uint_as_float(p[1]));
        auto r = __builtin_amdgcn_permlane16_swap(__float_as_uint(u), __float_as_uint(u), false, false);
        h[e] = rbf(ln[e] * rbf(1.f + __uint_as_float(r[1]))) + __uint_as_float(r[0]);
      }
      if (lane < 16) *(LAS v2u*)(L + L_HQ + cw * 128 + n * 8) = v2u{pack2(h[0], h[1]), pack2(h[2], h[3])};
      lds_arrive(FC + F_HCNT, lane);
      lds_wait(FL + F_HCNT, 4u);
      const v4u a = *(const LAS v4u*)(L + L_HQ + (n >> 2) * 128 + (cw & 1) * 64 + kg * 16);
      const f32x4 qp = gemv32x64(a, bq);
      if (cw & 1) {
        *(LAS f32x4*)(L + L_MRG + 4096 + (cw >> 1) * 1024 + lane * 16) = qp;
        lds_set(FL + F_G + (cw >> 1));
      } else {
        lds_wait(FL + F_G + (cw >> 1));
        st16(rws, WS_QP + ((kg * 8 + ks) * 4096 + 128 * ns + 64 * (cw >> 1) + 4 * n) * 4, put4(qp + *(const LAS f32x4*)(L + L_MRG + 4096 + (cw >> 1) * 1024 + lane * 16), tag));
      }
    }
    STAMP(4);
    if (cw == 3 && g >= 192) {
      const int rr = g - 192;
      const int mo = WS_MOD + ((((lane >> 3) & 3) * 4 + (rr >> 4)) * 3072 + 1024 + h5 * 512 + 32 * (rr & 15) + 4 * (lane & 7)) * 4;
      const int off[2] = {mo, mo + 4096};
      f32x4 v[2];
      const unsigned short xin = x[rr * 32 + u5];
      sweep<2, 8>(rws, off, v, tag);
#pragma unroll
      for (int i = 0; i < 2; ++i)
#pragma unroll
        for (int e = 0; e < 4; ++e) {
          float u = v[i][e]; u += dpp<ROR8>(u);
          auto p = __builtin_amdgcn_permlane16_swap(__float_as_uint(u), __float_as_uint(u), false, false);
          v[i][e] = rbf(__uint_as_float(p[0]) + __uint_as_float(p[1]));
        }
      *(LAS float*)(L + L_GG + 256 + u5 * 4) = bflo(xin);
      if (u5 < 8) {
        if (lane < 32) {
          *(LAS f32x4*)(L + L_GG + lane * 16) = v[0];
          st16(rws, WS_CB + rr * 128 + lane * 16, put4(f32x4{rbf(1.f + v[1][0]), rbf(1.f + v[1][1]), rbf(1.f + v[1][2]), rbf(1.f + v[1][3])}, tag));
        } else {
          *(LAS f32x4*)(L + L_GG + 128 + u5 * 16) = v[1];
          st16(rws, WS_SB + rr * 128 + u5 * 16, put4(v[0], tag));
        }
      }
    }
    const int qo = WS_QP + ((b * 8 + h5) * 4096 + kvh * 512 + cw * 128 + 4 * u5) * 4;
    wait_until(tstart, T_Q);
    v4u qv[4];
#pragma unroll
    for (int i = 0; i < 4; ++i) qv[i] = bld<AUX_SC1>(rws, qo, i * 32768);
    SB();
    lds_wait(FL + F_K + cw);
    STAMP(5);
    v4u kf[32];
#pragma unroll
    for (int t = 0; t < 8; ++t)
#pragma unroll
      for (int ds = 0; ds < 4; ++ds)
        kf[4 * t + ds] = *(const LAS v4u*)(L + L_K + (16 * (4 * t + (n >> 2)) + 4 * cw + (n & 3)) * 256 + (((4 * ds + kg) ^ n) << 4));
    SB();
    {
#pragma unroll 1
      for (unsigned spin = 0; spin < SPIN_G; ++spin) {
        if (all_ok(tagdiff(qv[0], tag) | tagdiff(qv[1], tag) | tagdiff(qv[2], tag) | tagdiff(qv[3], tag))) break;
        __builtin_amdgcn_s_sleep(4);
#pragma unroll
        for (int i = 0; i < 4; ++i) qv[i] = bld<AUX_SC1>(rws, qo, i * 32768);
      }
      const f32x4 t = (get4(qv[0]) + get4(qv[1])) + (get4(qv[2]) + get4(qv[3]));
      float q[4];
#pragma unroll
      for (int e = 0; e < 4; ++e) {
        auto p = __builtin_amdgcn_permlane32_swap(__float_as_uint(t[e]), __float_as_uint(t[e]), false, false);
        q[e] = __uint_as_float(p[0]) + __uint_as_float(p[1]);
      }
      if (lane < 32) *(LAS v2u*)(L + L_QB + cw * 320 + u5 * 8) = v2u{pack2(q[0], q[1]), pack2(q[2], q[3])};
      lds_arrive(FC + F_QCNT, lane);
      lds_wait(FL + F_QCNT, 4u);
    }
    STAMP(6);
    {
      f32x4 sc[2];
      v4u qf[4];
#pragma unroll
      for (int ds = 0; ds < 4; ++ds) qf[ds] = *(const LAS v4u*)(L + L_QB + (n & 3) * 320 + kg * 16 + ds * 64);
      f32x4 c[8];
#pragma unroll
      for (int t = 0; t < 8; ++t) {
        c[t] = f32x4{0.f, 0.f, 0.f, 0.f};
#pragma unroll
        for (int ds = 0; ds < 4; ++ds) c[t] = mfma32(qf[ds], kf[4 * t + ds], c[t]);
      }
#pragma unroll
      for (int sg = 0; sg < 2; ++sg)
#pragma unroll
        for (int h = 0; h < 4; ++h) sc[sg][h] = sel4(lane & 16, lane & 32, c[4 * sg][h], c[4 * sg + 1][h], c[4 * sg + 2][h], c[4 * sg + 3][h]);
      f32x4 e0, e1, mx;
#pragma unroll
      for (int h = 0; h < 4; ++h) {
        const f32x2 r = rbf2(rbf2(f32x2{sc[0][h], sc[1][h]}) * 0.08838834764831845f);
        e0[h] = r.x; e1[h] = r.y; mx[h] = fmx(r.x, r.y);
      }
      const f32x4 mw = wave_red4<true>(mx, lane);
#pragma unroll
      for (int h = 0; h < 4; ++h) {
        e0[h] = fexp(e0[h] - mw[h]); e1[h] = fexp(e1[h] - mw[h]);
        const unsigned pp = pack2(e0[h], e1[h]);
        LAS unsigned short* pd = (LAS unsigned short*)(L + L_P) + ((((cw * 4 + (kg >> 1)) * 4 + h) * 4 + (n & 3)) * 8 + 4 * (kg & 1) + (n >> 2));
        pd[0] = (unsigned short)(pp & 0xFFFFu);
        pd[2 * 128] = (unsigned short)(pp >> 16);
      }
      const f32x4 lw = wave_red4<false>(e0 + e1, lane);
      if (lane < 2) *(LAS f32x4*)(L + L_F + cw * 32 + lane * 16) = lane == 0 ? mw : lw;
      lds_arrive(FC + F_PCNT, lane);
    }
    STAMP(7);
    lds_wait(FL + F_PCNT, 4u);
    if (cw == 3) {
      const LAS f32x4* sp = (const LAS f32x4*)(L + L_F);
      f32x4 M, Ls;
#pragma unroll
      for (int h = 0; h < 4; ++h) {
        M[h] = fmx(fmx(sp[0][h], sp[2][h]), fmx(sp[4][h], sp[6][h]));
        Ls[h] = (sp[1][h] * fexp(sp[0][h] - M[h]) + sp[3][h] * fexp(sp[2][h] - M[h])) + (sp[5][h] * fexp(sp[4][h] - M[h]) + sp[7][h] * fexp(sp[6][h] - M[h]));
      }
      if (lane < 2) st16(rws, WS_ST + (grp * 8 + s) * 128 + lane * 16, put4(lane == 0 ? M : Ls, tag));
    }
#define GU_PTR() ((n & 2) ? wu : wg) + (size_t)(128 * wv + 32 * (n >> 2) + 8 * kg) * FF + 16 * jsl + 8 * (n & 1)
#define GU_LD(j) gur[j] = AUX_W ? __builtin_nontemporal_load((const v4u*)(gup + (j) * FF)) : *(const v4u*)(gup + (j) * FF)
#define W_HEAD() ((g & 7) + 8 * ((g - 128) >> 5))
#define W_CQ() ((g >> 3) & 3)
#define ST_OFF() (WS_ST + (((wv & 3) * 8 + (W_HEAD() >> 2)) * 8 + (lane & 7)) * 128)
    const int ru = ((g >> 5) << 3) + (g & 7), jsl = 4 * ru + ((g >> 3) & 3);
    if (g < 128) {
      if (ru < 28) {
        const bf16_t* gup = GU_PTR();
        const int mo = WS_SB + cw * 2048 + lane * 16;
        const int off[4] = {mo, mo + 1024, mo + 8192, mo + 8192 + 1024};
        f32x4 pre[4];
        sweep<4, 8>(rws, off, pre, tag);
#pragma unroll
        for (int i = 0; i < 4; ++i) *(LAS f32x4*)(L + L_SC + (i >> 1) * 8192 + cw * 2048 + (i & 1) * 1024 + lane * 16) = pre[i];
#pragma unroll
        for (int j = 0; j < GU_A; ++j) { GU_LD(j); if (j + 1 < GU_A) asm volatile("s_waitcnt vmcnt(0)" ::: "memory"); SB(); }
        STAMP(8);
        if (GU_B) {
          lds_wait(FL + F_VI + cw);
#pragma unroll
          for (int j = GU_A; j < GU_A + GU_B; ++j) GU_LD(j);
          SB();
        }
      }
    } else {
      const unsigned char* wsrc = (const unsigned char*)(wo + (size_t)(W_HEAD() * 128 + cw * 32) * 512 + W_CQ() * 128);
      const unsigned wdst = (unsigned)(uintptr_t)(L + L_WO) + cw * 8192;
      const int vo = kg * 1024 + n * 16, sto = ST_OFF();
      lds_wait(FL + F_VE + cw);
      const v4u s0 = bld<AUX_SC1>(rws, sto, 0), s1 = bld<AUX_SC1>(rws, sto + 16, 0);
      if (all_ok(tagdiff(s0, tag) | tagdiff(s1, tag))) {
        const float cs = split_weight(get4(s0), get4(s1), W_HEAD());
        if (lane < 8) *(LAS float*)(L + L_CS + cw * 32 + lane * 4) = cs;
        lds_set(FL + F_CS + cw);
      }
      STAMP(8);
      lds_wait(FL + F_VI + cw);
#pragma unroll
      for (int j = 0; j < 8; ++j) {
        unsigned keep;
        asm volatile("s_mov_b32 %0, m0\n\ts_mov_b32 m0, %3\n\ts_nop 0\n\tglobal_load_lds_dwordx4 %1, %2 nt\n\ts_mov_b32 m0, %0"
                     : "=&s"(keep) : "v"(vo), "s"(wsrc + j * 4096), "s"(wdst + j * 1024) : "memory");
      }
    }
  }
  int h1o = WS_H1 + (grp * 8 + s) * 2048 + tid * 16;
  PIN(h1o);
  STAMP(9);
  wg_barrier();
  if (wv < 2) {
    const LAS f32x4* mp = (const LAS f32x4*)(L + L_MRG + tid * 16);
    st16(rws, h1o, put4((mp[0] + mp[128]) + (mp[256] + mp[384]), tag));
  }
  SB();
  STAMP(10);
  if (ABL) { STAMP_OUT(); return; }
  if (g >= 128) {
    const int whead = W_HEAD(), wcq = W_CQ();
    int h2o = WS_H2 + ((kg * 16 + wcq * 4 + wv) * 32 + whead) * 128 + n * 8;
    int oar = L_OA + (n >> 2) * 320 + kg * 16;
    unsigned tg2 = tag >> ((n & 1) * 16), tg1 = tag >> (kg * 8);
    if (wv < 4) {
      asm volatile("s_waitcnt vmcnt(0)" ::: "memory");
      lds_arrive(FC + F_WCNT, lane);
      lds_wait(FL + F_WCNT, 4u);
      unsigned wor[32];
#pragma unroll
      for (int j = 0; j < 32; ++j) wor[j] = *(const LAS unsigned*)(L + L_WO + ((j >> 3) * 32 + 8 * kg + (j & 7)) * 256 + wv * 64 + n * 4);
#pragma unroll
      for (int k = 0; k < 8; ++k) {
        const unsigned sel = (k & 1) ? 0x07060302u : 0x05040100u;
#pragma unroll
        for (int jp = 0; jp < 4; ++jp) gur[k][jp] = __builtin_amdgcn_perm(wor[8 * (k >> 1) + 2 * jp + 1], wor[8 * (k >> 1) + 2 * jp], sel);
        PIN(gur[k]);
      }
      PIN(h2o); PIN(oar); PIN(tg2); PIN(tg1);
      STAMP(5);
    } else {
      const int rb = wv - 4;
      if (__builtin_amdgcn_readfirstlane(FL[F_CS + rb]) == 0u) {
        const int off[2] = {ST_OFF(), ST_OFF() + 16};
        f32x4 v[2];
        sweep<2, 0>(rws, off, v, tag);
        const float c_ = split_weight(v[0], v[1], whead);
        if (lane < 8) *(LAS float*)(L + L_CS + rb * 32 + lane * 4) = c_;
        LGKM0();
      }
      float cs = *(const LAS float*)(L + L_CS + rb * 32 + (lane >> 3) * 4);
      int oaw = L_OA + rb * 320 + n * 8 + kg * 2;
      PIN(cs); PIN(oaw);
      const int ho = WS_H1 + ((rb * 8 + (whead >> 2)) * 8 + (lane >> 3)) * 2048 + (whead & 3) * 512 + (lane & 7) * 16;
      const int off[4] = {ho, ho + 128, ho + 256, ho + 384};
      f32x4 v[4];
      sweep<4, 0>(rws, off, v, tag);
      const float o0 = part_sum(v[0] * cs), o1 = part_sum(v[1] * cs), o2 = part_sum(v[2] * cs), o3 = part_sum(v[3] * cs);
      const unsigned pk = pack2((lane & 8) ? o1 : o0, (lane & 8) ? o3 : o2);
      *(LAS unsigned short*)(L + oaw) = (unsigned short)pk;
      *(LAS unsigned short*)(L + oaw + 128) = (unsigned short)(pk >> 16);
    }
    wg_barrier();
    STAMP(11);
    if (wv < 4) {
      f32x4 c0 = {0.f, 0.f, 0.f, 0.f}, c1 = {0.f, 0.f, 0.f, 0.f};
#pragma unroll
      for (int k = 0; k < 4; ++k) { const v4u a = *(const LAS v4u*)(L + oar + k * 64); c0 = mfma32(a, gur[2 * k], c0); c1 = mfma32(a, gur[2 * k + 1], c1); }
      __builtin_amdgcn_raw_buffer_store_b64(v2u{put1(c0[0], tg2), put1(c1[0], tg2 >> 8)}, rws, h2o, 0, AUX_SC1);
    }
    SB();
    STAMP(12);
    if (g >= 192) {
      const int rr = g - 192, col = 4 * (lane & 7) + kg;
      float x1 = 0.f, gb = 0.f;
      if (wv == 0) {
        float ga = *(const LAS float*)(L + L_GG + col * 4), xin = *(const LAS float*)(L + L_GG + 256 + col * 4);
        gb = *(const LAS float*)(L + L_GG + 128 + col * 4);
        int x1o = WS_X1 + (n >> 3) * 8192 + rr * 128 + col * 4;
        PIN(ga); PIN(xin); PIN(x1o); PIN(gb);
        const int ho = WS_H2 + rr * 4096 + lane * 16;
        const int off[4] = {ho, ho + 1024, ho + 2048, ho + 3072};
        f32x4 v[4];
        sweep<4, 0>(rws, off, v, tag);
        STAMP(13);
        x1 = rbf(xin + rbf(ga * rbf(part_sum((v[0] + v[1]) + (v[2] + v[3])))));
        const unsigned xw = put1(x1, tg1);
        __builtin_amdgcn_raw_buffer_store_b32(xw, rws, x1o, 0, AUX_SC1);
        if (XREP > 2) __builtin_amdgcn_raw_buffer_store_b32(xw, rws, x1o, 16384, AUX_SC1);
        SB();
      }
      STAMP(14);
      if (wv == 7) {
        const int off[1] = {WS_X1 + (g & (XREP - 1)) * 8192 + lane * 128};
        f32x4 v[1];
        sweep<1, 0>(rws, off, v, tag);
        STAMP(13);
        __builtin_amdgcn_s_sleep(DLY_H4 > 0 ? DLY_H4 : 0);
        FL[F_H4] = 1u;
      } else {
        const int off[2] = {WS_H4 + rr * 14336 + wv * 2048 + lane * 16, WS_H4 + rr * 14336 + wv * 2048 + 1024 + lane * 16};
        f32x4 v[2];
        if (DLY_H4 >= 0) lds_wait(FL + F_H4);
        sweep<2, 0>(rws, off, v, tag);
        *(LAS float*)(L + L_RED + wv * 128 + col * 4) = part_sum(v[0] + v[1]);
      }
      wg_barrier();
      if (wv == 0) {
        const LAS float* rp = (const LAS float*)(L + L_RED + col * 4);
        const float a = ((rp[0] + rp[32]) + (rp[64] + rp[96])) + ((rp[128] + rp[160]) + rp[192]);
        const float o = x1 + rbf(gb * rbf(a));
        if (n < 8) __builtin_amdgcn_raw_buffer_store_b16((unsigned short)pack2(o, 0.f), mkrsrc(out, 4096), (rr * 32 + col) * 2, 0, AUX_SC1);
        if (g == 192 && lane == 0) __builtin_amdgcn_raw_buffer_store_b32(tag, rws, WS_EPOCH, 0, AUX_SC1);
      }
      STAMP(15);
    }
  } else {
    const int ru = ((g >> 5) << 3) + (g & 7), jsl = 4 * ru + ((g >> 3) & 3);
    if (ru < 28) {
      __builtin_amdgcn_s_barrier();
      v2u dnr[8];
      if (wv < 4) {
        const bf16_t* gup = GU_PTR();
#pragma unroll
        for (int j = GU_A + GU_B; j < 8; ++j) GU_LD(j);
      }
      {
        const rsrc_t rd = mkrsrc(wd + (size_t)(16 * jsl) * 512, 16384);
        const int dofs = (kg & 1) * 8192 + wv * 128 + n * 8;
#pragma unroll
        for (int j = 0; j < 8; ++j) dnr[j] = __builtin_amdgcn_raw_buffer_load_b64(rd, dofs, j * 1024, AUX_NT);
      }
      if (wv < 4) {
        LAS unsigned char* fb = L + L_FB + ((((n & 3) * 8 * 4 + wv) * 4 + (n >> 2)) * 4 + kg) * 16;
#define TG(E) *(LAS v4u*)(fb + E * 1024) = tcol8<E>(gur);
        FOR8(TG)
#undef TG
      }
      wg_barrier();
      STAMP(12);
      v4u bg[4], bd[2];
#pragma unroll
      for (int t = 0; t < 4; ++t) { bg[t] = *(const LAS v4u*)(L + L_FB + ((((((n >> 1) & 1) * 16 + 2 * wv + (n & 1)) * 4 + t) * 4 + (n >> 2)) * 4 + kg) * 16); PIN(bg[t]); }
      const int rb = wv >> 1, hf = wv & 1;
      f32x4 shv = *(const LAS f32x4*)(L + L_SC + rb * 2048 + hf * 1024 + lane * 16), scv = *(const LAS f32x4*)(L + L_SC + 8192 + rb * 2048 + hf * 1024 + lane * 16);
      PIN(shv); PIN(scv);
      {
        const unsigned sel = (kg >> 1) ? 0x07060302u : 0x05040100u;
#pragma unroll
        for (int m2 = 0; m2 < 2; ++m2) {
#pragma unroll
          for (int jp = 0; jp < 4; ++jp) bd[m2][jp] = __builtin_amdgcn_perm(dnr[2 * jp + 1][m2], dnr[2 * jp][m2], sel);
          PIN(bd[m2]);
        }
      }
      int hbw = L_HB + rb * 1024 + (rb >> 1) * 32 + hf * 512 + lane * 8;
      int hbr = L_HB + (n >> 2) * 1024 + (n >> 3) * 32 + (32 * (n & 3) + 8 * kg) * 2;
      int acr = ((n & 1) == (kg >> 1)) ? L_ACT + (n >> 2) * 32 + (kg & 1) * 16 : L_ZERO;
      int h4o = WS_H4 + ((kg * 16 + 2 * wv + (n >> 3)) * 112 + jsl) * 128 + (n & 7) * 16;
      PIN(hbw); PIN(hbr); PIN(acr); PIN(h4o);
      SB();
      {
        const int xr = WS_X1 + (g & (XREP - 1)) * 8192 + rb * 2048 + lane * 16;
        const int off[2] = {xr + hf * 1024, xr + (hf ^ 1) * 1024};
        f32x4 v[2];
        sweep<2, 0>(rws, off, v, tag);
        STAMP(13);
        const float x0 = __uint_as_float(__builtin_amdgcn_readfirstlane(__float_as_uint(0.5f * (v[0][0] + v[1][0]))));
        const f32x4 d0 = v[0] - x0, d1 = v[1] - x0;
        const f32x4 sa = d0 + d1, sb2 = d0 * d0 + d1 * d1;
        float s1 = (sa[0] + sa[1]) + (sa[2] + sa[3]), s2 = (sb2[0] + sb2[1]) + (sb2[2] + sb2[3]);
        s1 = wave_sum(s1); s2 = wave_sum(s2);
        const float md = s1 * (1.f / 512.f), var = fmx(s2 * (1.f / 512.f) - md * md, 0.f);
        const float mb = rbf(x0 + md), rden = __builtin_amdgcn_rcpf(rbf(__builtin_amdgcn_sqrtf(rbf(rbf(var) + 1e-5f))));
        const f32x2 ha = rbf2(rbf2(rbf2(f32x2{v[0][0], v[0][1]} - mb) * rden) * f32x2{scv[0], scv[1]}) + f32x2{shv[0], shv[1]};
        const f32x2 hb = rbf2(rbf2(rbf2(f32x2{v[0][2], v[0][3]} - mb) * rden) * f32x2{scv[2], scv[3]}) + f32x2{shv[2], shv[3]};
        *(LAS v2u*)(L + hbw) = v2u{pack2(ha.x, ha.y), pack2(hb.x, hb.y)};
      }
      wg_barrier();
      {
        f32x4 c = {0.f, 0.f, 0.f, 0.f};
#pragma unroll
        for (int t = 0; t < 4; ++t) c = mfma32(*(const LAS v4u*)(L + hbr + t * 256), bg[t], c);
        float gs = sel4(lane & 4, lane & 8, c[0], c[1], c[2], c[3]);
        gs += dpp<ROR4>(gs); gs += dpp<ROR8>(gs);
        gs = rbf(gs);
        const float act = rbf(gelu_f(gs)) * dpp<XOR2>(gs);
        const unsigned pk = pack2(act, dpp<XOR1>(act));
        if (n == 0) *(LAS unsigned*)(L + L_ACT + kg * 32 + wv * 4) = pk;
      }
      wg_barrier();
      STAMP(14);
      {
        const v4u a = *(const LAS v4u*)(L + acr);
        const f32x4 c0 = mfma32(a, bd[0], f32x4{0.f, 0.f, 0.f, 0.f}), c1 = mfma32(a, bd[1], f32x4{0.f, 0.f, 0.f, 0.f});
        st16(rws, h4o, put4(f32x4{c0[0], c0[1], c1[0], c1[1]}, tag));
      }
      STAMP(15);
    }
  }
  STAMP_OUT();
}

extern "C" void kernel_launch(void* const* d_in, const int* in_sizes, int n_in, void* d_out, int out_size, void* d_ws, size_t ws_size, hipStream_t stream) {
  adaln_block_kernel<<<256, 512, 0, stream>>>((const bf16_t*)d_in[0], (const bf16_t*)d_in[1], (const bf16_t*)d_in[2], (const bf16_t*)d_in[3],
                                              (const bf16_t*)d_in[4], (const bf16_t*)d_in[6], (const bf16_t*)d_in[7], (const bf16_t*)d_in[8],
                                              (const bf16_t*)d_in[9], (const bf16_t*)d_in[10], (bf16_t*)d_out, (unsigned char*)d_ws);
}
```

```cpp
#include <hip/hip_runtime.h>
#include <hip/hip_bf16.h>
#include <stdint.h>

#define GAS __attribute__((address_space(1)))
#define LAS __attribute__((address_space(3)))
typedef unsigned short bf16_t;
typedef unsigned v4u __attribute__((ext_vector_type(4)));
typedef unsigned v2u __attribute__((ext_vector_type(2)));
typedef float f32x4 __attribute__((ext_vector_type(4)));
typedef float f32x2 __attribute__((ext_vector_type(2)));
typedef __bf16 bf16x8 __attribute__((ext_vector_type(8)));
typedef __bf16 bf16x2 __attribute__((ext_vector_type(2)));
typedef short s16x4 __attribute__((ext_vector_type(4)));
typedef __amdgpu_buffer_rsrc_t rsrc_t;

#define PLO 4
#define PHI 8
#define SCH_A 110
#define SCH_F 60
#define SCH_G (-55)
#define SCH_T16 250
#define T_MOD 380
#define T_MODF 190
#define T_Q 490
#define GU_B 1
#define DLY_H4 34
#define VE_AT 26
#define XREP 4
#define ABL 0

namespace {
constexpr int SK = 16384, WIN = 4096, FF = 1792;
constexpr int AUX_SC1 = 16, AUX_NT = 2;
constexpr int WS_EPOCH = 0;
constexpr int WS_MT = 2048;
constexpr int WS_X1 = 4096;
constexpr int WS_SB = WS_X1 + 32768;
constexpr int WS_CB = WS_SB + 8192;
constexpr int WS_ST = WS_CB + 8192;
constexpr int WS_MOD = WS_ST + 32768;
constexpr int WS_QP = WS_MOD + 196608;
constexpr int WS_H1 = WS_QP + 524288;
constexpr int WS_H2 = WS_H1 + 524288;
constexpr int WS_H4 = WS_H2 + 262144;
constexpr int WS_END = WS_H4 + 917504;
constexpr int WS_STAMP = 8 << 20;
constexpr int L_K = 0;
constexpr int L_FB = 0;
constexpr int L_WO = 0;
constexpr int L_SC = 32768;
constexpr int L_P = 131072;
constexpr int L_QB = L_P + 4096;
constexpr int L_HQ = L_QB + 1280;
constexpr int L_ZERO = L_HQ + 1024;
constexpr int L_FLAG = L_ZERO + 1024;
constexpr int L_GG = L_FLAG + 256;
constexpr int L_OA = L_GG + 384;
constexpr int L_CS = L_OA + 1280;
constexpr int L_F = L_CS + 128;
constexpr int L_ACT = L_F + 128;
constexpr int L_RED = L_ACT + 128;
constexpr int L_MRG = L_RED + 2048;
constexpr int L_HB = L_MRG + 8192;
constexpr int L_END = L_HB + 4096 + 64;
enum { F_MCNT = 0, F_HCNT = 1, F_QCNT = 2, F_K = 4, F_PCNT = 8, F_G = 10, F_H4 = 12, F_WCNT = 13, F_VI = 16, F_VE = 20, F_CS = 24, F_ANC = 28 };
constexpr unsigned SPIN_G = 1u << 16, SPIN_L = 1u << 20;

__device__ __forceinline__ float bflo(unsigned u) { return __uint_as_float(u << 16); }
__device__ __forceinline__ float bfhi(unsigned u) { return __uint_as_float(u & 0xFFFF0000u); }
__device__ __forceinline__ unsigned pack2(float lo, float hi) {
  f32x2 v = {lo, hi};
  return __builtin_bit_cast(unsigned, __builtin_convertvector(v, bf16x2));
}
__device__ __forceinline__ float rbf(float x) { return bflo(pack2(x, x) & 0xFFFFu); }
__device__ __forceinline__ float fmx(float a, float b) { return __builtin_amdgcn_fmed3f(a, b, __builtin_inff()); }
template <int CTRL> __device__ __forceinline__ float dpp(float x) {
  return __builtin_bit_cast(float, __builtin_amdgcn_mov_dpp(__builtin_bit_cast(int, x), CTRL, 0xf, 0xf, true));
}
constexpr int XOR1 = 0xB1, XOR2 = 0x4E, HMIR = 0x141, ROR2 = 0x122, ROR4 = 0x124, ROR8 = 0x128;
__device__ __forceinline__ float xrow_sum(float x) {
  auto s = __builtin_amdgcn_permlane16_swap(__float_as_uint(x), __float_as_uint(x), false, false);
  x = __uint_as_float(s[0]) + __uint_as_float(s[1]);
  auto t = __builtin_amdgcn_permlane32_swap(__float_as_uint(x), __float_as_uint(x), false, false);
  return __uint_as_float(t[0]) + __uint_as_float(t[1]);
}
__device__ __forceinline__ float xrow_max(float x) {
  auto s = __builtin_amdgcn_permlane16_swap(__float_as_uint(x), __float_as_uint(x), false, false);
  x = fmx(__uint_as_float(s[0]), __uint_as_float(s[1]));
  auto t = __builtin_amdgcn_permlane32_swap(__float_as_uint(x), __float_as_uint(x), false, false);
  return fmx(__uint_as_float(t[0]), __uint_as_float(t[1]));
}
__device__ __forceinline__ float wave_sum(float x) {
  x += dpp<XOR1>(x); x += dpp<XOR2>(x); x += dpp<ROR4>(x); x += dpp<ROR8>(x);
  return xrow_sum(x);
}
__device__ __forceinline__ f32x4 mfma32(v4u a, v4u b, f32x4 c) {
  return __builtin_amdgcn_mfma_f32_16x16x32_bf16(__builtin_bit_cast(bf16x8, a), __builtin_bit_cast(bf16x8, b), c, 0, 0, 0);
}
template <int E> __device__ __forceinline__ v4u tcol8(const v4u* r) {
  constexpr unsigned sel = (E & 1) ? 0x07060302u : 0x05040100u;
  v4u o;
  o.x = __builtin_amdgcn_perm(r[1][E >> 1], r[0][E >> 1], sel);
  o.y = __builtin_amdgcn_perm(r[3][E >> 1], r[2][E >> 1], sel);
  o.z = __builtin_amdgcn_perm(r[5][E >> 1], r[4][E >> 1], sel);
  o.w = __builtin_amdgcn_perm(r[7][E >> 1], r[6][E >> 1], sel);
  return o;
}
__device__ __forceinline__ v4u tcol8r(const v4u* r, int e) {
  const unsigned sel = (e & 1) ? 0x07060302u : 0x05040100u;
  v4u o;
  o.x = __builtin_amdgcn_perm(r[1][e >> 1], r[0][e >> 1], sel);
  o.y = __builtin_amdgcn_perm(r[3][e >> 1], r[2][e >> 1], sel);
  o.z = __builtin_amdgcn_perm(r[5][e >> 1], r[4][e >> 1], sel);
  o.w = __builtin_amdgcn_perm(r[7][e >> 1], r[6][e >> 1], sel);
  return o;
}
#define FOR8(X) X(0) X(1) X(2) X(3) X(4) X(5) X(6) X(7)
__device__ __forceinline__ rsrc_t mkrsrc(const void* p, int bytes) { return __builtin_amdgcn_make_buffer_rsrc((void*)p, 0, bytes, 0x00020000); }
template <int AUX> __device__ __forceinline__ v4u bld(rsrc_t r, int voff, int soff) { return __builtin_amdgcn_raw_buffer_load_b128(r, voff, soff, AUX); }
__device__ __forceinline__ unsigned put1(float v, unsigned t) { return (__float_as_uint(v) & ~15u) | (t & 15u); }
__device__ __forceinline__ v4u put4(f32x4 v, unsigned tag) { return v4u{put1(v.x, tag), put1(v.y, tag >> 8), put1(v.z, tag >> 16), put1(v.w, tag >> 24)}; }
__device__ __forceinline__ unsigned tagdiff(v4u v, unsigned tag) {
  return (__builtin_amdgcn_perm(v.y, v.x, 0x0c0c0400u) | __builtin_amdgcn_perm(v.w, v.z, 0x04000c0cu)) ^ tag;
}
__device__ __forceinline__ bool all_ok(unsigned diff) { return __all((diff & 0x0F0F0F0Fu) == 0u); }
__device__ __forceinline__ f32x4 get4(v4u v) { return __builtin_bit_cast(f32x4, v & ~15u); }
__device__ __forceinline__ void st16(rsrc_t r, int off, v4u v) { __builtin_amdgcn_raw_buffer_store_b128(v, r, off, 0, AUX_SC1); }
#define CBAR() asm volatile("" ::: "memory")
#define LGKM0() asm volatile("s_waitcnt lgkmcnt(0)" ::: "memory")
#define SB() __builtin_amdgcn_sched_barrier(0)
#define PIN(x) asm volatile("" : "+v"(x))
__device__ __forceinline__ void lds_wait(volatile LAS unsigned* f, unsigned want = 1u) {
#pragma unroll 1
  for (unsigned i = 0; i < SPIN_L; ++i) { if (*f >= want) break; __builtin_amdgcn_s_sleep(1); }
  CBAR();
}
__device__ __forceinline__ void lds_set(volatile LAS unsigned* f) { LGKM0(); *f = 1u; }
__device__ __forceinline__ void lds_arrive(LAS unsigned* f, int lane) {
  LGKM0();
  if (lane == 0) __hip_atomic_fetch_add(f, 1u, __ATOMIC_RELAXED, __HIP_MEMORY_SCOPE_WORKGROUP);
}
__device__ __forceinline__ void wg_barrier() { LGKM0(); __builtin_amdgcn_s_barrier(); CBAR(); }
template <int N, int SLEEP> __device__ __forceinline__ void sweep(rsrc_t rs, const int (&off)[N], f32x4 (&o)[N], unsigned tag) {
  v4u v[N];
#pragma unroll 1
  for (unsigned spin = 0;; ++spin) {
    unsigned bad = 0;
#pragma unroll
    for (int i = 0; i < N; ++i) v[i] = bld<AUX_SC1>(rs, off[i], 0);
#pragma unroll
    for (int i = 0; i < N; ++i) bad |= tagdiff(v[i], tag);
    if (all_ok(bad) || spin >= SPIN_G) break;
    if (SLEEP) __builtin_amdgcn_s_sleep(SLEEP);
    CBAR();
  }
#pragma unroll
  for (int i = 0; i < N; ++i) o[i] = get4(v[i]);
}
__device__ __forceinline__ float fexp(float x) { return __builtin_amdgcn_exp2f(x * 1.4426950408889634f); }
__device__ __forceinline__ float silu_f(float x) { return x * __builtin_amdgcn_rcpf(1.f + fexp(-x)); }
__device__ __forceinline__ float gelu_f(float x) {
  const float t = x * fmaf(x * x, -0.10294324f, -2.3022082f);
  return x * __builtin_amdgcn_rcpf(1.f + __builtin_amdgcn_exp2f(t));
}
__device__ __forceinline__ void tcol4x8(const v2u* r, v4u* o) {
#pragma unroll
  for (int e = 0; e < 4; ++e) {
    const unsigned sel = (e & 1) ? 0x07060302u : 0x05040100u;
#pragma unroll
    for (int jp = 0; jp < 4; ++jp) o[e][jp] = __builtin_amdgcn_perm(r[2 * jp + 1][e >> 1], r[2 * jp][e >> 1], sel);
    PIN(o[e]);
  }
}
__device__ __forceinline__ f32x4 gemv32x64(v4u a, const v4u* bt) {
  f32x4 c[4];
#pragma unroll
  for (int e = 0; e < 4; ++e) c[e] = mfma32(a, bt[e], f32x4{0.f, 0.f, 0.f, 0.f});
  return f32x4{c[0][0], c[1][0], c[2][0], c[3][0]};
}
__device__ __forceinline__ void wait_until(unsigned long long t0, unsigned ticks) {
#pragma unroll 1
  for (int i = 0; i < 4096 && __builtin_amdgcn_s_memrealtime() - t0 < ticks; ++i) __builtin_amdgcn_s_sleep(1);
}
__device__ __forceinline__ float sel4(bool b0, bool b1, float c0, float c1, float c2, float c3) {
  const float lo = b0 ? c1 : c0, hi = b0 ? c3 : c2;
  return b1 ? hi : lo;
}
__device__ __forceinline__ float part_sum(f32x4 v) {
  auto p = __builtin_amdgcn_permlane32_swap(__float_as_uint(v[0]), __float_as_uint(v[2]), false, false);
  auto q = __builtin_amdgcn_permlane32_swap(__float_as_uint(v[1]), __float_as_uint(v[3]), false, false);
  const float A = __uint_as_float(p[0]) + __uint_as_float(p[1]), B = __uint_as_float(q[0]) + __uint_as_float(q[1]);
  auto r = __builtin_amdgcn_permlane16_swap(__float_as_uint(A), __float_as_uint(B), false, false);
  const float t = __uint_as_float(r[0]) + __uint_as_float(r[1]);
  return t + dpp<ROR8>(t);
}
__device__ __forceinline__ float split_weight(f32x4 m4, f32x4 l4, int hh) {
  const bool h0 = hh & 1, h1 = hh & 2;
  const float m = sel4(h0, h1, m4[0], m4[1], m4[2], m4[3]), l = sel4(h0, h1, l4[0], l4[1], l4[2], l4[3]);
  float M = fmx(m, dpp<XOR1>(m)); M = fmx(M, dpp<XOR2>(M)); M = fmx(M, dpp<HMIR>(M));
  const float e = fexp(m - M);
  float Ls = l * e; Ls += dpp<XOR1>(Ls); Ls += dpp<XOR2>(Ls); Ls += dpp<HMIR>(Ls);
  return e * __builtin_amdgcn_rcpf(Ls);
}
__device__ __forceinline__ f32x2 rbf2(f32x2 v) {
  const unsigned p = pack2(v.x, v.y);
  return f32x2{bflo(p), bfhi(p)};
}
template <bool MAX> __device__ __forceinline__ float red2(float a, float b) { return MAX ? fmx(a, b) : a + b; }
template <bool MAX> __device__ __forceinline__ f32x4 wave_red4(f32x4 s, int lane) {
  const bool o1 = lane & 1, o2 = lane & 2;
  const float a = red2<MAX>(o1 ? s[1] : s[0], dpp<XOR1>(o1 ? s[0] : s[1])), c = red2<MAX>(o1 ? s[3] : s[2], dpp<XOR1>(o1 ? s[2] : s[3]));
  float m = red2<MAX>(o2 ? c : a, dpp<XOR2>(o2 ? a : c));
  m = red2<MAX>(m, dpp<ROR4>(m)); m = red2<MAX>(m, dpp<ROR8>(m));
  m = MAX ? xrow_max(m) : xrow_sum(m);
  return f32x4{dpp<0x00>(m), dpp<0x55>(m), dpp<0xAA>(m), dpp<0xFF>(m)};
}
#define STAMP(i) do {} while (0)
#define STAMP_OUT() do {} while (0)
}

__global__ void __launch_bounds__(512, 2)
adaln_block_kernel(const bf16_t* __restrict__ x, const bf16_t* __restrict__ kc, const bf16_t* __restrict__ vc,
                   const bf16_t* __restrict__ wq, const bf16_t* __restrict__ wo, const bf16_t* __restrict__ wg,
                   const bf16_t* __restrict__ wu, const bf16_t* __restrict__ wd, const bf16_t* __restrict__ cc,
                   const bf16_t* __restrict__ wmod, bf16_t* __restrict__ out, unsigned char* ws) {
  __shared__ __attribute__((aligned(16))) unsigned char lds_raw[L_END];
  LAS unsigned char* L = (LAS unsigned char*)lds_raw;
  volatile LAS unsigned* FL = (volatile LAS unsigned*)(L + L_FLAG);
  LAS unsigned* FC = (LAS unsigned*)(L + L_FLAG);
  const unsigned long long tstart = __builtin_amdgcn_s_memrealtime();
  const int tid = threadIdx.x, lane = tid & 63, wv = __builtin_amdgcn_readfirstlane(tid >> 6);
  const int g = blockIdx.x, grp = (g & 7) * 4 + (g >> 6), b = grp >> 3, kvh = grp & 7, s = (g >> 3) & 7;
  const int n = lane & 15, kg = lane >> 4;
  if (tid < 320) *(LAS unsigned*)(L + L_ZERO + tid * 4) = 0u;
  asm volatile("" :: "s"(x), "s"(kc), "s"(vc), "s"(wq), "s"(cc), "s"(wmod), "s"(ws));
  wg_barrier();
  const rsrc_t rws = mkrsrc(ws, WS_END);
  unsigned tag;
  v4u gur[8];

  if (__builtin_expect(wv >= 4, 1)) {
    const int w = wv - 4;
    const size_t kvbase = ((size_t)grp * SK + (SK - WIN) + s * 512) * 128;
    const int rot = (g * 37) & 127;
    const unsigned char* kbase = (const unsigned char*)(kc + kvbase);
    const unsigned ldsk = (unsigned)(uintptr_t)(L + L_K);
    const int klane = kg * 256 + ((n ^ kg) << 4);
    unsigned due0 = (unsigned)tstart + SCH_A;
#define PACE_WAIT(now, i) do { \
      asm volatile("s_waitcnt vmcnt(%0)" :: "i"(PHI) : "memory"); \
      if (PHI > PLO && (int)((now) - due0) < (((i) * SCH_T16) >> 4)) asm volatile("s_waitcnt vmcnt(%0)" :: "i"(PLO) : "memory"); } while (0)
    STAMP(7);
#pragma unroll 4
    for (int i = 0; i < 32; ++i) {
      const int phys = (4 * i + w + rot) & 127;
      const unsigned char* sb_ = kbase + phys * 1024;
      const int vo = klane ^ ((i & 3) << 6);
      const unsigned dst = ldsk + (4 * i + w) * 1024;
      unsigned keep;
      asm volatile("s_mov_b32 %0, m0\n\ts_mov_b32 m0, %3\n\ts_nop 0\n\tglobal_load_lds_dwordx4 %1, %2 nt\n\ts_mov_b32 m0, %0"
                   : "=&s"(keep) : "v"(vo), "s"(sb_), "s"(dst) : "memory");
      const unsigned now = (unsigned)__builtin_amdgcn_s_memrealtime();
      if (i == PLO + 1 && (int)(now - SCH_F - due0) > 0) due0 = now - SCH_F;
      PACE_WAIT(now, i);
    }
    STAMP(1);
    { const unsigned anc = __builtin_amdgcn_readfirstlane(FL[F_ANC]); if (anc) due0 = anc + SCH_G; }
    const unsigned epoch = __builtin_amdgcn_raw_buffer_load_b32(rws, WS_EPOCH, 0, AUX_SC1);
    const rsrc_t rv = mkrsrc(vc + kvbase, 131072);
    v4u V[32], VT[24];
    constexpr int TR0 = 8 + PHI;
#pragma unroll
    for (int i = 0; i < 32; ++i) {
      V[i] = bld<AUX_NT>(rv, lane * 16, ((4 * i + w + rot) & 127) * 1024);
      const unsigned now = (unsigned)__builtin_amdgcn_s_memrealtime();
      if (i >= TR0) { const int k = i - TR0; VT[k] = tcol8r(V + 8 * (k >> 3), k & 7); PIN(VT[k]); }
      if (i == PLO - 1) { asm volatile("s_waitcnt vmcnt(%0)" :: "i"(PLO) : "memory"); FL[F_K + w] = 1u; STAMP(2); }
      else if (i < 31) PACE_WAIT(now, 32 + i);
      if (i == VE_AT) FL[F_VE + w] = 1u;
      SB();
    }
    FL[F_VI + w] = 1u;
    tag = __builtin_amdgcn_readfirstlane(epoch) + 0x01010101u;
    STAMP(3);
#pragma unroll
    for (int k = 32 - TR0; k < 24; ++k) { VT[k] = tcol8r(V + 8 * (k >> 3), k & 7); PIN(VT[k]); }
    lds_wait(FL + F_PCNT, 4u);
    STAMP(4);
    f32x4 f;
    {
      const LAS f32x4* sp = (const LAS f32x4*)(L + L_F);
      const f32x4 m0 = sp[0], m1 = sp[2], m2 = sp[4], m3 = sp[6], mo = sp[2 * w];
#pragma unroll
      for (int h = 0; h < 4; ++h) f[h] = fexp(mo[h] - fmx(fmx(m0[h], m1[h]), fmx(m2[h], m3[h])));
    }
    f32x4 acc[2] = {f32x4{0.f, 0.f, 0.f, 0.f}, f32x4{0.f, 0.f, 0.f, 0.f}};
    int paddr[4];
#pragma unroll
    for (int ep = 0; ep < 4; ++ep) paddr[ep] = (n >> 2) == ep ? L_P + w * 1024 + (((n & 3) * 4 + kg) << 4) : L_ZERO;
    int mra = L_MRG + (w * 512 + 8 * n + 2 * kg) * 4;
#pragma unroll
    for (int G = 0; G < 4; ++G) {
      v4u pa[4];
#pragma unroll
      for (int ep = 0; ep < 4; ++ep) pa[ep] = *(const LAS v4u*)(L + paddr[ep] + G * 256);
      if (G == 3) { PIN(pa[0]); PIN(pa[1]); PIN(pa[2]); PIN(pa[3]); PIN(f); PIN(mra); PIN(acc[0]); PIN(acc[1]); STAMP(5); PIN(V[24]); SB(); }
#define PV(E) acc[E & 1] = mfma32(pa[E >> 1], G < 3 ? VT[8 * G + E] : tcol8<E>(V + 24), acc[E & 1]);
      FOR8(PV)
#undef PV
      SB();
    }
    STAMP(6);
#pragma unroll
    for (int h = 0; h < 4; ++h) *(LAS f32x2*)(L + mra + h * 512) = f32x2{acc[0][h], acc[1][h]} * f[h];
  } else if (ABL == 6) {
    lds_arrive(FC + F_PCNT, lane); tag = 0;
  } else {
    const int cw = wv;
    __builtin_amdgcn_s_setprio(2);
    const int ks = g >> 5, ns = g & 31, u5 = lane & 31, h5 = lane >> 5;
    const int mkq = g < 64 ? g >> 4 : (g - 64) >> 5, mcb = g < 64 ? g & 15 : 16 + ((g - 64) & 31);
    v2u wm[8], wr[8];
    const rsrc_t rm = mkrsrc(wmod + (size_t)(mkq * 128 + cw * 32) * 3072, g < 192 ? 32 * 6144 : 0);
    const rsrc_t rq = mkrsrc(wq + (size_t)(ks * 64 + (cw & 1) * 32) * 4096, 32 * 8192);
    const v4u cf = *(const v4u*)(cc + (n >> 2) * 512 + ((128 * mkq) & 511) + 32 * cw + 8 * kg);
#pragma unroll
    for (int j = 0; j < 8; ++j) wm[j] = __builtin_amdgcn_raw_buffer_load_b64(rm, 8 * kg * 6144 + mcb * 128 + n * 8, j * 6144, AUX_NT);
    const unsigned epoch = __builtin_amdgcn_raw_buffer_load_b32(rws, WS_EPOCH, 0, AUX_SC1);
    const v4u xs = *(const v4u*)(x + cw * 512 + lane * 8);
    const v2u xo = *(const v2u*)(x + cw * 512 + 64 * ks + 4 * n);
#pragma unroll
    for (int j = 0; j < 8; ++j) wr[j] = __builtin_amdgcn_raw_buffer_load_b64(rq, 8 * kg * 8192 + ns * 256 + (cw >> 1) * 128 + n * 8, j * 8192, AUX_NT);
    SB();
    STAMP(1);
    v4u bq[4];
    unsigned long long tfirst;
    {
      v4u a, bt[4];
#pragma unroll
      for (int i = 0; i < 4; ++i) a[i] = pack2(silu_f(bflo(cf[i])), silu_f(bfhi(cf[i])));
      PIN(a);
      tcol4x8(wm, bt);
      tfirst = __builtin_amdgcn_s_memrealtime();
      *(LAS f32x4*)(L + L_MRG + cw * 1024 + lane * 16) = gemv32x64(a, bt);
      lds_arrive(FC + F_MCNT, lane);
      tag = __builtin_amdgcn_readfirstlane(epoch) + 0x01010101u;
      if (cw == 0 && g < 192) {
        lds_wait(FL + F_MCNT, 4u);
        const LAS f32x4* mp = (const LAS f32x4*)(L + L_MRG + lane * 16);
        st16(rws, WS_MOD + ((mkq * 4 + kg) * 3072 + 64 * mcb + 4 * n) * 4, put4((mp[0] + mp[64]) + (mp[128] + mp[192]), tag));
        if (g < 64 && lane == 0) { const float t = __uint_as_float((unsigned)__builtin_amdgcn_s_memrealtime() << 4); st16(rws, WS_MT + g * 16, put4(f32x4{t, t, t, t}, tag)); }
        STAMP(11);
      }
    }
    float ln[4];
    {
      float sum = 0.f;
#pragma unroll
      for (int i = 0; i < 4; ++i) sum += bflo(xs[i]) + bfhi(xs[i]);
      const float mean = wave_sum(sum) * (1.f / 512.f);
      float sq = 0.f;
#pragma unroll
      for (int i = 0; i < 4; ++i) { const float d0 = bflo(xs[i]) - mean, d1 = bfhi(xs[i]) - mean; sq = fmaf(d0, d0, sq); sq = fmaf(d1, d1, sq); }
      sq = wave_sum(sq);
      const float mb = rbf(mean), den = rbf(sqrtf(rbf(rbf(sq * (1.f / 512.f)) + 1e-5f)));
#pragma unroll
      for (int i = 0; i < 2; ++i) { ln[2 * i] = rbf(rbf(bflo(xo[i]) - mb) / den); ln[2 * i + 1] = rbf(rbf(bfhi(xo[i]) - mb) / den); }
    }
    tcol4x8(wr, bq);
    STAMP(2);
    wait_until(tstart, T_MOD);
    wait_until(tfirst, T_MODF);
    {
      const int mo = WS_MOD + ((h5 * 4 + cw) * 3072 + (kg & 1) * 512 + 64 * ks + 4 * n) * 4;
      const int off[2] = {mo, mo + 2 * 4 * 3072 * 4};
      f32x4 v[2];
      v4u mt;
      if (cw == 3) mt = bld<AUX_SC1>(rws, WS_MT + lane * 16, 0);
      sweep<2, 8>(rws, off, v, tag);
      STAMP(3);
      if (cw == 3) {
        const unsigned now = (unsigned)__builtin_amdgcn_s_memrealtime();
        const int d = (int)(mt.x - (now << 4)) >> 4;
        const bool ok = (tagdiff(mt, tag) & 0x0F0F0F0Fu) == 0u && d <= 0 && d > -2000;
        const float cnt = wave_sum(ok ? 1.f : 0.f), sum = wave_sum(ok ? (float)d : 0.f);
        FL[F_ANC] = (now + (cnt > 0.f ? (int)(sum / cnt) : -300)) | 1u;
      }
      const f32x4 t = v[0] + v[1];
      float h[4];
#pragma unroll
      for (int e = 0; e < 4; ++e) {
        auto p = __builtin_amdgcn_permlane32_swap(__float_as_uint(t[e]), __float_as_uint(t[e]), false, false);
        const float u = rbf(__uint_as_float(p[0]) + __uint_as_float(p[1]));
        auto r = __builtin_amdgcn_permlane16_swap(__float_as_uint(u), __float_as_uint(u), false, false);
        h[e] = rbf(ln[e] * rbf(1.f + __uint_as_float(r[1]))) + __uint_as_float(r[0]);
      }
      if (lane < 16) *(LAS v2u*)(L + L_HQ + cw * 128 + n * 8) = v2u{pack2(h[0], h[1]), pack2(h[2], h[3])};
      lds_arrive(FC + F_HCNT, lane);
      lds_wait(FL + F_HCNT, 4u);
      const v4u a = *(const LAS v4u*)(L + L_HQ + (n >> 2) * 128 + (cw & 1) * 64 + kg * 16);
      const f32x4 qp = gemv32x64(a, bq);
      if (cw & 1) {
        *(LAS f32x4*)(L + L_MRG + 4096 + (cw >> 1) * 1024 + lane * 16) = qp;
        lds_set(FL + F_G + (cw >> 1));
      } else {
        lds_wait(FL + F_G + (cw >> 1));
        st16(rws, WS_QP + ((kg * 8 + ks) * 4096 + 128 * ns + 64 * (cw >> 1) + 4 * n) * 4, put4(qp + *(const LAS f32x4*)(L + L_MRG + 4096 + (cw >> 1) * 1024 + lane * 16), tag));
      }
    }
    STAMP(4);
    if (cw == 3 && g >= 192) {
      const int rr = g - 192;
      const int mo = WS_MOD + ((((lane >> 3) & 3) * 4 + (rr >> 4)) * 3072 + 1024 + h5 * 512 + 32 * (rr & 15) + 4 * (lane & 7)) * 4;
      const int off[2] = {mo, mo + 4096};
      f32x4 v[2];
      const unsigned short xin = x[rr * 32 + u5];
      sweep<2, 8>(rws, off, v, tag);
#pragma unroll
      for (int i = 0; i < 2; ++i)
#pragma unroll
        for (int e = 0; e < 4; ++e) {
          float u = v[i][e]; u += dpp<ROR8>(u);
          auto p = __builtin_amdgcn_permlane16_swap(__float_as_uint(u), __float_as_uint(u), false, false);
          v[i][e] = rbf(__uint_as_float(p[0]) + __uint_as_float(p[1]));
        }
      *(LAS float*)(L + L_GG + 256 + u5 * 4) = bflo(xin);
      if (u5 < 8) {
        if (lane < 32) {
          *(LAS f32x4*)(L + L_GG + lane * 16) = v[0];
          st16(rws, WS_CB + rr * 128 + lane * 16, put4(f32x4{rbf(1.f + v[1][0]), rbf(1.f + v[1][1]), rbf(1.f + v[1][2]), rbf(1.f + v[1][3])}, tag));
        } else {
          *(LAS f32x4*)(L + L_GG + 128 + u5 * 16) = v[1];
          st16(rws, WS_SB + rr * 128 + u5 * 16, put4(v[0], tag));
        }
      }
    }
    const int qo = WS_QP + ((b * 8 + h5) * 4096 + kvh * 512 + cw * 128 + 4 * u5) * 4;
    {
      const unsigned tq = __builtin_amdgcn_readfirstlane(FL[F_ANC]) + T_Q;
#pragma unroll 1
      for (int i = 0; i < 4096 && (int)((unsigned)__builtin_amdgcn_s_memrealtime() - tq) < 0; ++i) __builtin_amdgcn_s_sleep(1);
    }
    v4u qv[4];
#pragma unroll
    for (int i = 0; i < 4; ++i) qv[i] = bld<AUX_SC1>(rws, qo, i * 32768);
    SB();
    lds_wait(FL + F_K + cw);
    STAMP(5);
    v4u kf[32];
#pragma unroll
    for (int t = 0; t < 8; ++t)
#pragma unroll
      for (int ds = 0; ds < 4; ++ds)
        kf[4 * t + ds] = *(const LAS v4u*)(L + L_K + (16 * (4 * t + (n >> 2)) + 4 * cw + (n & 3)) * 256 + (((4 * ds + kg) ^ n) << 4));
    SB();
    {
#pragma unroll 1
      for (unsigned spin = 0; spin < SPIN_G; ++spin) {
        if (all_ok(tagdiff(qv[0], tag) | tagdiff(qv[1], tag) | tagdiff(qv[2], tag) | tagdiff(qv[3], tag))) break;
        __builtin_amdgcn_s_sleep(8);
#pragma unroll
        for (int i = 0; i < 4; ++i) if (!all_ok(tagdiff(qv[i], tag))) qv[i] = bld<AUX_SC1>(rws, qo, i * 32768);
      }
      const f32x4 t = (get4(qv[0]) + get4(qv[1])) + (get4(qv[2]) + get4(qv[3]));
      float q[4];
#pragma unroll
      for (int e = 0; e < 4; ++e) {
        auto p = __builtin_amdgcn_permlane32_swap(__float_as_uint(t[e]), __float_as_uint(t[e]), false, false);
        q[e] = __uint_as_float(p[0]) + __uint_as_float(p[1]);
      }
      if (lane < 32) *(LAS v2u*)(L + L_QB + cw * 320 + u5 * 8) = v2u{pack2(q[0], q[1]), pack2(q[2], q[3])};
      lds_arrive(FC + F_QCNT, lane);
      lds_wait(FL + F_QCNT, 4u);
    }
    STAMP(6);
    {
      f32x4 sc[2];
      v4u qf[4];
#pragma unroll
      for (int ds = 0; ds < 4; ++ds) qf[ds] = *(const LAS v4u*)(L + L_QB + (n & 3) * 320 + kg * 16 + ds * 64);
      f32x4 c[8];
#pragma unroll
      for (int t = 0; t < 8; ++t) {
        c[t] = f32x4{0.f, 0.f, 0.f, 0.f};
#pragma unroll
        for (int ds = 0; ds < 4; ++ds) c[t] = mfma32(qf[ds], kf[4 * t + ds], c[t]);
      }
#pragma unroll
      for (int sg = 0; sg < 2; ++sg)
#pragma unroll
        for (int h = 0; h < 4; ++h) sc[sg][h] = sel4(lane & 16, lane & 32, c[4 * sg][h], c[4 * sg + 1][h], c[4 * sg + 2][h], c[4 * sg + 3][h]);
      f32x4 e0, e1, mx;
#pragma unroll
      for (int h = 0; h < 4; ++h) {
        const f32x2 r = rbf2(rbf2(f32x2{sc[0][h], sc[1][h]}) * 0.08838834764831845f);
        e0[h] = r.x; e1[h] = r.y; mx[h] = fmx(r.x, r.y);
      }
      const f32x4 mw = wave_red4<true>(mx, lane);
#pragma unroll
      for (int h = 0; h < 4; ++h) {
        e0[h] = fexp(e0[h] - mw[h]); e1[h] = fexp(e1[h] - mw[h]);
        const unsigned pp = pack2(e0[h], e1[h]);
        LAS unsigned short* pd = (LAS unsigned short*)(L + L_P) + ((((cw * 4 + (kg >> 1)) * 4 + h) * 4 + (n & 3)) * 8 + 4 * (kg & 1) + (n >> 2));
        pd[0] = (unsigned short)(pp & 0xFFFFu);
        pd[2 * 128] = (unsigned short)(pp >> 16);
      }
      const f32x4 lw = wave_red4<false>(e0 + e1, lane);
      if (lane < 2) *(LAS f32x4*)(L + L_F + cw * 32 + lane * 16) = lane == 0 ? mw : lw;
      lds_arrive(FC + F_PCNT, lane);
    }
    STAMP(7);
    lds_wait(FL + F_PCNT, 4u);
    if (cw == 3) {
      const LAS f32x4* sp = (const LAS f32x4*)(L + L_F);
      f32x4 M, Ls;
#pragma unroll
      for (int h = 0; h < 4; ++h) {
        M[h] = fmx(fmx(sp[0][h], sp[2][h]), fmx(sp[4][h], sp[6][h]));
        Ls[h] = (sp[1][h] * fexp(sp[0][h] - M[h]) + sp[3][h] * fexp(sp[2][h] - M[h])) + (sp[5][h] * fexp(sp[4][h] - M[h]) + sp[7][h] * fexp(sp[6][h] - M[h]));
      }
      if (lane < 2) st16(rws, WS_ST + (grp * 8 + s) * 128 + lane * 16, put4(lane == 0 ? M : Ls, tag));
    }
#define GU_PTR() ((n & 2) ? wu : wg) + (size_t)(128 * wv + 32 * (n >> 2) + 8 * kg) * FF + 16 * jsl + 8 * (n & 1)
#define GU_RANGE(P0, P1) { _Pragma("unroll") for (int j = P0; j < P1; ++j) gur[j] = *(const v4u*)(gup + j * FF); }
#define W_HEAD() ((g & 7) + 8 * ((g - 128) >> 5))
#define W_CQ() ((g >> 3) & 3)
#define ST_OFF() (WS_ST + (((wv & 3) * 8 + (W_HEAD() >> 2)) * 8 + (lane & 7)) * 128)
    const int ru = ((g >> 5) << 3) + (g & 7), jsl = 4 * ru + ((g >> 3) & 3);
    if (g < 128) {
      if (ru < 28) {
        const bf16_t* gup = GU_PTR();
        const int mo = WS_SB + cw * 2048 + lane * 16;
        const int off[4] = {mo, mo + 1024, mo + 8192, mo + 8192 + 1024};
        f32x4 pre[4];
        sweep<4, 8>(rws, off, pre, tag);
#pragma unroll
        for (int i = 0; i < 4; ++i) *(LAS f32x4*)(L + L_SC + (i >> 1) * 8192 + cw * 2048 + (i & 1) * 1024 + lane * 16) = pre[i];
        STAMP(8);
        lds_wait(FL + F_VI + cw);
        GU_RANGE(0, GU_B)
        SB();
      }
    } else {
      const unsigned char* wsrc = (const unsigned char*)(wo + (size_t)(W_HEAD() * 128 + cw * 32) * 512 + W_CQ() * 128);
      const unsigned wdst = (unsigned)(uintptr_t)(L + L_WO) + cw * 8192;
      const int vo = kg * 1024 + n * 16, sto = ST_OFF();
      lds_wait(FL + F_VE + cw);
      const v4u s0 = bld<AUX_SC1>(rws, sto, 0), s1 = bld<AUX_SC1>(rws, sto + 16, 0);
      if (all_ok(tagdiff(s0, tag) | tagdiff(s1, tag))) {
        const float cs = split_weight(get4(s0), get4(s1), W_HEAD());
        if (lane < 8) *(LAS float*)(L + L_CS + cw * 32 + lane * 4) = cs;
        lds_set(FL + F_CS + cw);
      }
      STAMP(8);
      lds_wait(FL + F_VI + cw);
#pragma unroll
      for (int j = 0; j < 8; ++j) {
        unsigned keep;
        asm volatile("s_mov_b32 %0, m0\n\ts_mov_b32 m0, %3\n\ts_nop 0\n\tglobal_load_lds_dwordx4 %1, %2 nt\n\ts_mov_b32 m0, %0"
                     : "=&s"(keep) : "v"(vo), "s"(wsrc + j * 4096), "s"(wdst + j * 1024) : "memory");
      }
    }
  }
  int h1o = WS_H1 + (grp * 8 + s) * 2048 + tid * 16;
  PIN(h1o);
  STAMP(9);
  wg_barrier();
  if (wv < 2) {
    const LAS f32x4* mp = (const LAS f32x4*)(L + L_MRG + tid * 16);
    st16(rws, h1o, put4((mp[0] + mp[128]) + (mp[256] + mp[384]), tag));
  }
  SB();
  STAMP(10);
  if (ABL) { STAMP_OUT(); return; }
  if (g >= 128) {
    const int whead = W_HEAD(), wcq = W_CQ();
    int h2o = WS_H2 + ((kg * 16 + wcq * 4 + wv) * 32 + whead) * 128 + n * 8;
    int oar = L_OA + (n >> 2) * 320 + kg * 16;
    unsigned tg2 = tag >> ((n & 1) * 16), tg1 = tag >> (kg * 8);
    if (wv < 4) {
      asm volatile("s_waitcnt vmcnt(0)" ::: "memory");
      lds_arrive(FC + F_WCNT, lane);
      lds_wait(FL + F_WCNT, 4u);
      unsigned wor[32];
#pragma unroll
      for (int j = 0; j < 32; ++j) wor[j] = *(const LAS unsigned*)(L + L_WO + ((j >> 3) * 32 + 8 * kg + (j & 7)) * 256 + wv * 64 + n * 4);
#pragma unroll
      for (int k = 0; k < 8; ++k) {
        const unsigned sel = (k & 1) ? 0x07060302u : 0x05040100u;
#pragma unroll
        for (int jp = 0; jp < 4; ++jp) gur[k][jp] = __builtin_amdgcn_perm(wor[8 * (k >> 1) + 2 * jp + 1], wor[8 * (k >> 1) + 2 * jp], sel);
        PIN(gur[k]);
      }
      PIN(h2o); PIN(oar); PIN(tg2); PIN(tg1);
      STAMP(5);
    } else {
      const int rb = wv - 4;
      if (__builtin_amdgcn_readfirstlane(FL[F_CS + rb]) == 0u) {
        const int off[2] = {ST_OFF(), ST_OFF() + 16};
        f32x4 v[2];
        sweep<2, 0>(rws, off, v, tag);
        const float c_ = split_weight(v[0], v[1], whead);
        if (lane < 8) *(LAS float*)(L + L_CS + rb * 32 + lane * 4) = c_;
        LGKM0();
      }
      float cs = *(const LAS float*)(L + L_CS + rb * 32 + (lane >> 3) * 4);
      int oaw = L_OA + rb * 320 + n * 8 + kg * 2;
      PIN(cs); PIN(oaw);
      const int ho = WS_H1 + ((rb * 8 + (whead >> 2)) * 8 + (lane >> 3)) * 2048 + (whead & 3) * 512 + (lane & 7) * 16;
      const int off[4] = {ho, ho + 128, ho + 256, ho + 384};
      f32x4 v[4];
      sweep<4, 0>(rws, off, v, tag);
      const float o0 = part_sum(v[0] * cs), o1 = part_sum(v[1] * cs), o2 = part_sum(v[2] * cs), o3 = part_sum(v[3] * cs);
      const unsigned pk = pack2((lane & 8) ? o1 : o0, (lane & 8) ? o3 : o2);
      *(LAS unsigned short*)(L + oaw) = (unsigned short)pk;
      *(LAS unsigned short*)(L + oaw + 128) = (unsigned short)(pk >> 16);
    }
    wg_barrier();
    STAMP(11);
    if (wv < 4) {
      f32x4 c0 = {0.f, 0.f, 0.f, 0.f}, c1 = {0.f, 0.f, 0.f, 0.f};
#pragma unroll
      for (int k = 0; k < 4; ++k) { const v4u a = *(const LAS v4u*)(L + oar + k * 64); c0 = mfma32(a, gur[2 * k], c0); c1 = mfma32(a, gur[2 * k + 1], c1); }
      __builtin_amdgcn_raw_buffer_store_b64(v2u{put1(c0[0], tg2), put1(c1[0], tg2 >> 8)}, rws, h2o, 0, AUX_SC1);
    }
    SB();
    STAMP(12);
    if (g >= 192) {
      const int rr = g - 192, col = 4 * (lane & 7) + kg;
      float x1 = 0.f, gb = 0.f;
      if (wv == 0) {
        float ga = *(const LAS float*)(L + L_GG + col * 4), xin = *(const LAS float*)(L + L_GG + 256 + col * 4);
        gb = *(const LAS float*)(L + L_GG + 128 + col * 4);
        int x1o = WS_X1 + (n >> 3) * 8192 + rr * 128 + col * 4;
        PIN(ga); PIN(xin); PIN(x1o); PIN(gb);
        const int ho = WS_H2 + rr * 4096 + lane * 16;
        const int off[4] = {ho, ho + 1024, ho + 2048, ho + 3072};
        f32x4 v[4];
        sweep<4, 0>(rws, off, v, tag);
        STAMP(13);
        x1 = rbf(xin + rbf(ga * rbf(part_sum((v[0] + v[1]) + (v[2] + v[3])))));
        const unsigned xw = put1(x1, tg1);
        __builtin_amdgcn_raw_buffer_store_b32(xw, rws, x1o, 0, AUX_SC1);
        if (XREP > 2) __builtin_amdgcn_raw_buffer_store_b32(xw, rws, x1o, 16384, AUX_SC1);
        SB();
      }
      STAMP(14);
      if (wv == 7) {
        const int off[1] = {WS_X1 + (g & (XREP - 1)) * 8192 + lane * 128};
        f32x4 v[1];
        sweep<1, 0>(rws, off, v, tag);
        STAMP(13);
        __builtin_amdgcn_s_sleep(DLY_H4 > 0 ? DLY_H4 : 0);
        FL[F_H4] = 1u;
      } else {
        const int off[2] = {WS_H4 + rr * 14336 + wv * 2048 + lane * 16, WS_H4 + rr * 14336 + wv * 2048 + 1024 + lane * 16};
        f32x4 v[2];
        if (DLY_H4 >= 0) lds_wait(FL + F_H4);
        sweep<2, 0>(rws, off, v, tag);
        *(LAS float*)(L + L_RED + wv * 128 + col * 4) = part_sum(v[0] + v[1]);
      }
      wg_barrier();
      if (wv == 0) {
        const LAS float* rp = (const LAS float*)(L + L_RED + col * 4);
        const float a = ((rp[0] + rp[32]) + (rp[64] + rp[96])) + ((rp[128] + rp[160]) + rp[192]);
        const float o = x1 + rbf(gb * rbf(a));
        if (n < 8) __builtin_amdgcn_raw_buffer_store_b16((unsigned short)pack2(o, 0.f), mkrsrc(out, 4096), (rr * 32 + col) * 2, 0, AUX_SC1);
        if (g == 192 && lane == 0) __builtin_amdgcn_raw_buffer_store_b32(tag, rws, WS_EPOCH, 0, AUX_SC1);
      }
      STAMP(15);
    }
  } else {
    const int ru = ((g >> 5) << 3) + (g & 7), jsl = 4 * ru + ((g >> 3) & 3);
    if (ru < 28) {
      __builtin_amdgcn_s_barrier();
      v2u dnr[8];
      if (wv < 4) {
        const bf16_t* gup = GU_PTR();
        GU_RANGE(GU_B, 8)
      }
      {
        const rsrc_t rd = mkrsrc(wd + (size_t)(16 * jsl) * 512, 16384);
        const int dofs = (kg & 1) * 8192 + wv * 128 + n * 8;
#pragma unroll
        for (int j = 0; j < 8; ++j) dnr[j] = __builtin_amdgcn_raw_buffer_load_b64(rd, dofs, j * 1024, AUX_NT);
      }
      if (wv < 4) {
        const int fb = L_FB + (n & 3) * 8192 + ((((wv * 4 + (n >> 2)) * 4 + kg) ^ (n & 3)) << 4);
#define TG(E) *(LAS v4u*)(L + (fb ^ ((E & 1) << 5)) + E * 1024) = tcol8<E>(gur);
        FOR8(TG)
#undef TG
      }
      wg_barrier();
      STAMP(12);
      v4u bg[4], bd[2];
#pragma unroll
      for (int t = 0; t < 4; ++t) { bg[t] = *(const LAS v4u*)(L + L_FB + (((n >> 1) & 1) * 16 + 2 * wv + (n & 1)) * 1024 + ((((t * 4 + (n >> 2)) * 4 + kg) ^ ((n & 2) + (wv >> 2)) ^ ((n & 1) << 1)) << 4)); PIN(bg[t]); }
      const int rb = wv >> 1, hf = wv & 1;
      f32x4 shv = *(const LAS f32x4*)(L + L_SC + rb * 2048 + hf * 1024 + lane * 16), scv = *(const LAS f32x4*)(L + L_SC + 8192 + rb * 2048 + hf * 1024 + lane * 16);
      PIN(shv); PIN(scv);
      {
        const unsigned sel = (kg >> 1) ? 0x07060302u : 0x05040100u;
#pragma unroll
        for (int m2 = 0; m2 < 2; ++m2) {
#pragma unroll
          for (int jp = 0; jp < 4; ++jp) bd[m2][jp] = __builtin_amdgcn_perm(dnr[2 * jp + 1][m2], dnr[2 * jp][m2], sel);
          PIN(bd[m2]);
        }
      }
      int hbw = L_HB + rb * 1024 + (rb >> 1) * 32 + hf * 512 + lane * 8;
      int hbr = L_HB + (n >> 2) * 1024 + (n >> 3) * 32 + (32 * (n & 3) + 8 * kg) * 2;
      int acr = ((n & 1) == (kg >> 1)) ? L_ACT + (n >> 2) * 32 + (kg & 1) * 16 : L_ZERO, acw = L_ACT + kg * 32 + wv * 4 + (n & 1) * 2;
      int h4o = WS_H4 + ((kg * 16 + 2 * wv + (n >> 3)) * 112 + jsl) * 128 + (n & 7) * 16;
      PIN(hbw); PIN(hbr); PIN(acr); PIN(acw); PIN(h4o);
      SB();
      {
        const int xr = WS_X1 + (g & (XREP - 1)) * 8192 + rb * 2048 + lane * 16;
        const int off[2] = {xr + hf * 1024, xr + (hf ^ 1) * 1024};
        f32x4 v[2];
        sweep<2, 0>(rws, off, v, tag);
        STAMP(13);
        const float x0 = __uint_as_float(__builtin_amdgcn_readfirstlane(__float_as_uint(0.5f * (v[0][0] + v[1][0]))));
        const f32x4 d0 = v[0] - x0, d1 = v[1] - x0;
        const f32x4 sa = d0 + d1, sb2 = d0 * d0 + d1 * d1;
        float s1 = (sa[0] + sa[1]) + (sa[2] + sa[3]), s2 = (sb2[0] + sb2[1]) + (sb2[2] + sb2[3]);
        s1 = wave_sum(s1); s2 = wave_sum(s2);
        const float md = s1 * (1.f / 512.f), var = fmx(s2 * (1.f / 512.f) - md * md, 0.f);
        const float mb = rbf(x0 + md), rden = __builtin_amdgcn_rcpf(rbf(__builtin_amdgcn_sqrtf(rbf(rbf(var) + 1e-5f))));
        const f32x2 ha = rbf2(rbf2(rbf2(f32x2{v[0][0], v[0][1]} - mb) * rden) * f32x2{scv[0], scv[1]}) + f32x2{shv[0], shv[1]};
        const f32x2 hb = rbf2(rbf2(rbf2(f32x2{v[0][2], v[0][3]} - mb) * rden) * f32x2{scv[2], scv[3]}) + f32x2{shv[2], shv[3]};
        *(LAS v2u*)(L + hbw) = v2u{pack2(ha.x, ha.y), pack2(hb.x, hb.y)};
      }
      wg_barrier();
      {
        v4u a[4];
#pragma unroll
        for (int t = 0; t < 4; ++t) a[t] = *(const LAS v4u*)(L + hbr + t * 256);
        SB();
        f32x4 c = {0.f, 0.f, 0.f, 0.f};
#pragma unroll
        for (int t = 0; t < 4; ++t) c = mfma32(a[t], bg[t], c);
        float gs = sel4(lane & 4, lane & 8, c[0], c[1], c[2], c[3]);
        gs += dpp<ROR4>(gs); gs += dpp<ROR8>(gs);
        gs = rbf(gs);
        const float act = rbf(gelu_f(gs)) * dpp<XOR2>(gs);
        if (n < 2) *(LAS unsigned short*)(L + acw) = (unsigned short)pack2(act, 0.f);
      }
      wg_barrier();
      STAMP(14);
      {
        const v4u a = *(const LAS v4u*)(L + acr);
        const f32x4 c0 = mfma32(a, bd[0], f32x4{0.f, 0.f, 0.f, 0.f}), c1 = mfma32(a, bd[1], f32x4{0.f, 0.f, 0.f, 0.f});
        st16(rws, h4o, put4(f32x4{c0[0], c0[1], c1[0], c1[1]}, tag));
      }
      STAMP(15);
    }
  }
  STAMP_OUT();
}

extern "C" void kernel_launch(void* const* d_in, const int* in_sizes, int n_in, void* d_out, int out_size, void* d_ws, size_t ws_size, hipStream_t stream) {
  adaln_block_kernel<<<256, 512, 0, stream>>>((const bf16_t*)d_in[0], (const bf16_t*)d_in[1], (const bf16_t*)d_in[2], (const bf16_t*)d_in[3],
                                              (const bf16_t*)d_in[4], (const bf16_t*)d_in[6], (const bf16_t*)d_in[7], (const bf16_t*)d_in[8],
                                              (const bf16_t*)d_in[9], (const bf16_t*)d_in[10], (bf16_t*)d_out, (unsigned char*)d_ws);
}
```

```cpp
#include <hip/hip_runtime.h>
#include <hip/hip_bf16.h>
#include <stdint.h>

#define GAS __attribute__((address_space(1)))
#define LAS __attribute__((address_space(3)))
typedef unsigned short bf16_t;
typedef unsigned v4u __attribute__((ext_vector_type(4)));
typedef unsigned v2u __attribute__((ext_vector_type(2)));
typedef float f32x4 __attribute__((ext_vector_type(4)));
typedef float f32x2 __attribute__((ext_vector_type(2)));
typedef __bf16 bf16x8 __attribute__((ext_vector_type(8)));
typedef __bf16 bf16x2 __attribute__((ext_vector_type(2)));
typedef short s16x4 __attribute__((ext_vector_type(4)));
typedef __amdgpu_buffer_rsrc_t rsrc_t;

#define PLO 4
#define PHI 8
#define SCH_A 110
#define SCH_T16 250
#define AUX_W 0
#define T_MOD 380
#define T_Q 640
#define GU_B 0
#define DLY_H4 34
#define VE_AT 26
#define XREP 4
#define GUROT 1
#define H2W 1
#define ABL 0

namespace {
constexpr int SK = 16384, WIN = 4096, FF = 1792;
constexpr int AUX_SC1 = 16, AUX_NT = 2;
constexpr int WS_EPOCH = 0;
constexpr int WS_X1 = 4096;
constexpr int WS_SB = WS_X1 + 32768;
constexpr int WS_CB = WS_SB + 8192;
constexpr int WS_ST = WS_CB + 8192;
constexpr int WS_MOD = WS_ST + 32768;
constexpr int WS_QP = WS_MOD + 196608;
constexpr int WS_H1 = WS_QP + 524288;
constexpr int WS_H2 = WS_H1 + 524288;
constexpr int WS_H4 = WS_H2 + 262144;
constexpr int WS_END = WS_H4 + 917504;
constexpr int WS_STAMP = 8 << 20;
constexpr int L_K = 0;
constexpr int L_FB = 0;
constexpr int L_WO = 0;
constexpr int L_SC = 32768;
constexpr int L_P = 131072;
constexpr int L_QB = L_P + 4096;
constexpr int L_HQ = L_QB + 1280;
constexpr int L_ZERO = L_HQ + 1024;
constexpr int L_FLAG = L_ZERO + 1024;
constexpr int L_GG = L_FLAG + 256;
constexpr int L_OA = L_GG + 384;
constexpr int L_CS = L_OA + 1280;
constexpr int L_F = L_CS + 128;
constexpr int L_ACT = L_F + 128;
constexpr int L_RED = L_ACT + 128;
constexpr int L_MRG = L_RED + 2048;
constexpr int L_HB = L_MRG + 8192;
constexpr int L_END = L_HB + 4096 + 64;
enum { F_MCNT = 0, F_HCNT = 1, F_QCNT = 2, F_K = 4, F_PCNT = 8, F_G = 10, F_H4 = 12, F_WCNT = 13, F_VI = 16, F_VE = 20, F_CS = 24 };
constexpr unsigned SPIN_G = 1u << 16, SPIN_L = 1u << 20;

__device__ __forceinline__ float bflo(unsigned u) { return __uint_as_float(u << 16); }
__device__ __forceinline__ float bfhi(unsigned u) { return __uint_as_float(u & 0xFFFF0000u); }
__device__ __forceinline__ unsigned pack2(float lo, float hi) {
  f32x2 v = {lo, hi};
  return __builtin_bit_cast(unsigned, __builtin_convertvector(v, bf16x2));
}
__device__ __forceinline__ float rbf(float x) { return bflo(pack2(x, x) & 0xFFFFu); }
__device__ __forceinline__ float fmx(float a, float b) { return __builtin_amdgcn_fmed3f(a, b, __builtin_inff()); }
template <int CTRL> __device__ __forceinline__ float dpp(float x) {
  return __builtin_bit_cast(float, __builtin_amdgcn_mov_dpp(__builtin_bit_cast(int, x), CTRL, 0xf, 0xf, true));
}
constexpr int XOR1 = 0xB1, XOR2 = 0x4E, HMIR = 0x141, ROR2 = 0x122, ROR4 = 0x124, ROR8 = 0x128;
__device__ __forceinline__ float xrow_sum(float x) {
  auto s = __builtin_amdgcn_permlane16_swap(__float_as_uint(x), __float_as_uint(x), false, false);
  x = __uint_as_float(s[0]) + __uint_as_float(s[1]);
  auto t = __builtin_amdgcn_permlane32_swap(__float_as_uint(x), __float_as_uint(x), false, false);
  return __uint_as_float(t[0]) + __uint_as_float(t[1]);
}
__device__ __forceinline__ float xrow_max(float x) {
  auto s = __builtin_amdgcn_permlane16_swap(__float_as_uint(x), __float_as_uint(x), false, false);
  x = fmx(__uint_as_float(s[0]), __uint_as_float(s[1]));
  auto t = __builtin_amdgcn_permlane32_swap(__float_as_uint(x), __float_as_uint(x), false, false);
  return fmx(__uint_as_float(t[0]), __uint_as_float(t[1]));
}
__device__ __forceinline__ float wave_sum(float x) {
  x += dpp<XOR1>(x); x += dpp<XOR2>(x); x += dpp<ROR4>(x); x += dpp<ROR8>(x);
  return xrow_sum(x);
}
__device__ __forceinline__ f32x4 mfma32(v4u a, v4u b, f32x4 c) {
  return __builtin_amdgcn_mfma_f32_16x16x32_bf16(__builtin_bit_cast(bf16x8, a), __builtin_bit_cast(bf16x8, b), c, 0, 0, 0);
}
template <int E> __device__ __forceinline__ v4u tcol8(const v4u* r) {
  constexpr unsigned sel = (E & 1) ? 0x07060302u : 0x05040100u;
  v4u o;
  o.x = __builtin_amdgcn_perm(r[1][E >> 1], r[0][E >> 1], sel);
  o.y = __builtin_amdgcn_perm(r[3][E >> 1], r[2][E >> 1], sel);
  o.z = __builtin_amdgcn_perm(r[5][E >> 1], r[4][E >> 1], sel);
  o.w = __builtin_amdgcn_perm(r[7][E >> 1], r[6][E >> 1], sel);
  return o;
}
__device__ __forceinline__ v4u tcol8r(const v4u* r, int e) {
  const unsigned sel = (e & 1) ? 0x07060302u : 0x05040100u;
  v4u o;
  o.x = __builtin_amdgcn_perm(r[1][e >> 1], r[0][e >> 1], sel);
  o.y = __builtin_amdgcn_perm(r[3][e >> 1], r[2][e >> 1], sel);
  o.z = __builtin_amdgcn_perm(r[5][e >> 1], r[4][e >> 1], sel);
  o.w = __builtin_amdgcn_perm(r[7][e >> 1], r[6][e >> 1], sel);
  return o;
}
#define FOR8(X) X(0) X(1) X(2) X(3) X(4) X(5) X(6) X(7)
__device__ __forceinline__ rsrc_t mkrsrc(const void* p, int bytes) { return __builtin_amdgcn_make_buffer_rsrc((void*)p, 0, bytes, 0x00020000); }
template <int AUX> __device__ __forceinline__ v4u bld(rsrc_t r, int voff, int soff) { return __builtin_amdgcn_raw_buffer_load_b128(r, voff, soff, AUX); }
__device__ __forceinline__ unsigned put1(float v, unsigned t) { return (__float_as_uint(v) & ~15u) | (t & 15u); }
__device__ __forceinline__ v4u put4(f32x4 v, unsigned tag) { return v4u{put1(v.x, tag), put1(v.y, tag >> 8), put1(v.z, tag >> 16), put1(v.w, tag >> 24)}; }
__device__ __forceinline__ unsigned tagdiff(v4u v, unsigned tag) {
  return (__builtin_amdgcn_perm(v.y, v.x, 0x0c0c0400u) | __builtin_amdgcn_perm(v.w, v.z, 0x04000c0cu)) ^ tag;
}
__device__ __forceinline__ bool all_ok(unsigned diff) { return __all((diff & 0x0F0F0F0Fu) == 0u); }
__device__ __forceinline__ f32x4 get4(v4u v) { return __builtin_bit_cast(f32x4, v & ~15u); }
__device__ __forceinline__ void st16(rsrc_t r, int off, v4u v) { __builtin_amdgcn_raw_buffer_store_b128(v, r, off, 0, AUX_SC1); }
#define CBAR() asm volatile("" ::: "memory")
#define LGKM0() asm volatile("s_waitcnt lgkmcnt(0)" ::: "memory")
#define SB() __builtin_amdgcn_sched_barrier(0)
#define PIN(x) asm volatile("" : "+v"(x))
__device__ __forceinline__ void lds_wait(volatile LAS unsigned* f, unsigned want = 1u) {
#pragma unroll 1
  for (unsigned i = 0; i < SPIN_L; ++i) { if (*f >= want) break; __builtin_amdgcn_s_sleep(1); }
  CBAR();
}
__device__ __forceinline__ void lds_set(volatile LAS unsigned* f) { LGKM0(); *f = 1u; }
__device__ __forceinline__ void lds_arrive(LAS unsigned* f, int lane) {
  LGKM0();
  if (lane == 0) __hip_atomic_fetch_add(f, 1u, __ATOMIC_RELAXED, __HIP_MEMORY_SCOPE_WORKGROUP);
}
__device__ __forceinline__ void wg_barrier() { LGKM0(); __builtin_amdgcn_s_barrier(); CBAR(); }
template <int N, int SLEEP> __device__ __forceinline__ void sweep(rsrc_t rs, const int (&off)[N], f32x4 (&o)[N], unsigned tag) {
  v4u v[N];
#pragma unroll 1
  for (unsigned spin = 0;; ++spin) {
    unsigned bad = 0;
#pragma unroll
    for (int i = 0; i < N; ++i) v[i] = bld<AUX_SC1>(rs, off[i], 0);
#pragma unroll
    for (int i = 0; i < N; ++i) bad |= tagdiff(v[i], tag);
    if (all_ok(bad) || spin >= SPIN_G) break;
    if (SLEEP) __builtin_amdgcn_s_sleep(SLEEP);
    CBAR();
  }
#pragma unroll
  for (int i = 0; i < N; ++i) o[i] = get4(v[i]);
}
__device__ __forceinline__ float fexp(float x) { return __builtin_amdgcn_exp2f(x * 1.4426950408889634f); }
__device__ __forceinline__ float silu_f(float x) { return x * __builtin_amdgcn_rcpf(1.f + fexp(-x)); }
__device__ __forceinline__ float gelu_f(float x) {
  const float u = 0.7978845608028654f * (x + 0.044715f * x * x * x);
  return x * __builtin_amdgcn_rcpf(1.f + fexp(-2.f * u));
}
__device__ __forceinline__ void tcol4x8(const v2u* r, v4u* o) {
#pragma unroll
  for (int e = 0; e < 4; ++e) {
    const unsigned sel = (e & 1) ? 0x07060302u : 0x05040100u;
#pragma unroll
    for (int jp = 0; jp < 4; ++jp) o[e][jp] = __builtin_amdgcn_perm(r[2 * jp + 1][e >> 1], r[2 * jp][e >> 1], sel);
    PIN(o[e]);
  }
}
__device__ __forceinline__ f32x4 gemv32x64(v4u a, const v4u* bt) {
  f32x4 c[4];
#pragma unroll
  for (int e = 0; e < 4; ++e) c[e] = mfma32(a, bt[e], f32x4{0.f, 0.f, 0.f, 0.f});
  return f32x4{c[0][0], c[1][0], c[2][0], c[3][0]};
}
__device__ __forceinline__ void wait_until(unsigned long long t0, unsigned ticks) {
#pragma unroll 1
  for (int i = 0; i < 4096 && __builtin_amdgcn_s_memrealtime() - t0 < ticks; ++i) __builtin_amdgcn_s_sleep(1);
}
__device__ __forceinline__ float sel4(bool b0, bool b1, float c0, float c1, float c2, float c3) {
  const float lo = b0 ? c1 : c0, hi = b0 ? c3 : c2;
  return b1 ? hi : lo;
}
__device__ __forceinline__ float part_sum(f32x4 v) {
  auto p = __builtin_amdgcn_permlane32_swap(__float_as_uint(v[0]), __float_as_uint(v[2]), false, false);
  auto q = __builtin_amdgcn_permlane32_swap(__float_as_uint(v[1]), __float_as_uint(v[3]), false, false);
  const float A = __uint_as_float(p[0]) + __uint_as_float(p[1]), B = __uint_as_float(q[0]) + __uint_as_float(q[1]);
  auto r = __builtin_amdgcn_permlane16_swap(__float_as_uint(A), __float_as_uint(B), false, false);
  const float t = __uint_as_float(r[0]) + __uint_as_float(r[1]);
  return t + dpp<ROR8>(t);
}
__device__ __forceinline__ float split_weight(f32x4 m4, f32x4 l4, int hh) {
  const bool h0 = hh & 1, h1 = hh & 2;
  const float m = sel4(h0, h1, m4[0], m4[1], m4[2], m4[3]), l = sel4(h0, h1, l4[0], l4[1], l4[2], l4[3]);
  float M = fmx(m, dpp<XOR1>(m)); M = fmx(M, dpp<XOR2>(M)); M = fmx(M, dpp<HMIR>(M));
  const float e = fexp(m - M);
  float Ls = l * e; Ls += dpp<XOR1>(Ls); Ls += dpp<XOR2>(Ls); Ls += dpp<HMIR>(Ls);
  return e * __builtin_amdgcn_rcpf(Ls);
}
__device__ __forceinline__ f32x2 rbf2(f32x2 v) {
  const unsigned p = pack2(v.x, v.y);
  return f32x2{bflo(p), bfhi(p)};
}
template <bool MAX> __device__ __forceinline__ float red2(float a, float b) { return MAX ? fmx(a, b) : a + b; }
template <bool MAX> __device__ __forceinline__ f32x4 wave_red4(f32x4 s, int lane) {
  const bool o1 = lane & 1, o2 = lane & 2;
  const float a = red2<MAX>(o1 ? s[1] : s[0], dpp<XOR1>(o1 ? s[0] : s[1])), c = red2<MAX>(o1 ? s[3] : s[2], dpp<XOR1>(o1 ? s[2] : s[3]));
  float m = red2<MAX>(o2 ? c : a, dpp<XOR2>(o2 ? a : c));
  m = red2<MAX>(m, dpp<ROR4>(m)); m = red2<MAX>(m, dpp<ROR8>(m));
  m = MAX ? xrow_max(m) : xrow_sum(m);
  return f32x4{dpp<0x00>(m), dpp<0x55>(m), dpp<0xAA>(m), dpp<0xFF>(m)};
}
#define STAMP(i) do {} while (0)
#define STAMP_OUT() do {} while (0)
}

__global__ void __launch_bounds__(512, 2)
adaln_block_kernel(const bf16_t* __restrict__ x, const bf16_t* __restrict__ kc, const bf16_t* __restrict__ vc,
                   const bf16_t* __restrict__ wq, const bf16_t* __restrict__ wo, const bf16_t* __restrict__ wg,
                   const bf16_t* __restrict__ wu, const bf16_t* __restrict__ wd, const bf16_t* __restrict__ cc,
                   const bf16_t* __restrict__ wmod, bf16_t* __restrict__ out, unsigned char* ws) {
  __shared__ __attribute__((aligned(16))) unsigned char lds_raw[L_END];
  LAS unsigned char* L = (LAS unsigned char*)lds_raw;
  volatile LAS unsigned* FL = (volatile LAS unsigned*)(L + L_FLAG);
  LAS unsigned* FC = (LAS unsigned*)(L + L_FLAG);
  const unsigned long long tstart = __builtin_amdgcn_s_memrealtime();
  const int tid = threadIdx.x, lane = tid & 63, wv = __builtin_amdgcn_readfirstlane(tid >> 6);
  const int g = blockIdx.x, grp = (g & 7) * 4 + (g >> 6), b = grp >> 3, kvh = grp & 7, s = (g >> 3) & 7;
  const int n = lane & 15, kg = lane >> 4;
  if (tid < 320) *(LAS unsigned*)(L + L_ZERO + tid * 4) = 0u;
  asm volatile("" :: "s"(x), "s"(kc), "s"(vc), "s"(wq), "s"(cc), "s"(wmod), "s"(ws));
  wg_barrier();
  const rsrc_t rws = mkrsrc(ws, WS_END);
  unsigned tag;
  v4u gur[8];

  if (__builtin_expect(wv >= 4, 1)) {
    const int w = wv - 4;
    const size_t kvbase = ((size_t)grp * SK + (SK - WIN) + s * 512) * 128;
    const int rot = (g * 37) & 127;
    const unsigned char* kbase = (const unsigned char*)(kc + kvbase);
    const unsigned ldsk = (unsigned)(uintptr_t)(L + L_K);
    const int klane = kg * 256 + ((n ^ kg) << 4);
    const unsigned due0 = (unsigned)tstart + SCH_A;
#define PACE_WAIT(now, i) do { \
      asm volatile("s_waitcnt vmcnt(%0)" :: "i"(PHI) : "memory"); \
      if (PHI > PLO && (int)((now) - due0) < (((i) * SCH_T16) >> 4)) asm volatile("s_waitcnt vmcnt(%0)" :: "i"(PLO) : "memory"); } while (0)
    STAMP(7);
#pragma unroll 4
    for (int i = 0; i < 32; ++i) {
      const int phys = (4 * i + w + rot) & 127;
      const unsigned char* sb_ = kbase + phys * 1024;
      const int vo = klane ^ ((i & 3) << 6);
      const unsigned dst = ldsk + (4 * i + w) * 1024;
      unsigned keep;
      asm volatile("s_mov_b32 %0, m0\n\ts_mov_b32 m0, %3\n\ts_nop 0\n\tglobal_load_lds_dwordx4 %1, %2 nt\n\ts_mov_b32 m0, %0"
                   : "=&s"(keep) : "v"(vo), "s"(sb_), "s"(dst) : "memory");
      const unsigned now = (unsigned)__builtin_amdgcn_s_memrealtime();
      PACE_WAIT(now, i);
    }
    STAMP(1);
    const unsigned epoch = __builtin_amdgcn_raw_buffer_load_b32(rws, WS_EPOCH, 0, AUX_SC1);
    const rsrc_t rv = mkrsrc(vc + kvbase, 131072);
    v4u V[32], VT[24];
    constexpr int TR0 = 8 + PHI;
#pragma unroll
    for (int i = 0; i < 32; ++i) {
      V[i] = bld<AUX_NT>(rv, lane * 16, ((4 * i + w + rot) & 127) * 1024);
      const unsigned now = (unsigned)__builtin_amdgcn_s_memrealtime();
      if (i >= TR0) { const int k = i - TR0; VT[k] = tcol8r(V + 8 * (k >> 3), k & 7); PIN(VT[k]); }
      if (i == PLO - 1) { asm volatile("s_waitcnt vmcnt(%0)" :: "i"(PLO) : "memory"); FL[F_K + w] = 1u; STAMP(2); }
      else if (i < 31) PACE_WAIT(now, 32 + i);
      if (i == VE_AT) FL[F_VE + w] = 1u;
      SB();
    }
    FL[F_VI + w] = 1u;
    tag = __builtin_amdgcn_readfirstlane(epoch) + 0x01010101u;
    STAMP(3);
#pragma unroll
    for (int k = 32 - TR0; k < 24; ++k) { VT[k] = tcol8r(V + 8 * (k >> 3), k & 7); PIN(VT[k]); }
    lds_wait(FL + F_PCNT, 4u);
    STAMP(4);
    f32x4 f;
    {
      const LAS f32x4* sp = (const LAS f32x4*)(L + L_F);
      const f32x4 m0 = sp[0], m1 = sp[2], m2 = sp[4], m3 = sp[6], mo = sp[2 * w];
#pragma unroll
      for (int h = 0; h < 4; ++h) f[h] = fexp(mo[h] - fmx(fmx(m0[h], m1[h]), fmx(m2[h], m3[h])));
    }
    f32x4 acc[2] = {f32x4{0.f, 0.f, 0.f, 0.f}, f32x4{0.f, 0.f, 0.f, 0.f}};
    int paddr[4];
#pragma unroll
    for (int ep = 0; ep < 4; ++ep) paddr[ep] = (n >> 2) == ep ? L_P + w * 1024 + (((n & 3) * 4 + kg) << 4) : L_ZERO;
    int mra = L_MRG + (w * 512 + 8 * n + 2 * kg) * 4;
#pragma unroll
    for (int G = 0; G < 4; ++G) {
      v4u pa[4];
#pragma unroll
      for (int ep = 0; ep < 4; ++ep) pa[ep] = *(const LAS v4u*)(L + paddr[ep] + G * 256);
      if (G == 3) { PIN(pa[0]); PIN(pa[1]); PIN(pa[2]); PIN(pa[3]); PIN(f); PIN(mra); PIN(acc[0]); PIN(acc[1]); STAMP(5); PIN(V[24]); SB(); }
#define PV(E) acc[E & 1] = mfma32(pa[E >> 1], G < 3 ? VT[8 * G + E] : tcol8<E>(V + 24), acc[E & 1]);
      FOR8(PV)
#undef PV
      SB();
    }
    STAMP(6);
#pragma unroll
    for (int h = 0; h < 4; ++h) *(LAS f32x2*)(L + mra + h * 512) = f32x2{acc[0][h], acc[1][h]} * f[h];
  } else if (ABL == 6) {
    lds_arrive(FC + F_PCNT, lane); tag = 0;
  } else {
    const int cw = wv;
    __builtin_amdgcn_s_setprio(2);
    const int ks = g >> 5, ns = g & 31, u5 = lane & 31, h5 = lane >> 5;
    const int mkq = g < 64 ? g >> 4 : (g - 64) >> 5, mcb = g < 64 ? g & 15 : 16 + ((g - 64) & 31);
    v2u wm[8], wr[8];
    const rsrc_t rm = mkrsrc(wmod + (size_t)(mkq * 128 + cw * 32) * 3072, g < 192 ? 32 * 6144 : 0);
    const rsrc_t rq = mkrsrc(wq + (size_t)(ks * 64 + (cw & 1) * 32) * 4096, 32 * 8192);
    const v4u cf = *(const v4u*)(cc + (n >> 2) * 512 + ((128 * mkq) & 511) + 32 * cw + 8 * kg);
#pragma unroll
    for (int j = 0; j < 8; ++j) wm[j] = __builtin_amdgcn_raw_buffer_load_b64(rm, 8 * kg * 6144 + mcb * 128 + n * 8, j * 6144, AUX_NT);
    const unsigned epoch = __builtin_amdgcn_raw_buffer_load_b32(rws, WS_EPOCH, 0, AUX_SC1);
    const v4u xs = *(const v4u*)(x + cw * 512 + lane * 8);
    const v2u xo = *(const v2u*)(x + cw * 512 + 64 * ks + 4 * n);
#pragma unroll
    for (int j = 0; j < 8; ++j) wr[j] = __builtin_amdgcn_raw_buffer_load_b64(rq, 8 * kg * 8192 + ns * 256 + (cw >> 1) * 128 + n * 8, j * 8192, AUX_NT);
    SB();
    STAMP(1);
    v4u bq[4];
    {
      v4u a, bt[4];
#pragma unroll
      for (int i = 0; i < 4; ++i) a[i] = pack2(silu_f(bflo(cf[i])), silu_f(bfhi(cf[i])));
      PIN(a);
      tcol4x8(wm, bt);
      *(LAS f32x4*)(L + L_MRG + cw * 1024 + lane * 16) = gemv32x64(a, bt);
      lds_arrive(FC + F_MCNT, lane);
      tag = __builtin_amdgcn_readfirstlane(epoch) + 0x01010101u;
      if (cw == 0 && g < 192) {
        lds_wait(FL + F_MCNT, 4u);
        const LAS f32x4* mp = (const LAS f32x4*)(L + L_MRG + lane * 16);
        st16(rws, WS_MOD + ((mkq * 4 + kg) * 3072 + 64 * mcb + 4 * n) * 4, put4((mp[0] + mp[64]) + (mp[128] + mp[192]), tag));
        STAMP(11);
      }
    }
    float ln[4];
    {
      float sum = 0.f;
#pragma unroll
      for (int i = 0; i < 4; ++i) sum += bflo(xs[i]) + bfhi(xs[i]);
      const float mean = wave_sum(sum) * (1.f / 512.f);
      float sq = 0.f;
#pragma unroll
      for (int i = 0; i < 4; ++i) { const float d0 = bflo(xs[i]) - mean, d1 = bfhi(xs[i]) - mean; sq = fmaf(d0, d0, sq); sq = fmaf(d1, d1, sq); }
      sq = wave_sum(sq);
      const float mb = rbf(mean), den = rbf(sqrtf(rbf(rbf(sq * (1.f / 512.f)) + 1e-5f)));
#pragma unroll
      for (int i = 0; i < 2; ++i) { ln[2 * i] = rbf(rbf(bflo(xo[i]) - mb) / den); ln[2 * i + 1] = rbf(rbf(bfhi(xo[i]) - mb) / den); }
    }
    tcol4x8(wr, bq);
    STAMP(2);
    wait_until(tstart, T_MOD);
    {
      const int mo = WS_MOD + ((h5 * 4 + cw) * 3072 + (kg & 1) * 512 + 64 * ks + 4 * n) * 4;
      const int off[2] = {mo, mo + 2 * 4 * 3072 * 4};
      f32x4 v[2];
      sweep<2, 8>(rws, off, v, tag);
      STAMP(3);
      const f32x4 t = v[0] + v[1];
      float h[4];
#pragma unroll
      for (int e = 0; e < 4; ++e) {
        auto p = __builtin_amdgcn_permlane32_swap(__float_as_uint(t[e]), __float_as_uint(t[e]), false, false);
        const float u = rbf(__uint_as_float(p[0]) + __uint_as_float(p[1]));
        auto r = __builtin_amdgcn_permlane16_swap(__float_as_uint(u), __float_as_uint(u), false, false);
        h[e] = rbf(ln[e] * rbf(1.f + __uint_as_float(r[1]))) + __uint_as_float(r[0]);
      }
      if (lane < 16) *(LAS v2u*)(L + L_HQ + cw * 128 + n * 8) = v2u{pack2(h[0], h[1]), pack2(h[2], h[3])};
      lds_arrive(FC + F_HCNT, lane);
      lds_wait(FL + F_HCNT, 4u);
      const v4u a = *(const LAS v4u*)(L + L_HQ + (n >> 2) * 128 + (cw & 1) * 64 + kg * 16);
      const f32x4 qp = gemv32x64(a, bq);
      if (cw & 1) {
        *(LAS f32x4*)(L + L_MRG + 4096 + (cw >> 1) * 1024 + lane * 16) = qp;
        lds_set(FL + F_G + (cw >> 1));
      } else {
        lds_wait(FL + F_G + (cw >> 1));
        st16(rws, WS_QP + ((kg * 8 + ks) * 4096 + 128 * ns + 64 * (cw >> 1) + 4 * n) * 4, put4(qp + *(const LAS f32x4*)(L + L_MRG + 4096 + (cw >> 1) * 1024 + lane * 16), tag));
      }
    }
    STAMP(4);
    if (cw == 3 && g >= 192) {
      const int rr = g - 192;
      const int mo = WS_MOD + ((((lane >> 3) & 3) * 4 + (rr >> 4)) * 3072 + 1024 + h5 * 512 + 32 * (rr & 15) + 4 * (lane & 7)) * 4;
      const int off[2] = {mo, mo + 4096};
      f32x4 v[2];
      const unsigned short xin = x[rr * 32 + u5];
      sweep<2, 8>(rws, off, v, tag);
#pragma unroll
      for (int i = 0; i < 2; ++i)
#pragma unroll
        for (int e = 0; e < 4; ++e) {
          float u = v[i][e]; u += dpp<ROR8>(u);
          auto p = __builtin_amdgcn_permlane16_swap(__float_as_uint(u), __float_as_uint(u), false, false);
          v[i][e] = rbf(__uint_as_float(p[0]) + __uint_as_float(p[1]));
        }
      *(LAS float*)(L + L_GG + 256 + u5 * 4) = bflo(xin);
      if (u5 < 8) {
        if (lane < 32) {
          *(LAS f32x4*)(L + L_GG + lane * 16) = v[0];
          st16(rws, WS_CB + rr * 128 + lane * 16, put4(f32x4{rbf(1.f + v[1][0]), rbf(1.f + v[1][1]), rbf(1.f + v[1][2]), rbf(1.f + v[1][3])}, tag));
        } else {
          *(LAS f32x4*)(L + L_GG + 128 + u5 * 16) = v[1];
          st16(rws, WS_SB + rr * 128 + u5 * 16, put4(v[0], tag));
        }
      }
    }
    const int qo = WS_QP + ((b * 8 + h5) * 4096 + kvh * 512 + cw * 128 + 4 * u5) * 4;
    wait_until(tstart, T_Q);
    v4u qv[4];
#pragma unroll
    for (int i = 0; i < 4; ++i) qv[i] = bld<AUX_SC1>(rws, qo, i * 32768);
    SB();
    lds_wait(FL + F_K + cw);
    STAMP(5);
    v4u kf[32];
#pragma unroll
    for (int t = 0; t < 8; ++t)
#pragma unroll
      for (int ds = 0; ds < 4; ++ds)
        kf[4 * t + ds] = *(const LAS v4u*)(L + L_K + (16 * (4 * t + (n >> 2)) + 4 * cw + (n & 3)) * 256 + (((4 * ds + kg) ^ n) << 4));
    SB();
    {
#pragma unroll 1
      for (unsigned spin = 0; spin < SPIN_G; ++spin) {
        if (all_ok(tagdiff(qv[0], tag) | tagdiff(qv[1], tag) | tagdiff(qv[2], tag) | tagdiff(qv[3], tag))) break;
        __builtin_amdgcn_s_sleep(4);
#pragma unroll
        for (int i = 0; i < 4; ++i) qv[i] = bld<AUX_SC1>(rws, qo, i * 32768);
      }
      const f32x4 t = (get4(qv[0]) + get4(qv[1])) + (get4(qv[2]) + get4(qv[3]));
      float q[4];
#pragma unroll
      for (int e = 0; e < 4; ++e) {
        auto p = __builtin_amdgcn_permlane32_swap(__float_as_uint(t[e]), __float_as_uint(t[e]), false, false);
        q[e] = __uint_as_float(p[0]) + __uint_as_float(p[1]);
      }
      if (lane < 32) *(LAS v2u*)(L + L_QB + cw * 320 + u5 * 8) = v2u{pack2(q[0], q[1]), pack2(q[2], q[3])};
      lds_arrive(FC + F_QCNT, lane);
      lds_wait(FL + F_QCNT, 4u);
    }
    STAMP(6);
    {
      f32x4 sc[2];
      v4u qf[4];
#pragma unroll
      for (int ds = 0; ds < 4; ++ds) qf[ds] = *(const LAS v4u*)(L + L_QB + (n & 3) * 320 + kg * 16 + ds * 64);
      f32x4 c[8];
#pragma unroll
      for (int t = 0; t < 8; ++t) {
        c[t] = f32x4{0.f, 0.f, 0.f, 0.f};
#pragma unroll
        for (int ds = 0; ds < 4; ++ds) c[t] = mfma32(qf[ds], kf[4 * t + ds], c[t]);
      }
#pragma unroll
      for (int sg = 0; sg < 2; ++sg)
#pragma unroll
        for (int h = 0; h < 4; ++h) sc[sg][h] = sel4(lane & 16, lane & 32, c[4 * sg][h], c[4 * sg + 1][h], c[4 * sg + 2][h], c[4 * sg + 3][h]);
      f32x4 e0, e1, mx;
#pragma unroll
      for (int h = 0; h < 4; ++h) {
        const f32x2 r = rbf2(rbf2(f32x2{sc[0][h], sc[1][h]}) * 0.08838834764831845f);
        e0[h] = r.x; e1[h] = r.y; mx[h] = fmx(r.x, r.y);
      }
      const f32x4 mw = wave_red4<true>(mx, lane);
#pragma unroll
      for (int h = 0; h < 4; ++h) {
        e0[h] = fexp(e0[h] - mw[h]); e1[h] = fexp(e1[h] - mw[h]);
        const unsigned pp = pack2(e0[h], e1[h]);
        LAS unsigned short* pd = (LAS unsigned short*)(L + L_P) + ((((cw * 4 + (kg >> 1)) * 4 + h) * 4 + (n & 3)) * 8 + 4 * (kg & 1) + (n >> 2));
        pd[0] = (unsigned short)(pp & 0xFFFFu);
        pd[2 * 128] = (unsigned short)(pp >> 16);
      }
      const f32x4 lw = wave_red4<false>(e0 + e1, lane);
      if (lane < 2) *(LAS f32x4*)(L + L_F + cw * 32 + lane * 16) = lane == 0 ? mw : lw;
      lds_arrive(FC + F_PCNT, lane);
    }
    STAMP(7);
    lds_wait(FL + F_PCNT, 4u);
    if (cw == 3) {
      const LAS f32x4* sp = (const LAS f32x4*)(L + L_F);
      f32x4 M, Ls;
#pragma unroll
      for (int h = 0; h < 4; ++h) {
        M[h] = fmx(fmx(sp[0][h], sp[2][h]), fmx(sp[4][h], sp[6][h]));
        Ls[h] = (sp[1][h] * fexp(sp[0][h] - M[h]) + sp[3][h] * fexp(sp[2][h] - M[h])) + (sp[5][h] * fexp(sp[4][h] - M[h]) + sp[7][h] * fexp(sp[6][h] - M[h]));
      }
      if (lane < 2) st16(rws, WS_ST + (grp * 8 + s) * 128 + lane * 16, put4(lane == 0 ? M : Ls, tag));
    }
#define GU_PTR() ((n & 2) ? wu : wg) + (size_t)(128 * wv + 32 * (n >> 2) + 8 * kg) * FF + 16 * jsl + 8 * (n & 1)
#define GU_LD(j) gur[j] = AUX_W ? __builtin_nontemporal_load((const v4u*)(gup + (j) * FF)) : *(const v4u*)(gup + (j) * FF)
#define GU_RQ(Q, P0, P1) { _Pragma("unroll") for (int p = P0; p < P1; ++p) GU_LD((p + 2 * Q) & 7); }
#define GU_RANGE(P0, P1) { const int q_ = GUROT ? (g >> 3) & 3 : 0; if (q_ == 0) GU_RQ(0, P0, P1) else if (q_ == 1) GU_RQ(1, P0, P1) else if (q_ == 2) GU_RQ(2, P0, P1) else GU_RQ(3, P0, P1) }
#define W_HEAD() ((g & 7) + 8 * ((g - 128) >> 5))
#define W_CQ() ((g >> 3) & 3)
#define ST_OFF() (WS_ST + (((wv & 3) * 8 + (W_HEAD() >> 2)) * 8 + (lane & 7)) * 128)
    const int ru = ((g >> 5) << 3) + (g & 7), jsl = 4 * ru + ((g >> 3) & 3);
    if (g < 128) {
      if (ru < 28) {
        const bf16_t* gup = GU_PTR();
        const int mo = WS_SB + cw * 2048 + lane * 16;
        const int off[4] = {mo, mo + 1024, mo + 8192, mo + 8192 + 1024};
        f32x4 pre[4];
        sweep<4, 8>(rws, off, pre, tag);
#pragma unroll
        for (int i = 0; i < 4; ++i) *(LAS f32x4*)(L + L_SC + (i >> 1) * 8192 + cw * 2048 + (i & 1) * 1024 + lane * 16) = pre[i];
        STAMP(8);
        lds_wait(FL + F_VI + cw);
        GU_RANGE(0, GU_B)
        SB();
      }
    } else {
      const unsigned char* wsrc = (const unsigned char*)(wo + (size_t)(W_HEAD() * 128 + cw * 32) * 512 + W_CQ() * 128);
      const unsigned wdst = (unsigned)(uintptr_t)(L + L_WO) + cw * 8192;
      const int vo = kg * 1024 + n * 16, sto = ST_OFF();
      lds_wait(FL + F_VE + cw);
      const v4u s0 = bld<AUX_SC1>(rws, sto, 0), s1 = bld<AUX_SC1>(rws, sto + 16, 0);
      if (all_ok(tagdiff(s0, tag) | tagdiff(s1, tag))) {
        const float cs = split_weight(get4(s0), get4(s1), W_HEAD());
        if (lane < 8) *(LAS float*)(L + L_CS + cw * 32 + lane * 4) = cs;
        lds_set(FL + F_CS + cw);
      }
      STAMP(8);
      lds_wait(FL + F_VI + cw);
#pragma unroll
      for (int j = 0; j < 8; ++j) {
        unsigned keep;
        asm volatile("s_mov_b32 %0, m0\n\ts_mov_b32 m0, %3\n\ts_nop 0\n\tglobal_load_lds_dwordx4 %1, %2 nt\n\ts_mov_b32 m0, %0"
                     : "=&s"(keep) : "v"(vo), "s"(wsrc + j * 4096), "s"(wdst + j * 1024) : "memory");
      }
    }
  }
  int h1o = WS_H1 + (grp * 8 + s) * 2048 + tid * 16;
  PIN(h1o);
  STAMP(9);
  wg_barrier();
  if (wv < 2) {
    const LAS f32x4* mp = (const LAS f32x4*)(L + L_MRG + tid * 16);
    st16(rws, h1o, put4((mp[0] + mp[128]) + (mp[256] + mp[384]), tag));
  }
  SB();
  STAMP(10);
  if (ABL) { STAMP_OUT(); return; }
  if (g >= 128) {
    const int whead = W_HEAD(), wcq = W_CQ();
    int h2o = WS_H2 + ((kg * 16 + wcq * 4 + wv) * 32 + whead) * 128 + n * 8;
    int oar = L_OA + (n >> 2) * 320 + kg * 16;
    unsigned tg2 = tag >> ((n & 1) * 16), tg1 = tag >> (kg * 8);
    if (wv < 4) {
      asm volatile("s_waitcnt vmcnt(0)" ::: "memory");
      lds_arrive(FC + F_WCNT, lane);
      lds_wait(FL + F_WCNT, 4u);
      unsigned wor[32];
#pragma unroll
      for (int j = 0; j < 32; ++j) wor[j] = *(const LAS unsigned*)(L + L_WO + ((j >> 3) * 32 + 8 * kg + (j & 7)) * 256 + wv * 64 + n * 4);
#pragma unroll
      for (int k = 0; k < 8; ++k) {
        const unsigned sel = (k & 1) ? 0x07060302u : 0x05040100u;
#pragma unroll
        for (int jp = 0; jp < 4; ++jp) gur[k][jp] = __builtin_amdgcn_perm(wor[8 * (k >> 1) + 2 * jp + 1], wor[8 * (k >> 1) + 2 * jp], sel);
        PIN(gur[k]);
      }
      PIN(h2o); PIN(oar); PIN(tg2); PIN(tg1);
      STAMP(5);
    } else {
      const int rb = wv - 4;
      if (__builtin_amdgcn_readfirstlane(FL[F_CS + rb]) == 0u) {
        const int off[2] = {ST_OFF(), ST_OFF() + 16};
        f32x4 v[2];
        sweep<2, 0>(rws, off, v, tag);
        const float c_ = split_weight(v[0], v[1], whead);
        if (lane < 8) *(LAS float*)(L + L_CS + rb * 32 + lane * 4) = c_;
        LGKM0();
      }
      float cs = *(const LAS float*)(L + L_CS + rb * 32 + (lane >> 3) * 4);
      int oaw = L_OA + rb * 320 + n * 8 + kg * 2;
      PIN(cs); PIN(oaw);
      const int ho = WS_H1 + ((rb * 8 + (whead >> 2)) * 8 + (lane >> 3)) * 2048 + (whead & 3) * 512 + (lane & 7) * 16;
      const int off[4] = {ho, ho + 128, ho + 256, ho + 384};
      f32x4 v[4];
      sweep<4, 0>(rws, off, v, tag);
      const float o0 = part_sum(v[0] * cs), o1 = part_sum(v[1] * cs), o2 = part_sum(v[2] * cs), o3 = part_sum(v[3] * cs);
      const unsigned pk = pack2((lane & 8) ? o1 : o0, (lane & 8) ? o3 : o2);
      *(LAS unsigned short*)(L + oaw) = (unsigned short)pk;
      *(LAS unsigned short*)(L + oaw + 128) = (unsigned short)(pk >> 16);
    }
    wg_barrier();
    STAMP(11);
    if (wv < 4) {
      f32x4 c0 = {0.f, 0.f, 0.f, 0.f}, c1 = {0.f, 0.f, 0.f, 0.f};
#pragma unroll
      for (int k = 0; k < 4; ++k) { const v4u a = *(const LAS v4u*)(L + oar + k * 64); c0 = mfma32(a, gur[2 * k], c0); c1 = mfma32(a, gur[2 * k + 1], c1); }
      __builtin_amdgcn_raw_buffer_store_b64(v2u{put1(c0[0], tg2), put1(c1[0], tg2 >> 8)}, rws, h2o, 0, AUX_SC1);
    }
    SB();
    STAMP(12);
    if (g >= 192) {
      const int rr = g - 192, col = 4 * (lane & 7) + kg;
      float x1 = 0.f, gb = 0.f;
      if (wv == 0) {
        float ga = *(const LAS float*)(L + L_GG + col * 4), xin = *(const LAS float*)(L + L_GG + 256 + col * 4);
        gb = *(const LAS float*)(L + L_GG + 128 + col * 4);
        int x1o = WS_X1 + (n >> 3) * 8192 + rr * 128 + col * 4;
        PIN(ga); PIN(xin); PIN(x1o); PIN(gb);
        float a;
        if (H2W == 1) {
          const int ho = WS_H2 + rr * 4096 + lane * 16;
          const int off[4] = {ho, ho + 1024, ho + 2048, ho + 3072};
          f32x4 v[4];
          sweep<4, 0>(rws, off, v, tag);
          a = part_sum((v[0] + v[1]) + (v[2] + v[3]));
        } else {
          wg_barrier();
          const LAS float* rp = (const LAS float*)(L + L_RED + 1024 + col * 4);
          a = (rp[0] + rp[32]) + (rp[64] + rp[96]);
        }
        STAMP(13);
        x1 = rbf(xin + rbf(ga * rbf(a)));
        const unsigned xw = put1(x1, tg1);
        __builtin_amdgcn_raw_buffer_store_b32(xw, rws, x1o, 0, AUX_SC1);
        if (XREP > 2) __builtin_amdgcn_raw_buffer_store_b32(xw, rws, x1o, 16384, AUX_SC1);
        SB();
      }
      else if (H2W != 1) {
        if (wv >= 4) {
          const int off[1] = {WS_H2 + rr * 4096 + (wv - 4) * 1024 + lane * 16};
          f32x4 v[1];
          sweep<1, 0>(rws, off, v, tag);
          *(LAS float*)(L + L_RED + 1024 + (wv - 4) * 128 + col * 4) = part_sum(v[0]);
        }
        wg_barrier();
      }
      STAMP(14);
      if (wv == 7) {
        const int off[1] = {WS_X1 + (g & (XREP - 1)) * 8192 + lane * 128};
        f32x4 v[1];
        sweep<1, 0>(rws, off, v, tag);
        STAMP(13);
        __builtin_amdgcn_s_sleep(DLY_H4 > 0 ? DLY_H4 : 0);
        FL[F_H4] = 1u;
      } else {
        const int off[2] = {WS_H4 + rr * 14336 + wv * 2048 + lane * 16, WS_H4 + rr * 14336 + wv * 2048 + 1024 + lane * 16};
        f32x4 v[2];
        if (DLY_H4 >= 0) lds_wait(FL + F_H4);
        sweep<2, 0>(rws, off, v, tag);
        *(LAS float*)(L + L_RED + wv * 128 + col * 4) = part_sum(v[0] + v[1]);
      }
      wg_barrier();
      if (wv == 0) {
        const LAS float* rp = (const LAS float*)(L + L_RED + col * 4);
        const float a = ((rp[0] + rp[32]) + (rp[64] + rp[96])) + ((rp[128] + rp[160]) + rp[192]);
        const float o = x1 + rbf(gb * rbf(a));
        if (n < 8) __builtin_amdgcn_raw_buffer_store_b16((unsigned short)pack2(o, 0.f), mkrsrc(out, 4096), (rr * 32 + col) * 2, 0, AUX_SC1);
        if (g == 192 && lane == 0) __builtin_amdgcn_raw_buffer_store_b32(tag, rws, WS_EPOCH, 0, AUX_SC1);
      }
      STAMP(15);
    }
  } else {
    const int ru = ((g >> 5) << 3) + (g & 7), jsl = 4 * ru + ((g >> 3) & 3);
    if (ru < 28) {
      __builtin_amdgcn_s_barrier();
      v2u dnr[8];
      if (wv < 4) {
        const bf16_t* gup = GU_PTR();
        GU_RANGE(GU_B, 8)
      }
      {
        const rsrc_t rd = mkrsrc(wd + (size_t)(16 * jsl) * 512, 16384);
        const int dofs = (kg & 1) * 8192 + wv * 128 + n * 8;
#pragma unroll
        for (int j = 0; j < 8; ++j) dnr[j] = __builtin_amdgcn_raw_buffer_load_b64(rd, dofs, j * 1024, AUX_NT);
      }
      if (wv < 4) {
        LAS unsigned char* fb = L + L_FB + ((((n & 3) * 8 * 4 + wv) * 4 + (n >> 2)) * 4 + kg) * 16;
#define TG(E) *(LAS v4u*)(fb + E * 1024) = tcol8<E>(gur);
        FOR8(TG)
#undef TG
      }
      wg_barrier();
      STAMP(12);
      v4u bg[4], bd[2];
#pragma unroll
      for (int t = 0; t < 4; ++t) { bg[t] = *(const LAS v4u*)(L + L_FB + ((((((n >> 1) & 1) * 16 + 2 * wv + (n & 1)) * 4 + t) * 4 + (n >> 2)) * 4 + kg) * 16); PIN(bg[t]); }
      const int rb = wv >> 1, hf = wv & 1;
      f32x4 shv = *(const LAS f32x4*)(L + L_SC + rb * 2048 + hf * 1024 + lane * 16), scv = *(const LAS f32x4*)(L + L_SC + 8192 + rb * 2048 + hf * 1024 + lane * 16);
      PIN(shv); PIN(scv);
      {
        const unsigned sel = (kg >> 1) ? 0x07060302u : 0x05040100u;
#pragma unroll
        for (int m2 = 0; m2 < 2; ++m2) {
#pragma unroll
          for (int jp = 0; jp < 4; ++jp) bd[m2][jp] = __builtin_amdgcn_perm(dnr[2 * jp + 1][m2], dnr[2 * jp][m2], sel);
          PIN(bd[m2]);
        }
      }
      int hbw = L_HB + rb * 1024 + (rb >> 1) * 32 + hf * 512 + lane * 8;
      int hbr = L_HB + (n >> 2) * 1024 + (n >> 3) * 32 + (32 * (n & 3) + 8 * kg) * 2;
      int acr = ((n & 1) == (kg >> 1)) ? L_ACT + (n >> 2) * 32 + (kg & 1) * 16 : L_ZERO;
      int h4o = WS_H4 + ((kg * 16 + 2 * wv + (n >> 3)) * 112 + jsl) * 128 + (n & 7) * 16;
      PIN(hbw); PIN(hbr); PIN(acr); PIN(h4o);
      SB();
      {
        const int xr = WS_X1 + (g & (XREP - 1)) * 8192 + rb * 2048 + lane * 16;
        const int off[2] = {xr + hf * 1024, xr + (hf ^ 1) * 1024};
        f32x4 v[2];
        sweep<2, 0>(rws, off, v, tag);
        STAMP(13);
        const float x0 = __uint_as_float(__builtin_amdgcn_readfirstlane(__float_as_uint(0.5f * (v[0][0] + v[1][0]))));
        const f32x4 d0 = v[0] - x0, d1 = v[1] - x0;
        const f32x4 sa = d0 + d1, sb2 = d0 * d0 + d1 * d1;
        float s1 = (sa[0] + sa[1]) + (sa[2] + sa[3]), s2 = (sb2[0] + sb2[1]) + (sb2[2] + sb2[3]);
        s1 = wave_sum(s1); s2 = wave_sum(s2);
        const float md = s1 * (1.f / 512.f), var = fmx(s2 * (1.f / 512.f) - md * md, 0.f);
        const float mb = rbf(x0 + md), rden = __builtin_amdgcn_rcpf(rbf(__builtin_amdgcn_sqrtf(rbf(rbf(var) + 1e-5f))));
        const f32x2 ha = rbf2(rbf2(rbf2(f32x2{v[0][0], v[0][1]} - mb) * rden) * f32x2{scv[0], scv[1]}) + f32x2{shv[0], shv[1]};
        const f32x2 hb = rbf2(rbf2(rbf2(f32x2{v[0][2], v[0][3]} - mb) * rden) * f32x2{scv[2], scv[3]}) + f32x2{shv[2], shv[3]};
        *(LAS v2u*)(L + hbw) = v2u{pack2(ha.x, ha.y), pack2(hb.x, hb.y)};
      }
      wg_barrier();
      {
        f32x4 c = {0.f, 0.f, 0.f, 0.f};
#pragma unroll
        for (int t = 0; t < 4; ++t) c = mfma32(*(const LAS v4u*)(L + hbr + t * 256), bg[t], c);
        float gs = sel4(lane & 4, lane & 8, c[0], c[1], c[2], c[3]);
        gs += dpp<ROR4>(gs); gs += dpp<ROR8>(gs);
        gs = rbf(gs);
        const float act = rbf(gelu_f(gs)) * dpp<XOR2>(gs);
        const unsigned pk = pack2(act, dpp<XOR1>(act));
        if (n == 0) *(LAS unsigned*)(L + L_ACT + kg * 32 + wv * 4) = pk;
      }
      wg_barrier();
      STAMP(14);
      {
        const v4u a = *(const LAS v4u*)(L + acr);
        const f32x4 c0 = mfma32(a, bd[0], f32x4{0.f, 0.f, 0.f, 0.f}), c1 = mfma32(a, bd[1], f32x4{0.f, 0.f, 0.f, 0.f});
        st16(rws, h4o, put4(f32x4{c0[0], c0[1], c1[0], c1[1]}, tag));
      }
      STAMP(15);
    }
  }
  STAMP_OUT();
}

extern "C" void kernel_launch(void* const* d_in, const int* in_sizes, int n_in, void* d_out, int out_size, void* d_ws, size_t ws_size, hipStream_t stream) {
  adaln_block_kernel<<<256, 512, 0, stream>>>((const bf16_t*)d_in[0], (const bf16_t*)d_in[1], (const bf16_t*)d_in[2], (const bf16_t*)d_in[3],
                                              (const bf16_t*)d_in[4], (const bf16_t*)d_in[6], (const bf16_t*)d_in[7], (const bf16_t*)d_in[8],
                                              (const bf16_t*)d_in[9], (const bf16_t*)d_in[10], (bf16_t*)d_out, (unsigned char*)d_ws);
}
```

```cpp
#include <hip/hip_runtime.h>
#include <hip/hip_bf16.h>
#include <stdint.h>

#define GAS __attribute__((address_space(1)))
#define LAS __attribute__((address_space(3)))
typedef unsigned short bf16_t;
typedef unsigned v4u __attribute__((ext_vector_type(4)));
typedef unsigned v2u __attribute__((ext_vector_type(2)));
typedef float f32x4 __attribute__((ext_vector_type(4)));
typedef float f32x2 __attribute__((ext_vector_type(2)));
typedef __bf16 bf16x8 __attribute__((ext_vector_type(8)));
typedef __bf16 bf16x2 __attribute__((ext_vector_type(2)));
typedef short s16x4 __attribute__((ext_vector_type(4)));
typedef __amdgpu_buffer_rsrc_t rsrc_t;

#define PLO 4
#define PHI 8
#define SCH_A 110
#define SCH_F 60
#define SCH_G (-55)
#define SCH_T16 250
#define T_MOD 380
#define T_MODF 190
#define T_Q 490
#define GU_B 0
#define DLY_H4 32
#define VE_AT 26
#define XREP 4
#define WO_DLY 0
#define ANCH 0
#define T_Q0 640
#define GDLY 12
#define H2DLY 14
#define GU_DLY 0
#define NOROT 0
#define ABL 0

namespace {
constexpr int SK = 16384, WIN = 4096, FF = 1792;
constexpr int AUX_SC1 = 16, AUX_NT = 2;
constexpr int WS_EPOCH = 0;
constexpr int WS_MT = 2048;
constexpr int WS_X1 = 4096;
constexpr int WS_SB = WS_X1 + 32768;
constexpr int WS_CB = WS_SB + 8192;
constexpr int WS_ST = WS_CB + 8192;
constexpr int WS_MOD = WS_ST + 32768;
constexpr int WS_QP = WS_MOD + 196608;
constexpr int WS_H1 = WS_QP + 524288;
constexpr int WS_H2 = WS_H1 + 524288;
constexpr int WS_H4 = WS_H2 + 262144;
constexpr int WS_END = WS_H4 + 917504;
constexpr int WS_STAMP = 8 << 20;
constexpr int L_K = 0;
constexpr int L_FB = 0;
constexpr int L_WO = 0;
constexpr int L_SC = 32768;
constexpr int L_DN = 49152;
constexpr int L_P = 131072;
constexpr int L_QB = L_P + 4096;
constexpr int L_HQ = L_QB + 1280;
constexpr int L_ZERO = L_HQ + 1024;
constexpr int L_FLAG = L_ZERO + 1024;
constexpr int L_GG = L_FLAG + 256;
constexpr int L_OA = L_GG + 384;
constexpr int L_CS = L_OA + 1280;
constexpr int L_F = L_CS + 128;
constexpr int L_ACT = L_F + 128;
constexpr int L_RED = L_ACT + 128;
constexpr int L_MRG = L_RED + 2048;
constexpr int L_HB = L_MRG + 8192;
constexpr int L_END = L_HB + 4096 + 64;
enum { F_MCNT = 0, F_HCNT = 1, F_QCNT = 2, F_K = 4, F_PCNT = 8, F_G = 10, F_H4 = 12, F_WCNT = 13, F_VI = 16, F_VE = 20, F_CS = 24, F_ANC = 28 };
constexpr unsigned SPIN_G = 1u << 16, SPIN_L = 1u << 20;

__device__ __forceinline__ float bflo(unsigned u) { return __uint_as_float(u << 16); }
__device__ __forceinline__ float bfhi(unsigned u) { return __uint_as_float(u & 0xFFFF0000u); }
__device__ __forceinline__ unsigned pack2(float lo, float hi) {
  f32x2 v = {lo, hi};
  return __builtin_bit_cast(unsigned, __builtin_convertvector(v, bf16x2));
}
__device__ __forceinline__ float dot2(unsigned a, unsigned b, float c) {
  return __builtin_amdgcn_fdot2_f32_bf16(__builtin_bit_cast(bf16x2, a), __builtin_bit_cast(bf16x2, b), c, false);
}
__device__ __forceinline__ float rbf(float x) { return bflo(pack2(x, x) & 0xFFFFu); }
__device__ __forceinline__ float fmx(float a, float b) { return __builtin_amdgcn_fmed3f(a, b, __builtin_inff()); }
template <int CTRL> __device__ __forceinline__ float dpp(float x) {
  return __builtin_bit_cast(float, __builtin_amdgcn_mov_dpp(__builtin_bit_cast(int, x), CTRL, 0xf, 0xf, true));
}
constexpr int XOR1 = 0xB1, XOR2 = 0x4E, HMIR = 0x141, ROR2 = 0x122, ROR4 = 0x124, ROR8 = 0x128;
__device__ __forceinline__ float xrow_sum(float x) {
  auto s = __builtin_amdgcn_permlane16_swap(__float_as_uint(x), __float_as_uint(x), false, false);
  x = __uint_as_float(s[0]) + __uint_as_float(s[1]);
  auto t = __builtin_amdgcn_permlane32_swap(__float_as_uint(x), __float_as_uint(x), false, false);
  return __uint_as_float(t[0]) + __uint_as_float(t[1]);
}
__device__ __forceinline__ float xrow_max(float x) {
  auto s = __builtin_amdgcn_permlane16_swap(__float_as_uint(x), __float_as_uint(x), false, false);
  x = fmx(__uint_as_float(s[0]), __uint_as_float(s[1]));
  auto t = __builtin_amdgcn_permlane32_swap(__float_as_uint(x), __float_as_uint(x), false, false);
  return fmx(__uint_as_float(t[0]), __uint_as_float(t[1]));
}
__device__ __forceinline__ float wave_sum(float x) {
  x += dpp<XOR1>(x); x += dpp<XOR2>(x); x += dpp<ROR4>(x); x += dpp<ROR8>(x);
  return xrow_sum(x);
}
__device__ __forceinline__ f32x4 mfma32(v4u a, v4u b, f32x4 c) {
  return __builtin_amdgcn_mfma_f32_16x16x32_bf16(__builtin_bit_cast(bf16x8, a), __builtin_bit_cast(bf16x8, b), c, 0, 0, 0);
}
template <int E> __device__ __forceinline__ v4u tcol8(const v4u* r) {
  constexpr unsigned sel = (E & 1) ? 0x07060302u : 0x05040100u;
  v4u o;
  o.x = __builtin_amdgcn_perm(r[1][E >> 1], r[0][E >> 1], sel);
  o.y = __builtin_amdgcn_perm(r[3][E >> 1], r[2][E >> 1], sel);
  o.z = __builtin_amdgcn_perm(r[5][E >> 1], r[4][E >> 1], sel);
  o.w = __builtin_amdgcn_perm(r[7][E >> 1], r[6][E >> 1], sel);
  return o;
}
__device__ __forceinline__ v4u tcol8r(const v4u* r, int e) {
  const unsigned sel = (e & 1) ? 0x07060302u : 0x05040100u;
  v4u o;
  o.x = __builtin_amdgcn_perm(r[1][e >> 1], r[0][e >> 1], sel);
  o.y = __builtin_amdgcn_perm(r[3][e >> 1], r[2][e >> 1], sel);
  o.z = __builtin_amdgcn_perm(r[5][e >> 1], r[4][e >> 1], sel);
  o.w = __builtin_amdgcn_perm(r[7][e >> 1], r[6][e >> 1], sel);
  return o;
}
#define FOR8(X) X(0) X(1) X(2) X(3) X(4) X(5) X(6) X(7)
__device__ __forceinline__ v2u lds_tr(LAS unsigned char* p) { return __builtin_bit_cast(v2u, __builtin_amdgcn_ds_read_tr16_b64_v4i16((LAS s16x4*)p)); }
__device__ __forceinline__ rsrc_t mkrsrc(const void* p, int bytes) { return __builtin_amdgcn_make_buffer_rsrc((void*)p, 0, bytes, 0x00020000); }
template <int AUX> __device__ __forceinline__ v4u bld(rsrc_t r, int voff, int soff) { return __builtin_amdgcn_raw_buffer_load_b128(r, voff, soff, AUX); }
__device__ __forceinline__ unsigned put1(float v, unsigned t) { return (__float_as_uint(v) & ~15u) | (t & 15u); }
__device__ __forceinline__ v4u put4(f32x4 v, unsigned tag) { return v4u{put1(v.x, tag), put1(v.y, tag >> 8), put1(v.z, tag >> 16), put1(v.w, tag >> 24)}; }
__device__ __forceinline__ unsigned tagdiff(v4u v, unsigned tag) {
  return (__builtin_amdgcn_perm(v.y, v.x, 0x0c0c0400u) | __builtin_amdgcn_perm(v.w, v.z, 0x04000c0cu)) ^ tag;
}
__device__ __forceinline__ bool all_ok(unsigned diff) { return __all((diff & 0x0F0F0F0Fu) == 0u); }
__device__ __forceinline__ f32x4 get4(v4u v) { return __builtin_bit_cast(f32x4, v & ~15u); }
__device__ __forceinline__ void st16(rsrc_t r, int off, v4u v) { __builtin_amdgcn_raw_buffer_store_b128(v, r, off, 0, AUX_SC1); }
#define CBAR() asm volatile("" ::: "memory")
#define LGKM0() asm volatile("s_waitcnt lgkmcnt(0)" ::: "memory")
#define SB() __builtin_amdgcn_sched_barrier(0)
#define PIN(x) asm volatile("" : "+v"(x))
__device__ __forceinline__ void lds_wait(volatile LAS unsigned* f, unsigned want = 1u) {
#pragma unroll 1
  for (unsigned i = 0; i < SPIN_L; ++i) { if (*f >= want) break; __builtin_amdgcn_s_sleep(1); }
  CBAR();
}
__device__ __forceinline__ void lds_set(volatile LAS unsigned* f) { LGKM0(); *f = 1u; }
__device__ __forceinline__ void lds_arrive(LAS unsigned* f, int lane) {
  LGKM0();
  if (lane == 0) __hip_atomic_fetch_add(f, 1u, __ATOMIC_RELAXED, __HIP_MEMORY_SCOPE_WORKGROUP);
}
__device__ __forceinline__ void wg_barrier() { LGKM0(); __builtin_amdgcn_s_barrier(); CBAR(); }
template <int N, int SLEEP> __device__ __forceinline__ void sweep_raw(rsrc_t rs, const int (&off)[N], v4u (&v)[N], unsigned tag) {
  unsigned spin = 0;
  unsigned long long more;
#pragma unroll 1
  do {
    if (SLEEP && spin) __builtin_amdgcn_s_sleep(SLEEP);
    CBAR();
#pragma unroll
    for (int i = 0; i < N; ++i) v[i] = bld<AUX_SC1>(rs, off[i], 0);
    ++spin;
    unsigned mask = 0x0F0F0F0Fu & (unsigned)((int)(spin - SPIN_G) >> 31);
    asm volatile("" : "+s"(mask));
    unsigned bad = 0;
#pragma unroll
    for (int i = 0; i < N; ++i) bad |= tagdiff(v[i], tag);
    more = __builtin_amdgcn_ballot_w64((bad & mask) != 0u);
  } while (more);
}
template <int N, int SLEEP> __device__ __forceinline__ void sweep(rsrc_t rs, const int (&off)[N], f32x4 (&o)[N], unsigned tag) {
  v4u v[N];
  sweep_raw<N, SLEEP>(rs, off, v, tag);
#pragma unroll
  for (int i = 0; i < N; ++i) o[i] = get4(v[i]);
}
__device__ __forceinline__ float fexp(float x) { return __builtin_amdgcn_exp2f(x * 1.4426950408889634f); }
__device__ __forceinline__ float silu_f(float x) { return x * __builtin_amdgcn_rcpf(1.f + fexp(-x)); }
__device__ __forceinline__ float gelu_f(float x) {
  const float t = x * fmaf(x * x, -0.10294324f, -2.3022082f);
  return x * __builtin_amdgcn_rcpf(1.f + __builtin_amdgcn_exp2f(t));
}
__device__ __forceinline__ void tcol4x8(const v2u* r, v4u* o) {
#pragma unroll
  for (int e = 0; e < 4; ++e) {
    const unsigned sel = (e & 1) ? 0x07060302u : 0x05040100u;
#pragma unroll
    for (int jp = 0; jp < 4; ++jp) o[e][jp] = __builtin_amdgcn_perm(r[2 * jp + 1][e >> 1], r[2 * jp][e >> 1], sel);
    PIN(o[e]);
  }
}
__device__ __forceinline__ f32x4 gemv32x64(v4u a, const v4u* bt) {
  f32x4 c[4];
#pragma unroll
  for (int e = 0; e < 4; ++e) c[e] = mfma32(a, bt[e], f32x4{0.f, 0.f, 0.f, 0.f});
  return f32x4{c[0][0], c[1][0], c[2][0], c[3][0]};
}
__device__ __forceinline__ void wait_until(unsigned long long t0, unsigned ticks) {
#pragma unroll 1
  for (int i = 0; i < 4096 && __builtin_amdgcn_s_memrealtime() - t0 < ticks; ++i) __builtin_amdgcn_s_sleep(1);
}
__device__ __forceinline__ float sel4(bool b0, bool b1, float c0, float c1, float c2, float c3) {
  const float lo = b0 ? c1 : c0, hi = b0 ? c3 : c2;
  return b1 ? hi : lo;
}
__device__ __forceinline__ float part_sum(f32x4 v) {
  auto p = __builtin_amdgcn_permlane32_swap(__float_as_uint(v[0]), __float_as_uint(v[2]), false, false);
  auto q = __builtin_amdgcn_permlane32_swap(__float_as_uint(v[1]), __float_as_uint(v[3]), false, false);
  const float A = __uint_as_float(p[0]) + __uint_as_float(p[1]), B = __uint_as_float(q[0]) + __uint_as_float(q[1]);
  auto r = __builtin_amdgcn_permlane16_swap(__float_as_uint(A), __float_as_uint(B), false, false);
  const float t = __uint_as_float(r[0]) + __uint_as_float(r[1]);
  return t + dpp<ROR8>(t);
}
__device__ __forceinline__ float split_weight(f32x4 m4, f32x4 l4, int hh) {
  const bool h0 = hh & 1, h1 = hh & 2;
  const float m = sel4(h0, h1, m4[0], m4[1], m4[2], m4[3]), l = sel4(h0, h1, l4[0], l4[1], l4[2], l4[3]);
  const float M = xrow_max(fmx(m, dpp<ROR8>(m)));
  const float e = fexp(m - M);
  float Ls = l * e; Ls += dpp<ROR8>(Ls);
  return e * __builtin_amdgcn_rcpf(xrow_sum(Ls));
}
__device__ __forceinline__ f32x2 rbf2(f32x2 v) {
  const unsigned p = pack2(v.x, v.y);
  return f32x2{bflo(p), bfhi(p)};
}
template <bool MAX> __device__ __forceinline__ float red2(float a, float b) { return MAX ? fmx(a, b) : a + b; }
template <bool MAX> __device__ __forceinline__ f32x4 wave_red4(f32x4 s, int lane) {
  const bool o1 = lane & 1, o2 = lane & 2;
  const float a = red2<MAX>(o1 ? s[1] : s[0], dpp<XOR1>(o1 ? s[0] : s[1])), c = red2<MAX>(o1 ? s[3] : s[2], dpp<XOR1>(o1 ? s[2] : s[3]));
  float m = red2<MAX>(o2 ? c : a, dpp<XOR2>(o2 ? a : c));
  m = red2<MAX>(m, dpp<ROR4>(m)); m = red2<MAX>(m, dpp<ROR8>(m));
  m = MAX ? xrow_max(m) : xrow_sum(m);
  return f32x4{dpp<0x00>(m), dpp<0x55>(m), dpp<0xAA>(m), dpp<0xFF>(m)};
}
#define STAMP(i) do {} while (0)
#define STAMP_OUT() do {} while (0)
}

__global__ void __launch_bounds__(512, 2)
adaln_block_kernel(const bf16_t* __restrict__ x, const bf16_t* __restrict__ kc, const bf16_t* __restrict__ vc,
                   const bf16_t* __restrict__ wq, const bf16_t* __restrict__ wo, const bf16_t* __restrict__ wg,
                   const bf16_t* __restrict__ wu, const bf16_t* __restrict__ wd, const bf16_t* __restrict__ cc,
                   const bf16_t* __restrict__ wmod, bf16_t* __restrict__ out, unsigned char* ws) {
  __shared__ __attribute__((aligned(16))) unsigned char lds_raw[L_END];
  LAS unsigned char* L = (LAS unsigned char*)lds_raw;
  volatile LAS unsigned* FL = (volatile LAS unsigned*)(L + L_FLAG);
  LAS unsigned* FC = (LAS unsigned*)(L + L_FLAG);
  const unsigned long long tstart = __builtin_amdgcn_s_memrealtime();
  const int tid = threadIdx.x, lane = tid & 63, wv = __builtin_amdgcn_readfirstlane(tid >> 6);
  const int g = blockIdx.x, grp = (g & 7) * 4 + (g >> 6), b = grp >> 3, kvh = grp & 7, s = (g >> 3) & 7;
  const int n = lane & 15, kg = lane >> 4;
  if (tid < 80) *(LAS v4u*)(L + L_ZERO + tid * 16) = v4u{0u, 0u, 0u, 0u};
  asm volatile("" :: "s"(x), "s"(kc), "s"(vc), "s"(wq), "s"(cc), "s"(wmod), "s"(ws));
  wg_barrier();
  const rsrc_t rws = mkrsrc(ws, WS_END);
  unsigned tag;

  if (__builtin_expect(wv >= 4, 1)) {
    const int w = wv - 4;
    const size_t kvbase = ((size_t)grp * SK + (SK - WIN) + s * 512) * 128;
    const int rot = (g * 37) & 127;
    const unsigned char* kbase = (const unsigned char*)(kc + kvbase);
    const unsigned ldsk = (unsigned)(uintptr_t)(L + L_K);
    const int klane = kg * 256 + ((n ^ kg) << 4);
    unsigned due0 = (unsigned)tstart + SCH_A;
#define PACE_WAIT(now, i) do { \
      asm volatile("s_waitcnt vmcnt(%0)" :: "i"(PHI) : "memory"); \
      if (PHI > PLO && (int)((now) - due0) < (((i) * SCH_T16) >> 4)) asm volatile("s_waitcnt vmcnt(%0)" :: "i"(PLO) : "memory"); } while (0)
    STAMP(7);
#pragma unroll 4
    for (int i = 0; i < 32; ++i) {
      const int phys = (4 * i + w + rot) & 127;
      const unsigned char* sb_ = kbase + phys * 1024;
      const int vo = klane ^ ((i & 3) << 6);
      const unsigned dst = ldsk + (4 * i + w) * 1024;
      unsigned keep;
      asm volatile("s_mov_b32 %0, m0\n\ts_mov_b32 m0, %3\n\ts_nop 0\n\tglobal_load_lds_dwordx4 %1, %2 nt\n\ts_mov_b32 m0, %0"
                   : "=&s"(keep) : "v"(vo), "s"(sb_), "s"(dst) : "memory");
      const unsigned now = (unsigned)__builtin_amdgcn_s_memrealtime();
      if (ANCH && i == PLO + 1 && (int)(now - SCH_F - due0) > 0) due0 = now - SCH_F;
      PACE_WAIT(now, i);
    }
    STAMP(1);
    if (ANCH) { const unsigned anc = __builtin_amdgcn_readfirstlane(FL[F_ANC]); if (anc) due0 = anc + SCH_G; }
    const unsigned epoch = __builtin_amdgcn_raw_buffer_load_b32(rws, WS_EPOCH, 0, AUX_SC1);
    const rsrc_t rv = mkrsrc(vc + kvbase, 131072);
    v4u V[32], VT[24], VL[8];
    constexpr int TR0 = 8 + PHI;
#pragma unroll
    for (int i = 0; i < 32; ++i) {
      V[i] = bld<AUX_NT>(rv, lane * 16, ((4 * i + w + rot) & 127) * 1024);
      const unsigned now = (unsigned)__builtin_amdgcn_s_memrealtime();
      if (i >= TR0) { const int k = i - TR0; VT[k] = tcol8r(V + 8 * (k >> 3), k & 7); PIN(VT[k]); }
      if (i == PLO - 1) { asm volatile("s_waitcnt vmcnt(%0)" :: "i"(PLO) : "memory"); FL[F_K + w] = 1u; STAMP(2); }
      else if (i < 31) PACE_WAIT(now, 32 + i);
      if (i == VE_AT) FL[F_VE + w] = 1u;
      SB();
    }
    FL[F_VI + w] = 1u;
    tag = __builtin_amdgcn_readfirstlane(epoch) + 0x01010101u;
    STAMP(3);
#pragma unroll
    for (int k = 32 - TR0; k < 24; ++k) { VT[k] = tcol8r(V + 8 * (k >> 3), k & 7); PIN(VT[k]); }
    lds_wait(FL + F_PCNT, 4u);
    STAMP(4);
    f32x4 f;
    {
      const LAS f32x4* sp = (const LAS f32x4*)(L + L_F);
      const f32x4 m0 = sp[0], m1 = sp[2], m2 = sp[4], m3 = sp[6], mo = sp[2 * w];
#pragma unroll
      for (int h = 0; h < 4; ++h) f[h] = fexp(mo[h] - fmx(fmx(m0[h], m1[h]), fmx(m2[h], m3[h])));
    }
    f32x4 acc[2] = {f32x4{0.f, 0.f, 0.f, 0.f}, f32x4{0.f, 0.f, 0.f, 0.f}};
    int paddr[4];
#pragma unroll
    for (int ep = 0; ep < 4; ++ep) paddr[ep] = (n >> 2) == ep ? L_P + w * 1024 + (((n & 3) * 4 + kg) << 4) : L_ZERO;
    int mra = L_MRG + (w * 512 + 8 * n + 2 * kg) * 4;
#pragma unroll
    for (int G = 0; G < 4; ++G) {
      v4u pa[4];
#pragma unroll
      for (int ep = 0; ep < 4; ++ep) pa[ep] = *(const LAS v4u*)(L + paddr[ep] + G * 256);
      if (G == 3) {
        PIN(pa[0]); PIN(pa[1]); PIN(pa[2]); PIN(pa[3]); PIN(f); PIN(mra); PIN(acc[0]); PIN(acc[1]);
        STAMP(5);
#pragma unroll
        for (int jp = 0; jp < 4; ++jp) {
#pragma unroll
          for (int e = 0; e < 8; ++e) {
            unsigned t = __builtin_amdgcn_perm(V[25 + 2 * jp][e >> 1], V[24 + 2 * jp][e >> 1], (e & 1) ? 0x07060302u : 0x05040100u);
            if (jp < 3) PIN(t);
            VL[e][jp] = t;
          }
          if (jp < 3) SB();
        }
      }
#define PV(E) acc[E & 1] = mfma32(pa[E >> 1], G < 3 ? VT[8 * G + E] : VL[E], acc[E & 1]);
      FOR8(PV)
#undef PV
      SB();
    }
    STAMP(6);
#pragma unroll
    for (int h = 0; h < 4; ++h) *(LAS f32x2*)(L + mra + h * 512) = f32x2{acc[0][h], acc[1][h]} * f[h];
  } else if (ABL == 6) {
    lds_arrive(FC + F_PCNT, lane); tag = 0;
  } else {
    const int cw = wv;
    __builtin_amdgcn_s_setprio(2);
    const int ks = g >> 5, ns = g & 31, u5 = lane & 31, h5 = lane >> 5;
    const int mkq = g < 64 ? g >> 4 : (g - 64) >> 5, mcb = g < 64 ? g & 15 : 16 + ((g - 64) & 31);
    v2u wm[8], wr[8];
    const rsrc_t rm = mkrsrc(wmod + (size_t)(mkq * 128 + cw * 32) * 3072, g < 192 ? 32 * 6144 : 0);
    const rsrc_t rq = mkrsrc(wq + (size_t)(ks * 64 + (cw & 1) * 32) * 4096, 32 * 8192);
    const v4u cf = *(const v4u*)(cc + (n >> 2) * 512 + ((128 * mkq) & 511) + 32 * cw + 8 * kg);
#pragma unroll
    for (int j = 0; j < 8; ++j) wm[j] = __builtin_amdgcn_raw_buffer_load_b64(rm, 8 * kg * 6144 + mcb * 128 + n * 8, j * 6144, AUX_NT);
    const unsigned epoch = __builtin_amdgcn_raw_buffer_load_b32(rws, WS_EPOCH, 0, AUX_SC1);
    const v4u xs = *(const v4u*)(x + cw * 512 + lane * 8);
    const v2u xo = *(const v2u*)(x + cw * 512 + 64 * ks + 4 * n);
#pragma unroll
    for (int j = 0; j < 8; ++j) wr[j] = __builtin_amdgcn_raw_buffer_load_b64(rq, 8 * kg * 8192 + ns * 256 + (cw >> 1) * 128 + n * 8, j * 8192, AUX_NT);
    SB();
    STAMP(1);
    v4u bq[4];
    unsigned long long tfirst;
    {
      v4u a, bt[4];
#pragma unroll
      for (int i = 0; i < 4; ++i) a[i] = pack2(silu_f(bflo(cf[i])), silu_f(bfhi(cf[i])));
      PIN(a);
      tcol4x8(wm, bt);
      tfirst = __builtin_amdgcn_s_memrealtime();
      *(LAS f32x4*)(L + L_MRG + cw * 1024 + lane * 16) = gemv32x64(a, bt);
      lds_arrive(FC + F_MCNT, lane);
      tag = __builtin_amdgcn_readfirstlane(epoch) + 0x01010101u;
      if (cw == 0 && g < 192) {
        lds_wait(FL + F_MCNT, 4u);
        const LAS f32x4* mp = (const LAS f32x4*)(L + L_MRG + lane * 16);
        st16(rws, WS_MOD + ((mkq * 4 + kg) * 3072 + 64 * mcb + 4 * n) * 4, put4((mp[0] + mp[64]) + (mp[128] + mp[192]), tag));
        if (ANCH && g < 64 && lane == 0) { const float t = __uint_as_float((unsigned)__builtin_amdgcn_s_memrealtime() << 4); st16(rws, WS_MT + g * 16, put4(f32x4{t, t, t, t}, tag)); }
        STAMP(11);
      }
    }
    float ln[4];
    {
      float sum = 0.f;
#pragma unroll
      for (int i = 0; i < 4; ++i) sum += bflo(xs[i]) + bfhi(xs[i]);
      const float mean = wave_sum(sum) * (1.f / 512.f);
      float sq = 0.f;
#pragma unroll
      for (int i = 0; i < 4; ++i) { const float d0 = bflo(xs[i]) - mean, d1 = bfhi(xs[i]) - mean; sq = fmaf(d0, d0, sq); sq = fmaf(d1, d1, sq); }
      sq = wave_sum(sq);
      const float mb = rbf(mean), den = rbf(sqrtf(rbf(rbf(sq * (1.f / 512.f)) + 1e-5f)));
#pragma unroll
      for (int i = 0; i < 2; ++i) { ln[2 * i] = rbf(rbf(bflo(xo[i]) - mb) / den); ln[2 * i + 1] = rbf(rbf(bfhi(xo[i]) - mb) / den); }
    }
    tcol4x8(wr, bq);
    STAMP(2);
    wait_until(tstart, T_MOD);
    if (ANCH) wait_until(tfirst, T_MODF);
    {
      const int mo = WS_MOD + ((h5 * 4 + cw) * 3072 + (kg & 1) * 512 + 64 * ks + 4 * n) * 4;
      const int off[2] = {mo, mo + 2 * 4 * 3072 * 4};
      f32x4 v[2];
      v4u mt;
      if (ANCH && cw == 3) mt = bld<AUX_SC1>(rws, WS_MT + lane * 16, 0);
      sweep<2, 8>(rws, off, v, tag);
      STAMP(3);
      if (ANCH && cw == 3) {
        const unsigned now = (unsigned)__builtin_amdgcn_s_memrealtime();
        const int d = (int)(mt.x - (now << 4)) >> 4;
        const bool ok = (tagdiff(mt, tag) & 0x0F0F0F0Fu) == 0u && d <= 0 && d > -2000;
        const float cnt = wave_sum(ok ? 1.f : 0.f), sum = wave_sum(ok ? (float)d : 0.f);
        FL[F_ANC] = (now + (cnt > 0.f ? (int)(sum / cnt) : -300)) | 1u;
      }
      const f32x4 t = v[0] + v[1];
      float h[4];
#pragma unroll
      for (int e = 0; e < 4; ++e) {
        auto p = __builtin_amdgcn_permlane32_swap(__float_as_uint(t[e]), __float_as_uint(t[e]), false, false);
        const float u = rbf(__uint_as_float(p[0]) + __uint_as_float(p[1]));
        auto r = __builtin_amdgcn_permlane16_swap(__float_as_uint(u), __float_as_uint(u), false, false);
        h[e] = rbf(ln[e] * rbf(1.f + __uint_as_float(r[1]))) + __uint_as_float(r[0]);
      }
      if (lane < 16) *(LAS v2u*)(L + L_HQ + cw * 128 + n * 8) = v2u{pack2(h[0], h[1]), pack2(h[2], h[3])};
      lds_arrive(FC + F_HCNT, lane);
      lds_wait(FL + F_HCNT, 4u);
      const v4u a = *(const LAS v4u*)(L + L_HQ + (n >> 2) * 128 + (cw & 1) * 64 + kg * 16);
      const f32x4 qp = gemv32x64(a, bq);
      if (cw & 1) {
        *(LAS f32x4*)(L + L_MRG + 4096 + (cw >> 1) * 1024 + lane * 16) = qp;
        lds_set(FL + F_G + (cw >> 1));
      } else {
        lds_wait(FL + F_G + (cw >> 1));
        st16(rws, WS_QP + ((kg * 8 + ks) * 4096 + 128 * ns + 64 * (cw >> 1) + 4 * n) * 4, put4(qp + *(const LAS f32x4*)(L + L_MRG + 4096 + (cw >> 1) * 1024 + lane * 16), tag));
      }
    }
    STAMP(4);
    if (cw == 3 && g >= 192) {
      const int rr = g - 192;
      const int mo = WS_MOD + ((((lane >> 3) & 3) * 4 + (rr >> 4)) * 3072 + 1024 + h5 * 512 + 32 * (rr & 15) + 4 * (lane & 7)) * 4;
      const int off[2] = {mo, mo + 4096};
      f32x4 v[2];
      const unsigned short xin = x[rr * 32 + u5];
      sweep<2, 8>(rws, off, v, tag);
#pragma unroll
      for (int i = 0; i < 2; ++i)
#pragma unroll
        for (int e = 0; e < 4; ++e) {
          float u = v[i][e]; u += dpp<ROR8>(u);
          auto p = __builtin_amdgcn_permlane16_swap(__float_as_uint(u), __float_as_uint(u), false, false);
          v[i][e] = rbf(__uint_as_float(p[0]) + __uint_as_float(p[1]));
        }
      *(LAS float*)(L + L_GG + 256 + u5 * 4) = bflo(xin);
      if (u5 < 8) {
        if (lane < 32) {
          *(LAS f32x4*)(L + L_GG + lane * 16) = v[0];
          st16(rws, WS_CB + rr * 128 + lane * 16, put4(f32x4{rbf(1.f + v[1][0]), rbf(1.f + v[1][1]), rbf(1.f + v[1][2]), rbf(1.f + v[1][3])}, tag));
        } else {
          *(LAS f32x4*)(L + L_GG + 128 + u5 * 16) = v[1];
          st16(rws, WS_SB + rr * 128 + u5 * 16, put4(v[0], tag));
        }
      }
    }
    const int qo = WS_QP + ((b * 8 + h5) * 4096 + kvh * 512 + cw * 128 + 4 * u5) * 4;
    {
      const unsigned tq = ANCH ? __builtin_amdgcn_readfirstlane(FL[F_ANC]) + T_Q : (unsigned)tstart + T_Q0;
#pragma unroll 1
      for (int i = 0; i < 4096 && (int)((unsigned)__builtin_amdgcn_s_memrealtime() - tq) < 0; ++i) __builtin_amdgcn_s_sleep(1);
    }
    v4u qv[4];
#pragma unroll
    for (int i = 0; i < 4; ++i) qv[i] = bld<AUX_SC1>(rws, qo, i * 32768);
    SB();
    lds_wait(FL + F_K + cw);
    STAMP(5);
    v4u kf[32];
#pragma unroll
    for (int t = 0; t < 8; ++t)
#pragma unroll
      for (int ds = 0; ds < 4; ++ds)
        kf[4 * t + ds] = *(const LAS v4u*)(L + L_K + (16 * (4 * t + (n >> 2)) + 4 * cw + (n & 3)) * 256 + (((4 * ds + kg) ^ n) << 4));
    SB();
    {
#pragma unroll 1
      for (unsigned spin = 0; spin < SPIN_G; ++spin) {
        if (all_ok(tagdiff(qv[0], tag) | tagdiff(qv[1], tag) | tagdiff(qv[2], tag) | tagdiff(qv[3], tag))) break;
        __builtin_amdgcn_s_sleep(8);
#pragma unroll
        for (int i = 0; i < 4; ++i) if (!all_ok(tagdiff(qv[i], tag))) qv[i] = bld<AUX_SC1>(rws, qo, i * 32768);
      }
      const f32x4 t = (get4(qv[0]) + get4(qv[1])) + (get4(qv[2]) + get4(qv[3]));
      float q[4];
#pragma unroll
      for (int e = 0; e < 4; ++e) {
        auto p = __builtin_amdgcn_permlane32_swap(__float_as_uint(t[e]), __float_as_uint(t[e]), false, false);
        q[e] = __uint_as_float(p[0]) + __uint_as_float(p[1]);
      }
      if (lane < 32) *(LAS v2u*)(L + L_QB + cw * 320 + u5 * 8) = v2u{pack2(q[0], q[1]), pack2(q[2], q[3])};
      lds_arrive(FC + F_QCNT, lane);
      lds_wait(FL + F_QCNT, 4u);
    }
    STAMP(6);
    {
      f32x4 sc[2];
      v4u qf[4];
#pragma unroll
      for (int ds = 0; ds < 4; ++ds) qf[ds] = *(const LAS v4u*)(L + L_QB + (n & 3) * 320 + kg * 16 + ds * 64);
      f32x4 c[8];
#pragma unroll
      for (int t = 0; t < 8; ++t) {
        c[t] = f32x4{0.f, 0.f, 0.f, 0.f};
#pragma unroll
        for (int ds = 0; ds < 4; ++ds) c[t] = mfma32(qf[ds], kf[4 * t + ds], c[t]);
      }
#pragma unroll
      for (int sg = 0; sg < 2; ++sg)
#pragma unroll
        for (int h = 0; h < 4; ++h) sc[sg][h] = sel4(lane & 16, lane & 32, c[4 * sg][h], c[4 * sg + 1][h], c[4 * sg + 2][h], c[4 * sg + 3][h]);
      f32x4 e0, e1, mx;
#pragma unroll
      for (int h = 0; h < 4; ++h) {
        const f32x2 r = rbf2(rbf2(f32x2{sc[0][h], sc[1][h]}) * 0.08838834764831845f);
        e0[h] = r.x; e1[h] = r.y; mx[h] = fmx(r.x, r.y);
      }
      const f32x4 mw = wave_red4<true>(mx, lane);
#pragma unroll
      for (int h = 0; h < 4; ++h) {
        e0[h] = fexp(e0[h] - mw[h]); e1[h] = fexp(e1[h] - mw[h]);
        const unsigned pp = pack2(e0[h], e1[h]);
        LAS unsigned short* pd = (LAS unsigned short*)(L + L_P) + ((((cw * 4 + (kg >> 1)) * 4 + h) * 4 + (n & 3)) * 8 + 4 * (kg & 1) + (n >> 2));
        pd[0] = (unsigned short)(pp & 0xFFFFu);
        pd[2 * 128] = (unsigned short)(pp >> 16);
      }
      const f32x4 lw = wave_red4<false>(e0 + e1, lane);
      if (lane < 2) *(LAS f32x4*)(L + L_F + cw * 32 + lane * 16) = lane == 0 ? mw : lw;
      lds_arrive(FC + F_PCNT, lane);
    }
    STAMP(7);
    lds_wait(FL + F_PCNT, 4u);
    if (cw == 3) {
      const LAS f32x4* sp = (const LAS f32x4*)(L + L_F);
      f32x4 M, Ls;
#pragma unroll
      for (int h = 0; h < 4; ++h) {
        M[h] = fmx(fmx(sp[0][h], sp[2][h]), fmx(sp[4][h], sp[6][h]));
        Ls[h] = (sp[1][h] * fexp(sp[0][h] - M[h]) + sp[3][h] * fexp(sp[2][h] - M[h])) + (sp[5][h] * fexp(sp[4][h] - M[h]) + sp[7][h] * fexp(sp[6][h] - M[h]));
      }
      if (lane < 2) st16(rws, WS_ST + (grp * 8 + s) * 128 + lane * 16, put4(lane == 0 ? M : Ls, tag));
    }
#define GU_SRC() (const unsigned char*)(((lane & 2) ? wu : wg) + (size_t)(32 * ((lane >> 2) & 1) + 8 * (lane >> 5) + ((lane >> 3) & 3)) * FF + 16 * jsl + 8 * ((lane ^ (lane >> 4)) & 1))
#define GU_DMA(k_) do { const int kk_ = ((k_) + (NOROT ? 0 : 8 * ((g >> 3) & 3))) & 31; unsigned keep; \
      asm volatile("s_mov_b32 %0, m0\n\ts_mov_b32 m0, %2\n\ts_nop 0\n\tglobal_load_lds_dwordx4 %1, off\n\ts_mov_b32 m0, %0" : "=&s"(keep) \
                   : "v"(gsrc + (64 * (kk_ >> 2) + 4 * ((kk_ >> 1) & 1) + 16 * (kk_ & 1)) * (FF * 2)), "s"((unsigned)(uintptr_t)(L + L_FB) + kk_ * 1024) : "memory"); } while (0)
#define W_HEAD() ((g & 7) + 8 * ((g - 128) >> 5))
#define W_CQ() ((g >> 3) & 3)
#define ST_OFF(row) (WS_ST + (((row) * 8 + (W_HEAD() >> 2)) * 8 + (lane >> 3)) * 128)
    const int ru = ((g >> 5) << 3) + (g & 7), jsl = 4 * ru + ((g >> 3) & 3);
    if (g < 128) {
      if (ru < 28) {
        const unsigned char* gsrc = GU_SRC();
        const int mo = WS_SB + cw * 2048 + lane * 16;
        const int off[4] = {mo, mo + 1024, mo + 8192, mo + 8192 + 1024};
        f32x4 pre[4];
        sweep<4, 8>(rws, off, pre, tag);
#pragma unroll
        for (int i = 0; i < 4; ++i) *(LAS f32x4*)(L + L_SC + (i >> 1) * 8192 + cw * 2048 + (i & 1) * 1024 + lane * 16) = pre[i];
        STAMP(8);
        lds_wait(FL + F_VI + cw);
        if (GU_DLY) __builtin_amdgcn_s_sleep(GU_DLY);
#pragma unroll
        for (int j = 0; j < GU_B; ++j) GU_DMA(4 * j + cw);
        SB();
      }
    } else {
      const unsigned char* wsrc = (const unsigned char*)(wo + (size_t)(W_HEAD() * 128 + cw * 32) * 512 + W_CQ() * 128);
      const unsigned wdst = (unsigned)(uintptr_t)(L + L_WO) + cw * 8192;
      const int vo = kg * 1024 + ((n ^ (kg << 1)) << 4), sto = ST_OFF(cw);
      lds_wait(FL + F_VE + cw);
      const v4u s0 = bld<AUX_SC1>(rws, sto, 0), s1 = bld<AUX_SC1>(rws, sto + 16, 0);
      if (all_ok(tagdiff(s0, tag) | tagdiff(s1, tag))) {
        const float cs = split_weight(get4(s0), get4(s1), W_HEAD());
        *(LAS float*)(L + L_CS + cw * 32 + (lane >> 3) * 4) = cs;
        lds_set(FL + F_CS + cw);
      }
      STAMP(8);
      lds_wait(FL + F_VI + cw);
      if (WO_DLY) __builtin_amdgcn_s_sleep(WO_DLY);
#pragma unroll
      for (int j = 0; j < 8; ++j) {
        unsigned keep;
        asm volatile("s_mov_b32 %0, m0\n\ts_mov_b32 m0, %3\n\ts_nop 0\n\tglobal_load_lds_dwordx4 %1, %2 nt\n\ts_mov_b32 m0, %0"
                     : "=&s"(keep) : "v"(vo ^ ((j & 2) << 6)), "s"(wsrc + j * 4096), "s"(wdst + j * 1024) : "memory");
      }
    }
  }
  int h1o = WS_H1 + (grp * 8 + s) * 2048 + tid * 16;
  PIN(h1o);
  STAMP(9);
  wg_barrier();
  if (wv < 2) {
    const LAS f32x4* mp = (const LAS f32x4*)(L + L_MRG + tid * 16);
    st16(rws, h1o, put4((mp[0] + mp[128]) + (mp[256] + mp[384]), tag));
  }
  SB();
  STAMP(10);
  if (ABL) { STAMP_OUT(); return; }
  if (g >= 128) {
    const int whead = W_HEAD(), wcq = W_CQ();
    const bool have_cs = __builtin_amdgcn_readfirstlane(FL[F_CS + (wv >> 1)]) != 0u;
    float cs = *(const LAS float*)(L + L_CS + (wv >> 1) * 32 + (lane >> 3) * 4);
    int h2o = WS_H2 + ((kg * 16 + wcq * 4 + wv) * 32 + whead) * 128 + n * 8;
    int oar = L_OA + (n >> 2) * 320 + kg * 16, oaw = L_OA + (wv >> 1) * 320 + (wv & 1) * 128 + n * 8 + kg * 2;
    int wtr = L_WO + (8 * kg + (n >> 2)) * 256 + (((4 * wv + ((n >> 1) & 1)) ^ (((n >> 2) | ((kg & 1) << 2)) << 1)) << 4) + (n & 1) * 8, wtr1 = wtr ^ 32;
    unsigned tg2 = tag >> ((n & 1) * 16);
    PIN(h2o); PIN(oar); PIN(oaw); PIN(cs); PIN(tg2); PIN(wtr); PIN(wtr1);
    SB();
    {
      const int ho = WS_H1 + (((wv >> 1) * 8 + (whead >> 2)) * 8 + (lane >> 3)) * 2048 + (whead & 3) * 512 + (wv & 1) * 256 + (lane & 7) * 16;
      f32x4 v[2];
      if (have_cs) {
        const int off[2] = {ho, ho + 128};
        sweep<2, 0>(rws, off, v, tag);
      } else {
        const int off[4] = {ho, ho + 128, ST_OFF(wv >> 1), ST_OFF(wv >> 1) + 16};
        f32x4 u[4];
        sweep<4, 0>(rws, off, u, tag);
        v[0] = u[0]; v[1] = u[1];
        cs = split_weight(u[2], u[3], whead);
      }
      const float o0 = part_sum(v[0] * cs), o1 = part_sum(v[1] * cs);
      *(LAS unsigned short*)(L + oaw) = (unsigned short)pack2((lane & 8) ? o1 : o0, 0.f);
    }
    asm volatile("s_waitcnt vmcnt(0)" ::: "memory");
    wg_barrier();
    STAMP(11);
    if (wv < 4) {
      v4u a[4]; v2u bl[8], bh[8];
#pragma unroll
      for (int k = 0; k < 4; ++k) {
        a[k] = *(const LAS v4u*)(L + oar + k * 64);
        bl[2 * k] = lds_tr(L + wtr + k * 8192); bh[2 * k] = lds_tr(L + wtr + k * 8192 + 1024);
        bl[2 * k + 1] = lds_tr(L + wtr1 + k * 8192); bh[2 * k + 1] = lds_tr(L + wtr1 + k * 8192 + 1024);
      }
      SB();
      f32x4 c0 = {0.f, 0.f, 0.f, 0.f}, c1 = {0.f, 0.f, 0.f, 0.f};
#pragma unroll
      for (int k = 0; k < 4; ++k) {
        c0 = mfma32(a[k], v4u{bl[2 * k].x, bl[2 * k].y, bh[2 * k].x, bh[2 * k].y}, c0);
        c1 = mfma32(a[k], v4u{bl[2 * k + 1].x, bl[2 * k + 1].y, bh[2 * k + 1].x, bh[2 * k + 1].y}, c1);
      }
      __builtin_amdgcn_raw_buffer_store_b64(v2u{put1(c0[0], tg2), put1(c1[0], tg2 >> 8)}, rws, h2o, 0, AUX_SC1);
    }
    SB();
    STAMP(12);
    if (g >= 192) {
      const int rr = g - 192, col = 16 * ((lane >> 2) & 1) + 4 * kg + (lane & 3);
      float x1 = 0.f, gb = 0.f;
      if (wv == 0) {
        const int pc = 2 * (lane & 7) + (kg >> 1) + 16 * (kg & 1);
        float ga = *(const LAS float*)(L + L_GG + pc * 4), xin = *(const LAS float*)(L + L_GG + 256 + pc * 4);
        gb = *(const LAS float*)(L + L_GG + 128 + col * 4);
        int x1o = WS_X1 + (n >> 3) * 8192 + rr * 128 + pc * 4;
        unsigned tg1 = tag >> ((pc & 3) * 8);
        PIN(ga); PIN(xin); PIN(x1o); PIN(gb); PIN(tg1);
        const int ho = WS_H2 + rr * 4096 + lane * 16;
        const int off[4] = {ho, ho + 1024, ho + 2048, ho + 3072};
        f32x4 v[4];
        if (H2DLY) __builtin_amdgcn_s_sleep(H2DLY);
        sweep<4, 0>(rws, off, v, tag);
        STAMP(13);
        x1 = rbf(xin + rbf(ga * rbf(part_sum((v[0] + v[1]) + (v[2] + v[3])))));
        const unsigned xw = put1(x1, tg1);
        __builtin_amdgcn_raw_buffer_store_b32(xw, rws, x1o, 0, AUX_SC1);
        if (XREP > 2) __builtin_amdgcn_raw_buffer_store_b32(xw, rws, x1o, 16384, AUX_SC1);
        SB();
        *(LAS float*)(L + L_GG + 256 + pc * 4) = x1;
        LGKM0();
        x1 = *(const LAS float*)(L + L_GG + 256 + col * 4);
        PIN(x1);
      }
      STAMP(14);
      if (wv == 7) {
        const int off[1] = {WS_X1 + (g & (XREP - 1)) * 8192 + lane * 128};
        f32x4 v[1];
        sweep<1, 0>(rws, off, v, tag);
        STAMP(13);
        __builtin_amdgcn_s_sleep(DLY_H4 > 0 ? DLY_H4 : 0);
        FL[F_H4] = 1u;
      } else {
        const int off[2] = {WS_H4 + rr * 14336 + wv * 2048 + lane * 16, WS_H4 + rr * 14336 + wv * 2048 + 1024 + lane * 16};
        f32x4 v[2];
        if (DLY_H4 >= 0) lds_wait(FL + F_H4);
        sweep<2, 0>(rws, off, v, tag);
        *(LAS float*)(L + L_RED + wv * 128 + col * 4) = part_sum(v[0] + v[1]);
      }
      wg_barrier();
      if (wv == 0) {
        const LAS float* rp = (const LAS float*)(L + L_RED + col * 4);
        const float a = ((rp[0] + rp[32]) + (rp[64] + rp[96])) + ((rp[128] + rp[160]) + rp[192]);
        const float o = x1 + rbf(gb * rbf(a));
        if (n < 8) __builtin_amdgcn_raw_buffer_store_b16((unsigned short)pack2(o, 0.f), mkrsrc(out, 4096), (rr * 32 + col) * 2, 0, AUX_SC1);
        if (g == 192 && lane == 0) __builtin_amdgcn_raw_buffer_store_b32(tag, rws, WS_EPOCH, 0, AUX_SC1);
      }
      STAMP(15);
    }
  } else {
    const int ru = ((g >> 5) << 3) + (g & 7), jsl = 4 * ru + ((g >> 3) & 3);
    if (ru < 28) {
      __builtin_amdgcn_s_barrier();
      if (GDLY) __builtin_amdgcn_s_sleep(GDLY);
      {
        const unsigned char* gsrc = GU_SRC();
        if (GU_B < 2 && wv < 8 - 4 * GU_B) GU_DMA(4 * GU_B + wv);
#pragma unroll
        for (int j = 1; j < 4; ++j) GU_DMA(8 * j + wv);
#pragma unroll
        for (int j = 0; j < 2; ++j) {
          unsigned keep;
          asm volatile("s_mov_b32 %0, m0\n\ts_mov_b32 m0, %3\n\ts_nop 0\n\tglobal_load_lds_dwordx4 %1, %2 nt\n\ts_mov_b32 m0, %0" : "=&s"(keep)
                       : "v"((lane ^ (j | ((wv & 1) << 3))) << 4), "s"(wd + (size_t)(16 * jsl + 2 * wv + j) * 512), "s"((unsigned)(uintptr_t)(L + L_DN) + (2 * wv + j) * 1024) : "memory");
        }
      }
      v4u bg[8], bd[2];
      const int rb = wv >> 1, hf = wv & 1;
      f32x4 shv = *(const LAS f32x4*)(L + L_SC + rb * 2048 + hf * 1024 + lane * 16), scv = *(const LAS f32x4*)(L + L_SC + 8192 + rb * 2048 + hf * 1024 + lane * 16);
      PIN(shv); PIN(scv);
      int hbw = L_HB + rb * 1024 + (rb >> 1) * 32 + hf * 512 + lane * 8;
      int hbr = L_HB + (n >> 2) * 1024 + (n >> 3) * 32 + (32 * (n & 1) + 8 * kg) * 2;
      int acr = ((n & 1) == (kg >> 1)) ? L_ACT + (n >> 2) * 32 + (kg & 1) * 16 : L_ZERO, acw = L_ACT + kg * 32 + wv * 8 + (n & 3) * 2;
      int h4o = WS_H4 + ((kg * 16 + 2 * wv + (n >> 3)) * 112 + jsl) * 128 + (n & 7) * 16;
      int dtr = L_DN + (8 * (kg & 1) + (n >> 2)) * 1024 + (((8 * wv + 2 * (n & 3)) ^ (((n >> 2) & 1) | (n & 8))) << 4) + (kg >> 1) * 8;
      PIN(hbw); PIN(hbr); PIN(acr); PIN(acw); PIN(h4o); PIN(dtr);
      asm volatile("s_waitcnt vmcnt(2)" ::: "memory");
      wg_barrier();
      STAMP(12);
      if (wv < 4) {
        LAS unsigned char* gt = L + L_FB + (kg >> 1) * 1024 + (lane & 31) * 32 + (((wv >> 1) ^ (n >> 3)) << 4) + (wv & 1) * 8;
#pragma unroll
        for (int t = 0; t < 8; ++t) {
          const v2u lo = lds_tr(gt + t * 4096), hi = lds_tr(gt + t * 4096 + 2048);
          bg[t] = v4u{lo.x, lo.y, hi.x, hi.y};
        }
      }
      SB();
      {
        const int xr = WS_X1 + (g & (XREP - 1)) * 8192 + rb * 2048 + lane * 16;
        const int off[2] = {xr + hf * 1024, xr + (hf ^ 1) * 1024};
        v4u r[2];
        sweep_raw<2, 0>(rws, off, r, tag);
        STAMP(13);
        const unsigned p0 = __builtin_amdgcn_perm(r[0].y, r[0].x, 0x07060302u), p1 = __builtin_amdgcn_perm(r[0].w, r[0].z, 0x07060302u);
        const unsigned p2 = __builtin_amdgcn_perm(r[1].y, r[1].x, 0x07060302u), p3 = __builtin_amdgcn_perm(r[1].w, r[1].z, 0x07060302u);
        const float s1 = dot2(p1, 0x3F803F80u, dot2(p0, 0x3F803F80u, 0.f)) + dot2(p3, 0x3F803F80u, dot2(p2, 0x3F803F80u, 0.f));
        const float s2 = dot2(p1, p1, dot2(p0, p0, 0.f)) + dot2(p3, p3, dot2(p2, p2, 0.f));
        auto sw = __builtin_amdgcn_permlane32_swap(__float_as_uint(s1), __float_as_uint(s2), false, false);
        float t = __uint_as_float(sw[0]) + __uint_as_float(sw[1]);
        t += dpp<XOR1>(t); t += dpp<XOR2>(t); t += dpp<ROR4>(t); t += dpp<ROR8>(t);
        const float mean = (__uint_as_float(__builtin_amdgcn_readlane(__float_as_uint(t), 0)) + __uint_as_float(__builtin_amdgcn_readlane(__float_as_uint(t), 16))) * (1.f / 512.f);
        const float msq = (__uint_as_float(__builtin_amdgcn_readlane(__float_as_uint(t), 32)) + __uint_as_float(__builtin_amdgcn_readlane(__float_as_uint(t), 48))) * (1.f / 512.f);
        const float mb = rbf(mean), rden = __builtin_amdgcn_rcpf(rbf(__builtin_amdgcn_sqrtf(rbf(rbf(fmx(fmaf(-mean, mean, msq), 0.f)) + 1e-5f))));
        const f32x2 da = rbf2(f32x2{bflo(p0), bfhi(p0)} - mb), db = rbf2(f32x2{bflo(p1), bfhi(p1)} - mb);
        const f32x2 na = rbf2(da * rden), nb = rbf2(db * rden);
        const f32x2 ha = rbf2(na * f32x2{scv[0], scv[1]}) + f32x2{shv[0], shv[1]}, hb = rbf2(nb * f32x2{scv[2], scv[3]}) + f32x2{shv[2], shv[3]};
        *(LAS v2u*)(L + hbw) = v2u{pack2(ha.x, ha.y), pack2(hb.x, hb.y)};
      }
      wg_barrier();
      v4u a[8];
      if (wv < 4) {
#pragma unroll
        for (int t = 0; t < 8; ++t) a[t] = *(const LAS v4u*)(L + hbr + t * 128);
      }
#pragma unroll
      for (int m2 = 0; m2 < 2; ++m2) {
        const v2u lo = lds_tr(L + (dtr ^ (m2 << 4))), hi = lds_tr(L + (dtr ^ (m2 << 4)) + 4096);
        bd[m2] = v4u{lo.x, lo.y, hi.x, hi.y};
      }
      SB();
      if (wv < 4) {
        f32x4 c = {0.f, 0.f, 0.f, 0.f};
#pragma unroll
        for (int t = 0; t < 8; ++t) c = mfma32(a[t], bg[t], c);
        float gs = (lane & 8) ? c[1] : c[0];
        gs += dpp<ROR8>(gs);
        gs = rbf(gs);
        const float act = rbf(gelu_f(gs)) * dpp<ROR4>(gs);
        if (n < 4) *(LAS unsigned short*)(L + acw) = (unsigned short)pack2(act, 0.f);
      }
      wg_barrier();
      STAMP(14);
      {
        const v4u a = *(const LAS v4u*)(L + acr);
        const f32x4 c0 = mfma32(a, bd[0], f32x4{0.f, 0.f, 0.f, 0.f}), c1 = mfma32(a, bd[1], f32x4{0.f, 0.f, 0.f, 0.f});
        st16(rws, h4o, put4(f32x4{c0[0], c0[1], c1[0], c1[1]}, tag));
      }
      STAMP(15);
    }
  }
  STAMP_OUT();
}

extern "C" void kernel_launch(void* const* d_in, const int* in_sizes, int n_in, void* d_out, int out_size, void* d_ws, size_t ws_size, hipStream_t stream) {
  adaln_block_kernel<<<256, 512, 0, stream>>>((const bf16_t*)d_in[0], (const bf16_t*)d_in[1], (const bf16_t*)d_in[2], (const bf16_t*)d_in[3],
                                              (const bf16_t*)d_in[4], (const bf16_t*)d_in[6], (const bf16_t*)d_in[7], (const bf16_t*)d_in[8],
                                              (const bf16_t*)d_in[9], (const bf16_t*)d_in[10], (bf16_t*)d_out, (unsigned char*)d_ws);
}
```

```cpp
#include <hip/hip_runtime.h>
#include <hip/hip_bf16.h>
#include <stdint.h>

#define GAS __attribute__((address_space(1)))
#define LAS __attribute__((address_space(3)))
typedef unsigned short bf16_t;
typedef unsigned v4u __attribute__((ext_vector_type(4)));
typedef unsigned v2u __attribute__((ext_vector_type(2)));
typedef float f32x4 __attribute__((ext_vector_type(4)));
typedef float f32x2 __attribute__((ext_vector_type(2)));
typedef __bf16 bf16x8 __attribute__((ext_vector_type(8)));
typedef __bf16 bf16x2 __attribute__((ext_vector_type(2)));
typedef short s16x4 __attribute__((ext_vector_type(4)));
typedef __amdgpu_buffer_rsrc_t rsrc_t;

#define PLO 4
#define PHI 8
#define SCH_A 110
#define SCH_F 60
#define SCH_G (-100)
#define SCH_T16 250
#define T_MOD 380
#define T_MODF 190
#define T_Q 440
#define DLY_H4 35
#define XREP 4
#define WO_DLY 20
#define ANCH 1
#define T_Q0 640
#define GDLY 12
#define H2DLY 14
#define NOROT 0
#define DRY 0
#define CPF 1
#define QAK 1
#define QSL 260
#define KPHI 1
#define DRY_AT 20
#define ABL 0

namespace {
constexpr int SK = 16384, WIN = 4096, FF = 1792;
constexpr int AUX_SC1 = 16, AUX_NT = 2;
constexpr int WS_EPOCH = 0;
constexpr int WS_MT = 2048;
constexpr int WS_X1 = 4096;
constexpr int WS_SB = WS_X1 + 32768;
constexpr int WS_CB = WS_SB + 8192;
constexpr int WS_ST = WS_CB + 8192;
constexpr int WS_MOD = WS_ST + 32768;
constexpr int WS_QP = WS_MOD + 196608;
constexpr int WS_H1 = WS_QP + 524288;
constexpr int WS_H2 = WS_H1 + 524288;
constexpr int WS_H4 = WS_H2 + 262144;
constexpr int WS_END = WS_H4 + 917504;
constexpr int WS_STAMP = 8 << 20;
constexpr int L_K = 0;
constexpr int L_FB = 0;
constexpr int L_WO = 0;
constexpr int L_SC = 32768;
constexpr int L_DN = 49152;
constexpr int L_P = 131072;
constexpr int L_QB = L_P + 4096;
constexpr int L_HQ = L_QB + 1280;
constexpr int L_ZERO = L_HQ + 1024;
constexpr int L_FLAG = L_ZERO + 1024;
constexpr int L_GG = L_FLAG + 256;
constexpr int L_OA = L_GG + 512;
constexpr int L_CS = L_OA + 1280;
constexpr int L_F = L_CS + 128;
constexpr int L_ACT = L_F + 128;
constexpr int L_RED = L_ACT + 128;
constexpr int L_MRG = L_RED + 2048;
constexpr int L_HB = L_MRG + 8192;
constexpr int L_END = L_HB + 4096 + 64;
enum { F_MCNT = 0, F_HCNT = 1, F_QCNT = 2, F_K = 4, F_PCNT = 8, F_G = 10, F_H4 = 12, F_WCNT = 13, F_VI = 16, F_VE = 20, F_CS = 24, F_ANC = 28 };
constexpr unsigned SPIN_G = 1u << 16, SPIN_L = 1u << 20;

__device__ __forceinline__ float bflo(unsigned u) { return __uint_as_float(u << 16); }
__device__ __forceinline__ float bfhi(unsigned u) { return __uint_as_float(u & 0xFFFF0000u); }
__device__ __forceinline__ unsigned pack2(float lo, float hi) {
  f32x2 v = {lo, hi};
  return __builtin_bit_cast(unsigned, __builtin_convertvector(v, bf16x2));
}
__device__ __forceinline__ float dot2(unsigned a, unsigned b, float c) {
  return __builtin_amdgcn_fdot2_f32_bf16(__builtin_bit_cast(bf16x2, a), __builtin_bit_cast(bf16x2, b), c, false);
}
__device__ __forceinline__ float rbf(float x) { return bflo(pack2(x, x) & 0xFFFFu); }
__device__ __forceinline__ float fmx(float a, float b) { return __builtin_amdgcn_fmed3f(a, b, __builtin_inff()); }
template <int CTRL> __device__ __forceinline__ float dpp(float x) {
  return __builtin_bit_cast(float, __builtin_amdgcn_mov_dpp(__builtin_bit_cast(int, x), CTRL, 0xf, 0xf, true));
}
constexpr int XOR1 = 0xB1, XOR2 = 0x4E, HMIR = 0x141, ROR2 = 0x122, ROR4 = 0x124, ROR8 = 0x128;
__device__ __forceinline__ float xrow_sum(float x) {
  auto s = __builtin_amdgcn_permlane16_swap(__float_as_uint(x), __float_as_uint(x), false, false);
  x = __uint_as_float(s[0]) + __uint_as_float(s[1]);
  auto t = __builtin_amdgcn_permlane32_swap(__float_as_uint(x), __float_as_uint(x), false, false);
  return __uint_as_float(t[0]) + __uint_as_float(t[1]);
}
__device__ __forceinline__ float xrow_max(float x) {
  auto s = __builtin_amdgcn_permlane16_swap(__float_as_uint(x), __float_as_uint(x), false, false);
  x = fmx(__uint_as_float(s[0]), __uint_as_float(s[1]));
  auto t = __builtin_amdgcn_permlane32_swap(__float_as_uint(x), __float_as_uint(x), false, false);
  return fmx(__uint_as_float(t[0]), __uint_as_float(t[1]));
}
__device__ __forceinline__ float wave_sum(float x) {
  x += dpp<XOR1>(x); x += dpp<XOR2>(x); x += dpp<ROR4>(x); x += dpp<ROR8>(x);
  return xrow_sum(x);
}
__device__ __forceinline__ f32x4 mfma32(v4u a, v4u b, f32x4 c) {
  return __builtin_amdgcn_mfma_f32_16x16x32_bf16(__builtin_bit_cast(bf16x8, a), __builtin_bit_cast(bf16x8, b), c, 0, 0, 0);
}
template <int E> __device__ __forceinline__ v4u tcol8(const v4u* r) {
  constexpr unsigned sel = (E & 1) ? 0x07060302u : 0x05040100u;
  v4u o;
  o.x = __builtin_amdgcn_perm(r[1][E >> 1], r[0][E >> 1], sel);
  o.y = __builtin_amdgcn_perm(r[3][E >> 1], r[2][E >> 1], sel);
  o.z = __builtin_amdgcn_perm(r[5][E >> 1], r[4][E >> 1], sel);
  o.w = __builtin_amdgcn_perm(r[7][E >> 1], r[6][E >> 1], sel);
  return o;
}
__device__ __forceinline__ v4u tcol8r(const v4u* r, int e) {
  const unsigned sel = (e & 1) ? 0x07060302u : 0x05040100u;
  v4u o;
  o.x = __builtin_amdgcn_perm(r[1][e >> 1], r[0][e >> 1], sel);
  o.y = __builtin_amdgcn_perm(r[3][e >> 1], r[2][e >> 1], sel);
  o.z = __builtin_amdgcn_perm(r[5][e >> 1], r[4][e >> 1], sel);
  o.w = __builtin_amdgcn_perm(r[7][e >> 1], r[6][e >> 1], sel);
  return o;
}
#define FOR8(X) X(0) X(1) X(2) X(3) X(4) X(5) X(6) X(7)
__device__ __forceinline__ v2u lds_tr(LAS unsigned char* p) { return __builtin_bit_cast(v2u, __builtin_amdgcn_ds_read_tr16_b64_v4i16((LAS s16x4*)p)); }
__device__ __forceinline__ rsrc_t mkrsrc(const void* p, int bytes) { return __builtin_amdgcn_make_buffer_rsrc((void*)p, 0, bytes, 0x00020000); }
template <int AUX> __device__ __forceinline__ v4u bld(rsrc_t r, int voff, int soff) { return __builtin_amdgcn_raw_buffer_load_b128(r, voff, soff, AUX); }
__device__ __forceinline__ unsigned put1(float v, unsigned t) { return (__float_as_uint(v) & ~15u) | (t & 15u); }
__device__ __forceinline__ v4u put4(f32x4 v, unsigned tag) { return v4u{put1(v.x, tag), put1(v.y, tag >> 8), put1(v.z, tag >> 16), put1(v.w, tag >> 24)}; }
__device__ __forceinline__ unsigned tagdiff(v4u v, unsigned tag) {
  return (__builtin_amdgcn_perm(v.y, v.x, 0x0c0c0400u) | __builtin_amdgcn_perm(v.w, v.z, 0x04000c0cu)) ^ tag;
}
__device__ __forceinline__ bool all_ok(unsigned diff) { return __all((diff & 0x0F0F0F0Fu) == 0u); }
__device__ __forceinline__ f32x4 get4(v4u v) { return __builtin_bit_cast(f32x4, v & ~15u); }
__device__ __forceinline__ void st16(rsrc_t r, int off, v4u v) { __builtin_amdgcn_raw_buffer_store_b128(v, r, off, 0, AUX_SC1); }
#define CBAR() asm volatile("" ::: "memory")
#define LGKM0() asm volatile("s_waitcnt lgkmcnt(0)" ::: "memory")
#define SB() __builtin_amdgcn_sched_barrier(0)
#define PIN(x) asm volatile("" : "+v"(x))
#define FRESH(x) asm volatile("" : "=v"(x))
__device__ __forceinline__ void lds_wait(volatile LAS unsigned* f, unsigned want = 1u) {
#pragma unroll 1
  for (unsigned i = 0; i < SPIN_L; ++i) { if (*f >= want) break; __builtin_amdgcn_s_sleep(1); }
  CBAR();
}
__device__ __forceinline__ void lds_set(volatile LAS unsigned* f) { LGKM0(); *f = 1u; }
__device__ __forceinline__ void lds_arrive(LAS unsigned* f, int lane) {
  LGKM0();
  if (lane == 0) __hip_atomic_fetch_add(f, 1u, __ATOMIC_RELAXED, __HIP_MEMORY_SCOPE_WORKGROUP);
}
__device__ __forceinline__ void wg_barrier() { LGKM0(); __builtin_amdgcn_s_barrier(); CBAR(); }
template <int N, int SLEEP> __device__ __forceinline__ void sweep_raw(rsrc_t rs, const int (&off)[N], v4u (&v)[N], unsigned tag) {
  unsigned spin = 0;
  unsigned long long more;
#pragma unroll 1
  do {
    if (SLEEP && spin) __builtin_amdgcn_s_sleep(SLEEP);
    CBAR();
#pragma unroll
    for (int i = 0; i < N; ++i) v[i] = bld<AUX_SC1>(rs, off[i], 0);
    ++spin;
    unsigned mask = 0x0F0F0F0Fu & (unsigned)((int)(spin - SPIN_G) >> 31);
    asm volatile("" : "+s"(mask));
    unsigned bad = 0;
#pragma unroll
    for (int i = 0; i < N; ++i) bad |= tagdiff(v[i], tag);
    more = __builtin_amdgcn_ballot_w64((bad & mask) != 0u);
  } while (more);
}
template <int N, int SLEEP> __device__ __forceinline__ void sweep(rsrc_t rs, const int (&off)[N], f32x4 (&o)[N], unsigned tag) {
  v4u v[N];
  sweep_raw<N, SLEEP>(rs, off, v, tag);
#pragma unroll
  for (int i = 0; i < N; ++i) o[i] = get4(v[i]);
}
__device__ __forceinline__ float fexp(float x) { return __builtin_amdgcn_exp2f(x * 1.4426950408889634f); }
__device__ __forceinline__ float silu_f(float x) { return x * __builtin_amdgcn_rcpf(1.f + fexp(-x)); }
__device__ __forceinline__ float gelu_f(float x) {
  const float t = x * fmaf(x * x, -0.10294324f, -2.3022082f);
  return x * __builtin_amdgcn_rcpf(1.f + __builtin_amdgcn_exp2f(t));
}
__device__ __forceinline__ void tcol4x8(const v2u* r, v4u* o) {
#pragma unroll
  for (int e = 0; e < 4; ++e) {
    const unsigned sel = (e & 1) ? 0x07060302u : 0x05040100u;
#pragma unroll
    for (int jp = 0; jp < 4; ++jp) o[e][jp] = __builtin_amdgcn_perm(r[2 * jp + 1][e >> 1], r[2 * jp][e >> 1], sel);
    PIN(o[e]);
  }
}
__device__ __forceinline__ f32x4 gemv32x64(v4u a, const v4u* bt) {
  f32x4 c[4];
#pragma unroll
  for (int e = 0; e < 4; ++e) c[e] = mfma32(a, bt[e], f32x4{0.f, 0.f, 0.f, 0.f});
  return f32x4{c[0][0], c[1][0], c[2][0], c[3][0]};
}
__device__ __forceinline__ void wait_until(unsigned long long t0, unsigned ticks) {
#pragma unroll 1
  for (int i = 0; i < 4096 && __builtin_amdgcn_s_memrealtime() - t0 < ticks; ++i) __builtin_amdgcn_s_sleep(1);
}
__device__ __forceinline__ float sel4(bool b0, bool b1, float c0, float c1, float c2, float c3) {
  const float lo = b0 ? c1 : c0, hi = b0 ? c3 : c2;
  return b1 ? hi : lo;
}
__device__ __forceinline__ float part_sum(f32x4 v) {
  auto p = __builtin_amdgcn_permlane32_swap(__float_as_uint(v[0]), __float_as_uint(v[2]), false, false);
  auto q = __builtin_amdgcn_permlane32_swap(__float_as_uint(v[1]), __float_as_uint(v[3]), false, false);
  const float A = __uint_as_float(p[0]) + __uint_as_float(p[1]), B = __uint_as_float(q[0]) + __uint_as_float(q[1]);
  auto r = __builtin_amdgcn_permlane16_swap(__float_as_uint(A), __float_as_uint(B), false, false);
  const float t = __uint_as_float(r[0]) + __uint_as_float(r[1]);
  return t + dpp<ROR8>(t);
}
__device__ __forceinline__ float split_weight(f32x4 m4, f32x4 l4, int hh) {
  const bool h0 = hh & 1, h1 = hh & 2;
  const float m = sel4(h0, h1, m4[0], m4[1], m4[2], m4[3]), l = sel4(h0, h1, l4[0], l4[1], l4[2], l4[3]);
  const float M = xrow_max(fmx(m, dpp<ROR8>(m)));
  const float e = fexp(m - M);
  float Ls = l * e; Ls += dpp<ROR8>(Ls);
  return e * __builtin_amdgcn_rcpf(xrow_sum(Ls));
}
__device__ __forceinline__ f32x2 rbf2(f32x2 v) {
  const unsigned p = pack2(v.x, v.y);
  return f32x2{bflo(p), bfhi(p)};
}
template <bool MAX> __device__ __forceinline__ float red2(float a, float b) { return MAX ? fmx(a, b) : a + b; }
template <bool MAX> __device__ __forceinline__ f32x4 wave_red4(f32x4 s, int lane) {
  const bool o1 = lane & 1, o2 = lane & 2;
  const float a = red2<MAX>(o1 ? s[1] : s[0], dpp<XOR1>(o1 ? s[0] : s[1])), c = red2<MAX>(o1 ? s[3] : s[2], dpp<XOR1>(o1 ? s[2] : s[3]));
  float m = red2<MAX>(o2 ? c : a, dpp<XOR2>(o2 ? a : c));
  m = red2<MAX>(m, dpp<ROR4>(m)); m = red2<MAX>(m, dpp<ROR8>(m));
  m = MAX ? xrow_max(m) : xrow_sum(m);
  return f32x4{dpp<0x00>(m), dpp<0x55>(m), dpp<0xAA>(m), dpp<0xFF>(m)};
}
#define STAMP(i) do {} while (0)
#define STAMP_OUT() do {} while (0)
}

static __device__ __attribute__((noinline, used)) void code_begin_marker() { __builtin_amdgcn_s_sleep(0); }
static __device__ __attribute__((noinline, used)) void code_end_marker();

__global__ void __launch_bounds__(512, 2)
adaln_block_kernel(const bf16_t* __restrict__ x, const bf16_t* __restrict__ kc, const bf16_t* __restrict__ vc,
                   const bf16_t* __restrict__ wq, const bf16_t* __restrict__ wo, const bf16_t* __restrict__ wg,
                   const bf16_t* __restrict__ wu, const bf16_t* __restrict__ wd, const bf16_t* __restrict__ cc,
                   const bf16_t* __restrict__ wmod, bf16_t* __restrict__ out, unsigned char* ws) {
  __shared__ __attribute__((aligned(16))) unsigned char lds_raw[L_END];
  LAS unsigned char* L = (LAS unsigned char*)lds_raw;
  volatile LAS unsigned* FL = (volatile LAS unsigned*)(L + L_FLAG);
  LAS unsigned* FC = (LAS unsigned*)(L + L_FLAG);
  const unsigned long long tstart = __builtin_amdgcn_s_memrealtime();
  const int tid = threadIdx.x, lane = tid & 63, wv = __builtin_amdgcn_readfirstlane(tid >> 6);
  const int g = blockIdx.x, grp = (g & 7) * 4 + (g >> 6), b = grp >> 3, kvh = grp & 7, s = (g >> 3) & 7;
  const int n = lane & 15, kg = lane >> 4;
#define CODE_FETCH() do {     \
    const unsigned long long c0 = (unsigned long long)(uintptr_t)&code_begin_marker, c1 = (unsigned long long)(uintptr_t)&code_end_marker; \
    const int len = (int)(c1 - c0); \
    const v4u rc = {(unsigned)c0, (unsigned)(c0 >> 32) & 0xFFFFu, (unsigned)(len > 0 ? len : 0), 0x00020000u};     \
    unsigned keep; \
    asm volatile("s_mov_b32 %0, m0\n\ts_mov_b32 m0, %3\n\ts_nop 0\n\tbuffer_load_dwordx4 %1, %2, 0 offen lds\n\ts_mov_b32 m0, %0" \
                 : "=&s"(keep) : "v"((g >> 3) * 1024 + lane * 16), "s"(rc), "s"((unsigned)(uintptr_t)(L + L_HB)) : "memory"); } while (0)
  if (CPF == 1 && wv == 3) CODE_FETCH();
  if (tid < 80) *(LAS v4u*)(L + L_ZERO + tid * 16) = v4u{0u, 0u, 0u, 0u};
  asm volatile("" :: "s"(x), "s"(kc), "s"(vc), "s"(wq), "s"(cc), "s"(wmod), "s"(ws));
  wg_barrier();
  const rsrc_t rws = mkrsrc(ws, WS_END);
  unsigned tag;

  if (__builtin_expect(wv >= 4, 1)) {
    const int w = wv - 4;
    const size_t kvbase = ((size_t)grp * SK + (SK - WIN) + s * 512) * 128;
    const int rot = (g * 37) & 127;
    const unsigned char* kbase = (const unsigned char*)(kc + kvbase);
    const unsigned ldsk = (unsigned)(uintptr_t)(L + L_K);
    const int klane = kg * 256 + ((n ^ kg) << 4);
    unsigned due0 = (unsigned)tstart + SCH_A;
#define PACE_WAIT(now, i) do { \
      asm volatile("s_waitcnt vmcnt(%0)" :: "i"(PHI) : "memory"); \
      if (PHI > PLO && (int)((now) - due0) < (((i) * SCH_T16) >> 4)) asm volatile("s_waitcnt vmcnt(%0)" :: "i"(PLO) : "memory"); } while (0)
    if (CPF == 2 && w == 3) CODE_FETCH();
    STAMP(7);
#pragma unroll 4
    for (int i = 0; i < 32; ++i) {
      const int phys = (4 * i + w + rot) & 127;
      const unsigned char* sb_ = kbase + phys * 1024;
      const int vo = klane ^ ((i & 3) << 6);
      const unsigned dst = ldsk + (4 * i + w) * 1024;
      unsigned keep;
      asm volatile("s_mov_b32 %0, m0\n\ts_mov_b32 m0, %3\n\ts_nop 0\n\tglobal_load_lds_dwordx4 %1, %2 nt\n\ts_mov_b32 m0, %0"
                   : "=&s"(keep) : "v"(vo), "s"(sb_), "s"(dst) : "memory");
      if (i < PLO) continue;
      const unsigned now = KPHI ? (unsigned)__builtin_amdgcn_s_memrealtime() : 0u;
      if (KPHI != 0 && ANCH != 0 && i == PLO + 1 && (int)(now - SCH_F - due0) > 0) due0 = now - SCH_F;
      if (KPHI) PACE_WAIT(now, i); else asm volatile("s_waitcnt vmcnt(%0)" :: "i"(PLO) : "memory");
    }
    STAMP(1);
    if (ANCH) { const unsigned anc = __builtin_amdgcn_readfirstlane(FL[F_ANC]); if (anc) { due0 = (unsigned)tstart + SCH_A; if (ANCH == 1 || (int)(anc + SCH_G - due0) > 0) due0 = anc + SCH_G; } }
    const unsigned epoch = __builtin_amdgcn_raw_buffer_load_b32(rws, WS_EPOCH, 0, AUX_SC1);
    const rsrc_t rv = mkrsrc(vc + kvbase, 131072);
    v4u V[32], VT[24], VL[8];
    constexpr int TR0 = 8 + PHI;
#pragma unroll
    for (int i = 0; i < 32; ++i) {
      V[i] = bld<AUX_NT>(rv, lane * 16, ((4 * i + w + rot) & 127) * 1024);
      const unsigned now = (unsigned)__builtin_amdgcn_s_memrealtime();
      if (i >= TR0) { const int k = i - TR0; VT[k] = tcol8r(V + 8 * (k >> 3), k & 7); PIN(VT[k]); }
      if (i == PLO - 1) { asm volatile("s_waitcnt vmcnt(%0)" :: "i"(PLO) : "memory"); FL[F_K + w] = 1u; STAMP(2); }
      else if (i < 31) PACE_WAIT(now, 32 + i);
      if (DRY && i == DRY_AT) FL[F_VE + w] = 1u;
      SB();
    }
    FL[F_VI + w] = 1u;
    tag = __builtin_amdgcn_readfirstlane(epoch) + 0x01010101u;
    STAMP(3);
#pragma unroll
    for (int k = 32 - TR0; k < 24; ++k) { VT[k] = tcol8r(V + 8 * (k >> 3), k & 7); PIN(VT[k]); }
    lds_wait(FL + F_PCNT, 4u);
    STAMP(4);
    f32x4 f;
    {
      const LAS f32x4* sp = (const LAS f32x4*)(L + L_F);
      const f32x4 m0 = sp[0], m1 = sp[2], m2 = sp[4], m3 = sp[6], mo = sp[2 * w];
#pragma unroll
      for (int h = 0; h < 4; ++h) f[h] = fexp(mo[h] - fmx(fmx(m0[h], m1[h]), fmx(m2[h], m3[h])));
    }
    f32x4 acc[2] = {f32x4{0.f, 0.f, 0.f, 0.f}, f32x4{0.f, 0.f, 0.f, 0.f}};
    int paddr[4];
#pragma unroll
    for (int ep = 0; ep < 4; ++ep) paddr[ep] = (n >> 2) == ep ? L_P + w * 1024 + (((n & 3) * 4 + kg) << 4) : L_ZERO;
    int mra = L_MRG + (w * 512 + 8 * n + 2 * kg) * 4;
#pragma unroll
    for (int G = 0; G < 4; ++G) {
      v4u pa[4];
#pragma unroll
      for (int ep = 0; ep < 4; ++ep) pa[ep] = *(const LAS v4u*)(L + paddr[ep] + G * 256);
      if (G == 3) {
        PIN(pa[0]); PIN(pa[1]); PIN(pa[2]); PIN(pa[3]); PIN(f); PIN(mra); PIN(acc[0]); PIN(acc[1]);
        STAMP(5);
#pragma unroll
        for (int jp = 0; jp < 4; ++jp) {
#pragma unroll
          for (int e = 0; e < 8; ++e) {
            unsigned t = __builtin_amdgcn_perm(V[25 + 2 * jp][e >> 1], V[24 + 2 * jp][e >> 1], (e & 1) ? 0x07060302u : 0x05040100u);
            if (jp < 3) PIN(t);
            VL[e][jp] = t;
          }
          if (jp < 3) SB();
        }
      }
#define PV(E) acc[E & 1] = mfma32(pa[E >> 1], G < 3 ? VT[8 * G + E] : VL[E], acc[E & 1]);
      FOR8(PV)
#undef PV
      SB();
    }
    STAMP(6);
#pragma unroll
    for (int h = 0; h < 4; ++h) *(LAS f32x2*)(L + mra + h * 512) = f32x2{acc[0][h], acc[1][h]} * f[h];
    wg_barrier();
    SB();
  } else if (ABL == 6) {
    lds_arrive(FC + F_PCNT, lane); tag = 0;
  } else {
    const int cw = wv;
    __builtin_amdgcn_s_setprio(2);
    if (CPF == 3 && cw == 3) CODE_FETCH();
    const int ks = g >> 5, ns = g & 31, u5 = lane & 31, h5 = lane >> 5;
    const int mkq = g < 64 ? g >> 4 : (g - 64) >> 5, mcb = g < 64 ? g & 15 : 16 + ((g - 64) & 31);
    v2u wm[8], wr[8];
    const rsrc_t rm = mkrsrc(wmod + (size_t)(mkq * 128 + cw * 32) * 3072, g < 192 ? 32 * 6144 : 0);
    const rsrc_t rq = mkrsrc(wq + (size_t)(ks * 64 + (cw & 1) * 32) * 4096, 32 * 8192);
    const v4u cf = *(const v4u*)(cc + (n >> 2) * 512 + ((128 * mkq) & 511) + 32 * cw + 8 * kg);
#pragma unroll
    for (int j = 0; j < 8; ++j) wm[j] = __builtin_amdgcn_raw_buffer_load_b64(rm, 8 * kg * 6144 + mcb * 128 + n * 8, j * 6144, AUX_NT);
    const unsigned epoch = __builtin_amdgcn_raw_buffer_load_b32(rws, WS_EPOCH, 0, AUX_SC1);
    const v4u xs = *(const v4u*)(x + cw * 512 + lane * 8);
    const v2u xo = *(const v2u*)(x + cw * 512 + 64 * ks + 4 * n);
#pragma unroll
    for (int j = 0; j < 8; ++j) wr[j] = __builtin_amdgcn_raw_buffer_load_b64(rq, 8 * kg * 8192 + ns * 256 + (cw >> 1) * 128 + n * 8, j * 8192, AUX_NT);
    SB();
    STAMP(1);
    v4u bq[4];
    unsigned long long tfirst;
    {
      v4u a, bt[4];
#pragma unroll
      for (int i = 0; i < 4; ++i) a[i] = pack2(silu_f(bflo(cf[i])), silu_f(bfhi(cf[i])));
      PIN(a);
      tcol4x8(wm, bt);
      tfirst = __builtin_amdgcn_s_memrealtime();
      *(LAS f32x4*)(L + L_MRG + cw * 1024 + lane * 16) = gemv32x64(a, bt);
      lds_arrive(FC + F_MCNT, lane);
      tag = __builtin_amdgcn_readfirstlane(epoch) + 0x01010101u;
      if (cw == 0 && g < 192) {
        lds_wait(FL + F_MCNT, 4u);
        const LAS f32x4* mp = (const LAS f32x4*)(L + L_MRG + lane * 16);
        st16(rws, WS_MOD + ((mkq * 4 + kg) * 3072 + 64 * mcb + 4 * n) * 4, put4((mp[0] + mp[64]) + (mp[128] + mp[192]), tag));
        if (ANCH && g < 64 && lane == 0) { const float t = __uint_as_float((unsigned)__builtin_amdgcn_s_memrealtime() << 4); st16(rws, WS_MT + g * 16, put4(f32x4{t, t, t, t}, tag)); }
        STAMP(11);
      }
    }
    float ln[4];
    {
      float sum = 0.f;
#pragma unroll
      for (int i = 0; i < 4; ++i) sum += bflo(xs[i]) + bfhi(xs[i]);
      const float mean = wave_sum(sum) * (1.f / 512.f);
      float sq = 0.f;
#pragma unroll
      for (int i = 0; i < 4; ++i) { const float d0 = bflo(xs[i]) - mean, d1 = bfhi(xs[i]) - mean; sq = fmaf(d0, d0, sq); sq = fmaf(d1, d1, sq); }
      sq = wave_sum(sq);
      const float mb = rbf(mean), den = rbf(sqrtf(rbf(rbf(sq * (1.f / 512.f)) + 1e-5f)));
#pragma unroll
      for (int i = 0; i < 2; ++i) { ln[2 * i] = rbf(rbf(bflo(xo[i]) - mb) / den); ln[2 * i + 1] = rbf(rbf(bfhi(xo[i]) - mb) / den); }
    }
    tcol4x8(wr, bq);
    STAMP(2);
    wait_until(tstart, T_MOD);
    if (ANCH) wait_until(tfirst, T_MODF);
    {
      const int mo = WS_MOD + ((h5 * 4 + cw) * 3072 + (kg & 1) * 512 + 64 * ks + 4 * n) * 4;
      const int off[2] = {mo, mo + 2 * 4 * 3072 * 4};
      f32x4 v[2];
      v4u mt;
      if (ANCH && cw == 3) mt = bld<AUX_SC1>(rws, WS_MT + lane * 16, 0);
      sweep<2, 8>(rws, off, v, tag);
      STAMP(3);
      if (ANCH && cw == 3) {
        const unsigned now = (unsigned)__builtin_amdgcn_s_memrealtime();
        const int d = (int)(mt.x - (now << 4)) >> 4;
        const bool ok = (tagdiff(mt, tag) & 0x0F0F0F0Fu) == 0u && d <= 0 && d > -2000;
        const float cnt = wave_sum(ok ? 1.f : 0.f), sum = wave_sum(ok ? (float)d : 0.f);
        FL[F_ANC] = (now + (cnt > 0.f ? (int)(sum / cnt) : -300)) | 1u;
      }
      const f32x4 t = v[0] + v[1];
      float h[4];
#pragma unroll
      for (int e = 0; e < 4; ++e) {
        auto p = __builtin_amdgcn_permlane32_swap(__float_as_uint(t[e]), __float_as_uint(t[e]), false, false);
        const float u = rbf(__uint_as_float(p[0]) + __uint_as_float(p[1]));
        auto r = __builtin_amdgcn_permlane16_swap(__float_as_uint(u), __float_as_uint(u), false, false);
        h[e] = rbf(ln[e] * rbf(1.f + __uint_as_float(r[1]))) + __uint_as_float(r[0]);
      }
      if (lane < 16) *(LAS v2u*)(L + L_HQ + cw * 128 + n * 8) = v2u{pack2(h[0], h[1]), pack2(h[2], h[3])};
      lds_arrive(FC + F_HCNT, lane);
      lds_wait(FL + F_HCNT, 4u);
      const v4u a = *(const LAS v4u*)(L + L_HQ + (n >> 2) * 128 + (cw & 1) * 64 + kg * 16);
      const f32x4 qp = gemv32x64(a, bq);
      if (cw & 1) {
        *(LAS f32x4*)(L + L_MRG + 4096 + (cw >> 1) * 1024 + lane * 16) = qp;
        lds_set(FL + F_G + (cw >> 1));
      } else {
        lds_wait(FL + F_G + (cw >> 1));
        st16(rws, WS_QP + ((kg * 8 + ks) * 4096 + 128 * ns + 64 * (cw >> 1) + 4 * n) * 4, put4(qp + *(const LAS f32x4*)(L + L_MRG + 4096 + (cw >> 1) * 1024 + lane * 16), tag));
      }
    }
    STAMP(4);
    if (cw == 3 && g >= 192) {
      const int rr = g - 192;
      const int mo = WS_MOD + ((((lane >> 3) & 3) * 4 + (rr >> 4)) * 3072 + 1024 + h5 * 512 + 32 * (rr & 15) + 4 * (lane & 7)) * 4;
      const int off[2] = {mo, mo + 4096};
      f32x4 v[2];
      const unsigned short xin = x[rr * 32 + u5];
      sweep<2, 8>(rws, off, v, tag);
#pragma unroll
      for (int i = 0; i < 2; ++i)
#pragma unroll
        for (int e = 0; e < 4; ++e) {
          float u = v[i][e]; u += dpp<ROR8>(u);
          auto p = __builtin_amdgcn_permlane16_swap(__float_as_uint(u), __float_as_uint(u), false, false);
          v[i][e] = rbf(__uint_as_float(p[0]) + __uint_as_float(p[1]));
        }
      *(LAS float*)(L + L_GG + 256 + u5 * 4) = bflo(xin);
      if (u5 < 8) {
        if (lane < 32) {
          *(LAS f32x4*)(L + L_GG + lane * 16) = v[0];
          st16(rws, WS_CB + rr * 128 + lane * 16, put4(f32x4{rbf(1.f + v[1][0]), rbf(1.f + v[1][1]), rbf(1.f + v[1][2]), rbf(1.f + v[1][3])}, tag));
        } else {
          *(LAS f32x4*)(L + L_GG + 128 + u5 * 16) = v[1];
          st16(rws, WS_SB + rr * 128 + u5 * 16, put4(v[0], tag));
        }
      }
    }
    const int qo = WS_QP + ((b * 8 + h5) * 4096 + kvh * 512 + cw * 128 + 4 * u5) * 4;
    {
      if (ANCH == 2) wait_until(tstart, T_Q0);
      unsigned tq = ANCH ? __builtin_amdgcn_readfirstlane(FL[F_ANC]) + T_Q : (unsigned)tstart + T_Q0;
      if (QSL) { const int late = (int)(tq - (unsigned)tstart) - T_Q - QSL; if (late > 0) tq += late; }
#pragma unroll 1
      for (int i = 0; i < 4096 && (int)((unsigned)__builtin_amdgcn_s_memrealtime() - tq) < 0; ++i) __builtin_amdgcn_s_sleep(1);
    }
    if (QAK) lds_wait(FL + F_K + cw);
    v4u qv[4];
#pragma unroll
    for (int i = 0; i < 4; ++i) qv[i] = bld<AUX_SC1>(rws, qo, i * 32768);
    SB();
    if (!QAK) lds_wait(FL + F_K + cw);
    STAMP(5);
    v4u kf[32];
#pragma unroll
    for (int t = 0; t < 8; ++t)
#pragma unroll
      for (int ds = 0; ds < 4; ++ds)
        kf[4 * t + ds] = *(const LAS v4u*)(L + L_K + (16 * (4 * t + (n >> 2)) + 4 * cw + (n & 3)) * 256 + (((4 * ds + kg) ^ n) << 4));
    SB();
    {
#pragma unroll 1
      for (unsigned spin = 0; spin < SPIN_G; ++spin) {
        if (all_ok(tagdiff(qv[0], tag) | tagdiff(qv[1], tag) | tagdiff(qv[2], tag) | tagdiff(qv[3], tag))) break;
        __builtin_amdgcn_s_sleep(8);
#pragma unroll
        for (int i = 0; i < 4; ++i) if (!all_ok(tagdiff(qv[i], tag))) qv[i] = bld<AUX_SC1>(rws, qo, i * 32768);
      }
      const f32x4 t = (get4(qv[0]) + get4(qv[1])) + (get4(qv[2]) + get4(qv[3]));
      float q[4];
#pragma unroll
      for (int e = 0; e < 4; ++e) {
        auto p = __builtin_amdgcn_permlane32_swap(__float_as_uint(t[e]), __float_as_uint(t[e]), false, false);
        q[e] = __uint_as_float(p[0]) + __uint_as_float(p[1]);
      }
      if (lane < 32) *(LAS v2u*)(L + L_QB + cw * 320 + u5 * 8) = v2u{pack2(q[0], q[1]), pack2(q[2], q[3])};
      lds_arrive(FC + F_QCNT, lane);
      lds_wait(FL + F_QCNT, 4u);
    }
    STAMP(6);
    {
      f32x4 sc[2];
      v4u qf[4];
#pragma unroll
      for (int ds = 0; ds < 4; ++ds) qf[ds] = *(const LAS v4u*)(L + L_QB + (n & 3) * 320 + kg * 16 + ds * 64);
      f32x4 c[8];
#pragma unroll
      for (int t = 0; t < 8; ++t) {
        c[t] = f32x4{0.f, 0.f, 0.f, 0.f};
#pragma unroll
        for (int ds = 0; ds < 4; ++ds) c[t] = mfma32(qf[ds], kf[4 * t + ds], c[t]);
      }
#pragma unroll
      for (int sg = 0; sg < 2; ++sg)
#pragma unroll
        for (int h = 0; h < 4; ++h) sc[sg][h] = sel4(lane & 16, lane & 32, c[4 * sg][h], c[4 * sg + 1][h], c[4 * sg + 2][h], c[4 * sg + 3][h]);
      f32x4 e0, e1, mx;
#pragma unroll
      for (int h = 0; h < 4; ++h) {
        const f32x2 r = rbf2(rbf2(f32x2{sc[0][h], sc[1][h]}) * 0.08838834764831845f);
        e0[h] = r.x; e1[h] = r.y; mx[h] = fmx(r.x, r.y);
      }
      const f32x4 mw = wave_red4<true>(mx, lane);
#pragma unroll
      for (int h = 0; h < 4; ++h) {
        e0[h] = fexp(e0[h] - mw[h]); e1[h] = fexp(e1[h] - mw[h]);
        const unsigned pp = pack2(e0[h], e1[h]);
        LAS unsigned short* pd = (LAS unsigned short*)(L + L_P) + ((((cw * 4 + (kg >> 1)) * 4 + h) * 4 + (n & 3)) * 8 + 4 * (kg & 1) + (n >> 2));
        pd[0] = (unsigned short)(pp & 0xFFFFu);
        pd[2 * 128] = (unsigned short)(pp >> 16);
      }
      const f32x4 lw = wave_red4<false>(e0 + e1, lane);
      if (lane < 2) *(LAS f32x4*)(L + L_F + cw * 32 + lane * 16) = lane == 0 ? mw : lw;
      lds_arrive(FC + F_PCNT, lane);
    }
    STAMP(7);
    lds_wait(FL + F_PCNT, 4u);
    if (cw == 3) {
      const LAS f32x4* sp = (const LAS f32x4*)(L + L_F);
      f32x4 M, Ls;
#pragma unroll
      for (int h = 0; h < 4; ++h) {
        M[h] = fmx(fmx(sp[0][h], sp[2][h]), fmx(sp[4][h], sp[6][h]));
        Ls[h] = (sp[1][h] * fexp(sp[0][h] - M[h]) + sp[3][h] * fexp(sp[2][h] - M[h])) + (sp[5][h] * fexp(sp[4][h] - M[h]) + sp[7][h] * fexp(sp[6][h] - M[h]));
      }
      if (lane < 2) st16(rws, WS_ST + (grp * 8 + s) * 128 + lane * 16, put4(lane == 0 ? M : Ls, tag));
    }
#define GU_SRC() (const unsigned char*)(((lane & 2) ? wu : wg) + (size_t)(32 * ((lane >> 2) & 1) + 8 * (lane >> 5) + ((lane >> 3) & 3)) * FF + 16 * jsl + 8 * ((lane ^ (lane >> 4)) & 1))
#define GU_DMA(k_) do { const int kk_ = ((k_) + (NOROT ? 0 : 8 * ((g >> 3) & 3))) & 31; unsigned keep; \
      asm volatile("s_mov_b32 %0, m0\n\ts_mov_b32 m0, %2\n\ts_nop 0\n\tglobal_load_lds_dwordx4 %1, off\n\ts_mov_b32 m0, %0" : "=&s"(keep) \
                   : "v"(gsrc + (64 * (kk_ >> 2) + 4 * ((kk_ >> 1) & 1) + 16 * (kk_ & 1)) * (FF * 2)), "s"((unsigned)(uintptr_t)(L + L_FB) + kk_ * 1024) : "memory"); } while (0)
#define W_HEAD() ((g & 7) + 8 * ((g - 128) >> 5))
#define W_CQ() ((g >> 3) & 3)
#define ST_OFF(row) (WS_ST + (((row) * 8 + (W_HEAD() >> 2)) * 8 + (lane >> 3)) * 128)
    if (g < 128 && ((g >> 5) << 3) + (g & 7) < 28) {
      const int mo = WS_SB + cw * 2048 + lane * 16;
      const int off[4] = {mo, mo + 1024, mo + 8192, mo + 8192 + 1024};
      f32x4 pre[4];
      sweep<4, 8>(rws, off, pre, tag);
#pragma unroll
      for (int i = 0; i < 4; ++i) *(LAS f32x4*)(L + L_SC + (i >> 1) * 8192 + cw * 2048 + (i & 1) * 1024 + lane * 16) = pre[i];
    }
    STAMP(8);
  }
  const int wv0 = wv, h1b = WS_H1 + (grp * 8 + s) * 2048;
  const unsigned tag0 = tag;
#pragma unroll 1
  for (int pass = (DRY && wv0 < 4 && FL[F_VE + wv0] == 0u) ? 0 : 1; pass < 2; ++pass) {
  int live = __builtin_amdgcn_readfirstlane(pass), g = blockIdx.x, wv = wv0;
  unsigned tag = tag0;
  asm volatile("" : "+s"(live), "+s"(g), "+s"(wv), "+s"(tag));
  int tid = threadIdx.x;
  PIN(tid);
  const int lane = tid & 63, n = lane & 15, kg = lane >> 4;
#define BARL() do { if (live) wg_barrier(); } while (0)
  v4u stv[2];
  FRESH(stv[0]); FRESH(stv[1]);
  if (live && wv < 4 && g >= 128) {
    const unsigned char* wsrc = (const unsigned char*)(wo + (size_t)(W_HEAD() * 128 + wv * 32) * 512 + W_CQ() * 128);
    const unsigned wdst = (unsigned)(uintptr_t)(L + L_WO) + wv * 8192;
    const int vo = kg * 1024 + ((n ^ (kg << 1)) << 4), sto = ST_OFF(wv);
    lds_wait(FL + F_VI + wv);
    if (WO_DLY) __builtin_amdgcn_s_sleep(WO_DLY);
    stv[0] = bld<AUX_SC1>(rws, sto, 0); stv[1] = bld<AUX_SC1>(rws, sto + 16, 0);
#pragma unroll
    for (int j = 0; j < 8; ++j) {
      unsigned keep;
      asm volatile("s_mov_b32 %0, m0\n\ts_mov_b32 m0, %3\n\ts_nop 0\n\tglobal_load_lds_dwordx4 %1, %2 nt\n\ts_mov_b32 m0, %0"
                   : "=&s"(keep) : "v"(vo ^ ((j & 2) << 6)), "s"(wsrc + j * 4096), "s"(wdst + j * 1024) : "memory");
    }
    SB();
  }
  int h1o = h1b + tid * 16;
  PIN(h1o);
  STAMP(9);
  if (wv < 4) BARL();
  if (wv < 2) {
    const LAS f32x4* mp = (const LAS f32x4*)(L + L_MRG + tid * 16);
    v4u o = put4((mp[0] + mp[128]) + (mp[256] + mp[384]), tag);
    PIN(o);
    if (live) st16(rws, h1o, o);
  }
  SB();
  STAMP(10);
  if (g >= 128) {
    const int whead = W_HEAD(), wcq = W_CQ();
    int csa = L_CS + (wv >> 1) * 32 + (lane >> 3) * 4;
    int h2o = WS_H2 + ((kg * 16 + wcq * 4 + wv) * 32 + whead) * 128 + n * 8;
    int oar = L_OA + (n >> 2) * 320 + kg * 16, oaw = L_OA + (wv >> 1) * 320 + (wv & 1) * 128 + n * 8 + kg * 2;
    int wtr = L_WO + (8 * kg + (n >> 2)) * 256 + (((4 * wv + ((n >> 1) & 1)) ^ (((n >> 2) | ((kg & 1) << 2)) << 1)) << 4) + (n & 1) * 8, wtr1 = wtr ^ 32;
    unsigned tg2 = tag >> ((n & 1) * 16);
    PIN(h2o); PIN(oar); PIN(oaw); PIN(csa); PIN(tg2); PIN(wtr); PIN(wtr1);
    SB();
    {
      const int ho = WS_H1 + (((wv >> 1) * 8 + (whead >> 2)) * 8 + (lane >> 3)) * 2048 + (whead & 3) * 512 + (wv & 1) * 256 + (lane & 7) * 16;
      const int off[2] = {ho, ho + 128};
      v4u r[2];
      FRESH(r[0]); FRESH(r[1]);
      bool got = false;
      if (wv < 4) {
        if (live) {
          r[0] = bld<AUX_SC1>(rws, off[0], 0); r[1] = bld<AUX_SC1>(rws, off[1], 0);
          if (!all_ok(tagdiff(stv[0], tag) | tagdiff(stv[1], tag))) {
            const int so[2] = {ST_OFF(wv), ST_OFF(wv) + 16};
            sweep_raw<2, 0>(rws, so, stv, tag);
          }
        }
        *(LAS float*)(L + L_CS + wv * 32 + (lane >> 3) * 4) = split_weight(get4(stv[0]), get4(stv[1]), whead);
        CBAR();
        FL[F_CS + wv] = live;
        got = all_ok(tagdiff(r[0], tag) | tagdiff(r[1], tag));
      }
      if (live && !got) sweep_raw<2, 0>(rws, off, r, tag);
      const f32x4 v[2] = {get4(r[0]), get4(r[1])};
      float cs;
#pragma unroll 1
      for (unsigned i = live ? 0u : SPIN_L - 1; i < SPIN_L; ++i) {
        const unsigned fl = FL[F_CS + (wv >> 1)];
        cs = *(const volatile LAS float*)(L + csa);
        if (fl) break;
      }
      const float o0 = part_sum(v[0] * cs), o1 = part_sum(v[1] * cs);
      *(LAS unsigned short*)(L + oaw) = (unsigned short)pack2((lane & 8) ? o1 : o0, 0.f);
    }
    asm volatile("s_waitcnt vmcnt(0)" ::: "memory");
    BARL();
    STAMP(11);
    if (wv < 4) {
      v4u a[4]; v2u bl[8], bh[8];
#pragma unroll
      for (int k = 0; k < 4; ++k) {
        a[k] = *(const LAS v4u*)(L + oar + k * 64);
        bl[2 * k] = lds_tr(L + wtr + k * 8192); bh[2 * k] = lds_tr(L + wtr + k * 8192 + 1024);
        bl[2 * k + 1] = lds_tr(L + wtr1 + k * 8192); bh[2 * k + 1] = lds_tr(L + wtr1 + k * 8192 + 1024);
      }
      SB();
      f32x4 c0 = {0.f, 0.f, 0.f, 0.f}, c1 = {0.f, 0.f, 0.f, 0.f};
#pragma unroll
      for (int k = 0; k < 4; ++k) {
        c0 = mfma32(a[k], v4u{bl[2 * k].x, bl[2 * k].y, bh[2 * k].x, bh[2 * k].y}, c0);
        c1 = mfma32(a[k], v4u{bl[2 * k + 1].x, bl[2 * k + 1].y, bh[2 * k + 1].x, bh[2 * k + 1].y}, c1);
      }
      v2u o = {put1(c0[0], tg2), put1(c1[0], tg2 >> 8)};
      PIN(o);
      if (live) __builtin_amdgcn_raw_buffer_store_b64(o, rws, h2o, 0, AUX_SC1);
    }
    SB();
    STAMP(12);
    if (g >= 192) {
      const int rr = g - 192, col = 16 * ((lane >> 2) & 1) + 4 * kg + (lane & 3);
      float x1 = 0.f, gb = 0.f;
      int oo = (rr * 32 + col) * 2, rda = L_RED + col * 4;
      PIN(oo); PIN(rda);
      if (wv == 0) {
        const int pc = 2 * (lane & 7) + (kg >> 1) + 16 * (kg & 1);
        float ga = *(const LAS float*)(L + L_GG + pc * 4), xin = *(const LAS float*)(L + L_GG + 256 + pc * 4);
        gb = *(const LAS float*)(L + L_GG + 128 + col * 4);
        int x1o = WS_X1 + (n >> 3) * 8192 + rr * 128 + pc * 4;
        unsigned tg1 = tag >> ((pc & 3) * 8);
        PIN(ga); PIN(xin); PIN(x1o); PIN(gb); PIN(tg1);
        const int ho = WS_H2 + rr * 4096 + lane * 16;
        const int off[4] = {ho, ho + 1024, ho + 2048, ho + 3072};
        v4u v[4];
        FRESH(v[0]); FRESH(v[1]); FRESH(v[2]); FRESH(v[3]);
        if (live) {
          if (H2DLY) __builtin_amdgcn_s_sleep(H2DLY);
          sweep_raw<4, 0>(rws, off, v, tag);
        }
        STAMP(13);
        x1 = rbf(xin + rbf(ga * rbf(part_sum((get4(v[0]) + get4(v[1])) + (get4(v[2]) + get4(v[3]))))));
        unsigned xw = put1(x1, tg1);
        PIN(xw);
        if (live) {
          __builtin_amdgcn_raw_buffer_store_b32(xw, rws, x1o, 0, AUX_SC1);
          if (XREP > 2) __builtin_amdgcn_raw_buffer_store_b32(xw, rws, x1o, 16384, AUX_SC1);
        }
        SB();
        *(LAS float*)(L + L_GG + 384 + pc * 4) = x1;
        LGKM0();
        x1 = *(const LAS float*)(L + L_GG + 384 + col * 4);
        PIN(x1);
      }
      STAMP(14);
      if (wv == 7) {
        const int off[1] = {WS_X1 + (g & (XREP - 1)) * 8192 + lane * 128};
        v4u v[1];
        sweep_raw<1, 0>(rws, off, v, tag);
        STAMP(13);
        __builtin_amdgcn_s_sleep(DLY_H4);
        FL[F_H4] = 1u;
      } else {
        const int off[2] = {WS_H4 + rr * 14336 + wv * 2048 + lane * 16, WS_H4 + rr * 14336 + wv * 2048 + 1024 + lane * 16};
        v4u v[2];
        FRESH(v[0]); FRESH(v[1]);
        if (live) {
          lds_wait(FL + F_H4);
          sweep_raw<2, 0>(rws, off, v, tag);
        }
        *(LAS float*)(L + rda + wv * 128) = part_sum(get4(v[0]) + get4(v[1]));
      }
      BARL();
      if (wv == 0) {
        const LAS float* rp = (const LAS float*)(L + rda);
        const float a = ((rp[0] + rp[32]) + (rp[64] + rp[96])) + ((rp[128] + rp[160]) + rp[192]);
        unsigned ob = pack2(x1 + rbf(gb * rbf(a)), 0.f);
        PIN(ob);
        if (live) {
          if (n < 8) __builtin_amdgcn_raw_buffer_store_b16((unsigned short)ob, mkrsrc(out, 4096), oo, 0, AUX_SC1);
          if (g == 192 && lane == 0) __builtin_amdgcn_raw_buffer_store_b32(tag, rws, WS_EPOCH, 0, AUX_SC1);
        }
      }
      STAMP(15);
    }
  } else {
    const int ru = ((g >> 5) << 3) + (g & 7), jsl = 4 * ru + ((g >> 3) & 3);
    if (ru < 28) {
      if (live) {
        __builtin_amdgcn_s_barrier();
        if (GDLY) __builtin_amdgcn_s_sleep(GDLY);
        const unsigned char* gsrc = GU_SRC();
#pragma unroll
        for (int j = 0; j < 4; ++j) GU_DMA(8 * j + wv);
#pragma unroll
        for (int j = 0; j < 2; ++j) {
          unsigned keep;
          asm volatile("s_mov_b32 %0, m0\n\ts_mov_b32 m0, %3\n\ts_nop 0\n\tglobal_load_lds_dwordx4 %1, %2 nt\n\ts_mov_b32 m0, %0" : "=&s"(keep)
                       : "v"((lane ^ (j | ((wv & 1) << 3))) << 4), "s"(wd + (size_t)(16 * jsl + 2 * wv + j) * 512), "s"((unsigned)(uintptr_t)(L + L_DN) + (2 * wv + j) * 1024) : "memory");
        }
      }
      v4u bg[8], bd[2], a[8];
#pragma unroll
      for (int t = 0; t < 8; ++t) { FRESH(bg[t]); }
      const int rb = wv >> 1, hf = wv & 1;
      f32x4 shv = *(const LAS f32x4*)(L + L_SC + rb * 2048 + hf * 1024 + lane * 16), scv = *(const LAS f32x4*)(L + L_SC + 8192 + rb * 2048 + hf * 1024 + lane * 16);
      PIN(shv); PIN(scv);
      int hbw = L_HB + rb * 1024 + (rb >> 1) * 32 + hf * 512 + lane * 8;
      int hbr = L_HB + (n >> 2) * 1024 + (n >> 3) * 32 + (32 * (n & 1) + 8 * kg) * 2;
      int acr = ((n & 1) == (kg >> 1)) ? L_ACT + (n >> 2) * 32 + (kg & 1) * 16 : L_ZERO, acw = L_ACT + kg * 32 + wv * 8 + (n & 3) * 2;
      int h4o = WS_H4 + ((kg * 16 + 2 * wv + (n >> 3)) * 112 + jsl) * 128 + (n & 7) * 16;
      int dtr = L_DN + (8 * (kg & 1) + (n >> 2)) * 1024 + (((8 * wv + 2 * (n & 3)) ^ (((n >> 2) & 1) | (n & 8))) << 4) + (kg >> 1) * 8;
      PIN(hbw); PIN(hbr); PIN(acr); PIN(acw); PIN(h4o); PIN(dtr);
      asm volatile("s_waitcnt vmcnt(2)" ::: "memory");
      BARL();
      STAMP(12);
      if (wv < 4) {
        LAS unsigned char* gt = L + L_FB + (kg >> 1) * 1024 + (lane & 31) * 32 + (((wv >> 1) ^ (n >> 3)) << 4) + (wv & 1) * 8;
#pragma unroll
        for (int t = 0; t < 8; ++t) {
          const v2u lo = lds_tr(gt + t * 4096), hi = lds_tr(gt + t * 4096 + 2048);
          bg[t] = v4u{lo.x, lo.y, hi.x, hi.y};
        }
      }
      SB();
      {
        const int xr = WS_X1 + (g & (XREP - 1)) * 8192 + rb * 2048 + lane * 16;
        const int off[2] = {xr + hf * 1024, xr + (hf ^ 1) * 1024};
        v4u r[2];
        FRESH(r[0]); FRESH(r[1]);
        if (live) sweep_raw<2, 0>(rws, off, r, tag);
        STAMP(13);
        const unsigned p0 = __builtin_amdgcn_perm(r[0].y, r[0].x, 0x07060302u), p1 = __builtin_amdgcn_perm(r[0].w, r[0].z, 0x07060302u);
        const unsigned p2 = __builtin_amdgcn_perm(r[1].y, r[1].x, 0x07060302u), p3 = __builtin_amdgcn_perm(r[1].w, r[1].z, 0x07060302u);
        const float s1 = dot2(p1, 0x3F803F80u, dot2(p0, 0x3F803F80u, 0.f)) + dot2(p3, 0x3F803F80u, dot2(p2, 0x3F803F80u, 0.f));
        const float s2 = dot2(p1, p1, dot2(p0, p0, 0.f)) + dot2(p3, p3, dot2(p2, p2, 0.f));
        auto sw = __builtin_amdgcn_permlane32_swap(__float_as_uint(s1), __float_as_uint(s2), false, false);
        float t = __uint_as_float(sw[0]) + __uint_as_float(sw[1]);
        t += dpp<XOR1>(t); t += dpp<XOR2>(t); t += dpp<ROR4>(t); t += dpp<ROR8>(t);
        const float mean = (__uint_as_float(__builtin_amdgcn_readlane(__float_as_uint(t), 0)) + __uint_as_float(__builtin_amdgcn_readlane(__float_as_uint(t), 16))) * (1.f / 512.f);
        const float msq = (__uint_as_float(__builtin_amdgcn_readlane(__float_as_uint(t), 32)) + __uint_as_float(__builtin_amdgcn_readlane(__float_as_uint(t), 48))) * (1.f / 512.f);
        const float mb = rbf(mean), rden = __builtin_amdgcn_rcpf(rbf(__builtin_amdgcn_sqrtf(rbf(rbf(fmx(fmaf(-mean, mean, msq), 0.f)) + 1e-5f))));
        const f32x2 da = rbf2(f32x2{bflo(p0), bfhi(p0)} - mb), db = rbf2(f32x2{bflo(p1), bfhi(p1)} - mb);
        const f32x2 na = rbf2(da * rden), nb = rbf2(db * rden);
        const f32x2 ha = rbf2(na * f32x2{scv[0], scv[1]}) + f32x2{shv[0], shv[1]}, hb = rbf2(nb * f32x2{scv[2], scv[3]}) + f32x2{shv[2], shv[3]};
        *(LAS v2u*)(L + hbw) = v2u{pack2(ha.x, ha.y), pack2(hb.x, hb.y)};
      }
      BARL();
#pragma unroll
      for (int t = 0; t < 8; ++t) { FRESH(a[t]); }
      if (wv < 4) {
#pragma unroll
        for (int t = 0; t < 8; ++t) a[t] = *(const LAS v4u*)(L + hbr + t * 128);
      }
#pragma unroll
      for (int m2 = 0; m2 < 2; ++m2) {
        const v2u lo = lds_tr(L + (dtr ^ (m2 << 4))), hi = lds_tr(L + (dtr ^ (m2 << 4)) + 4096);
        bd[m2] = v4u{lo.x, lo.y, hi.x, hi.y};
      }
      SB();
      if (wv < 4) {
        f32x4 c = {0.f, 0.f, 0.f, 0.f};
#pragma unroll
        for (int t = 0; t < 8; ++t) c = mfma32(a[t], bg[t], c);
        float gs = (lane & 8) ? c[1] : c[0];
        gs += dpp<ROR8>(gs);
        gs = rbf(gs);
        const float act = rbf(gelu_f(gs)) * dpp<ROR4>(gs);
        if (n < 4) *(LAS unsigned short*)(L + acw) = (unsigned short)pack2(act, 0.f);
      }
      BARL();
      STAMP(14);
      {
        const v4u aa = *(const LAS v4u*)(L + acr);
        const f32x4 c0 = mfma32(aa, bd[0], f32x4{0.f, 0.f, 0.f, 0.f}), c1 = mfma32(aa, bd[1], f32x4{0.f, 0.f, 0.f, 0.f});
        v4u o = put4(f32x4{c0[0], c0[1], c1[0], c1[1]}, tag);
        PIN(o);
        if (live) st16(rws, h4o, o);
      }
      STAMP(15);
    }
  }
  }
  STAMP_OUT();
}

static __device__ __attribute__((noinline, used)) void code_end_marker() { __builtin_amdgcn_s_sleep(1); }

extern "C" void kernel_launch(void* const* d_in, const int* in_sizes, int n_in, void* d_out, int out_size, void* d_ws, size_t ws_size, hipStream_t stream) {
  adaln_block_kernel<<<256, 512, 0, stream>>>((const bf16_t*)d_in[0], (const bf16_t*)d_in[1], (const bf16_t*)d_in[2], (const bf16_t*)d_in[3],
                                              (const bf16_t*)d_in[4], (const bf16_t*)d_in[6], (const bf16_t*)d_in[7], (const bf16_t*)d_in[8],
                                              (const bf16_t*)d_in[9], (const bf16_t*)d_in[10], (bf16_t*)d_out, (unsigned char*)d_ws);
}
```

```cpp
#include <hip/hip_runtime.h>
#include <hip/hip_bf16.h>
#include <stdint.h>

#define LAS __attribute__((address_space(3)))
typedef unsigned short bf16_t;
typedef unsigned v4u __attribute__((ext_vector_type(4)));
typedef unsigned v2u __attribute__((ext_vector_type(2)));
typedef float f32x4 __attribute__((ext_vector_type(4)));
typedef float f32x2 __attribute__((ext_vector_type(2)));
typedef __bf16 bf16x8 __attribute__((ext_vector_type(8)));
typedef __bf16 bf16x2 __attribute__((ext_vector_type(2)));
typedef short s16x4 __attribute__((ext_vector_type(4)));
typedef __amdgpu_buffer_rsrc_t rsrc_t;

#define PMIN 3
#define PLO 4
#define PMID 6
#define PHI 10
#define LV0 (-40)
#define LV1 0
#define LV2 30
#define SCH_T16 247
#define SCH_A 110
#define SCH_F 60
#define SCH_G (-100)
#define T_MOD 380
#define T_MODF 190
#define T_Q 440
#define QSL 260
#define WO_DLY 7
#define H1DLY 25
#define H2DLY 14
#define GDLY 4
#define DLY_H4 34

namespace {
constexpr int SK = 16384, WIN = 4096, FF = 1792;
constexpr int AUX_SC1 = 16, AUX_NT = 2;
constexpr int WS_EPOCH = 0;
constexpr int WS_MT = 2048;
constexpr int WS_X1 = 4096;
constexpr int WS_SB = WS_X1 + 16384;
constexpr int WS_CB = WS_SB + 8192;
constexpr int WS_ST = WS_CB + 8192;
constexpr int WS_MOD = WS_ST + 32768;
constexpr int WS_QP = WS_MOD + 196608;
constexpr int WS_H1 = WS_QP + 524288;
constexpr int WS_H2 = WS_H1 + 524288;
constexpr int WS_H4 = WS_H2 + 262144;
constexpr int WS_END = WS_H4 + 917504;
constexpr int L_K = 0;
constexpr int L_FB = 0;
constexpr int L_WO = 0;
constexpr int L_SC = 32768;
constexpr int L_DN = 49152;
constexpr int L_P = 131072;
constexpr int L_QB = L_P + 4096;
constexpr int L_HQ = L_QB + 1280;
constexpr int L_ZERO = L_HQ + 1024;
constexpr int L_FLAG = L_ZERO + 1024;
constexpr int L_GG = L_FLAG + 256;
constexpr int L_OA = L_GG + 512;
constexpr int L_CS = L_OA + 1280;
constexpr int L_F = L_CS + 128;
constexpr int L_ACT = L_F + 128;
constexpr int L_RED = L_ACT + 128;
constexpr int L_MRG = L_RED + 2048;
constexpr int L_HB = L_MRG + 8192;
constexpr int L_END = L_HB + 4096 + 64;
enum { F_MCNT = 0, F_HCNT = 1, F_QCNT = 2, F_K = 4, F_PCNT = 8, F_G = 10, F_H4 = 12, F_VI = 16, F_CS = 24, F_ANC = 28 };
constexpr unsigned SPIN_G = 1u << 16, SPIN_L = 1u << 20;

__device__ __forceinline__ float bflo(unsigned u) { return __uint_as_float(u << 16); }
__device__ __forceinline__ float bfhi(unsigned u) { return __uint_as_float(u & 0xFFFF0000u); }
__device__ __forceinline__ unsigned pack2(float lo, float hi) {
  f32x2 v = {lo, hi};
  return __builtin_bit_cast(unsigned, __builtin_convertvector(v, bf16x2));
}
__device__ __forceinline__ float dot2(unsigned a, unsigned b, float c) {
  return __builtin_amdgcn_fdot2_f32_bf16(__builtin_bit_cast(bf16x2, a), __builtin_bit_cast(bf16x2, b), c, false);
}
__device__ __forceinline__ float rbf(float x) { return bflo(pack2(x, x) & 0xFFFFu); }
__device__ __forceinline__ float fmx(float a, float b) { return __builtin_amdgcn_fmed3f(a, b, __builtin_inff()); }
template <int CTRL> __device__ __forceinline__ float dpp(float x) {
  return __builtin_bit_cast(float, __builtin_amdgcn_mov_dpp(__builtin_bit_cast(int, x), CTRL, 0xf, 0xf, true));
}
constexpr int XOR1 = 0xB1, XOR2 = 0x4E, ROR4 = 0x124, ROR8 = 0x128;
__device__ __forceinline__ float xrow_sum(float x) {
  auto s = __builtin_amdgcn_permlane16_swap(__float_as_uint(x), __float_as_uint(x), false, false);
  x = __uint_as_float(s[0]) + __uint_as_float(s[1]);
  auto t = __builtin_amdgcn_permlane32_swap(__float_as_uint(x), __float_as_uint(x), false, false);
  return __uint_as_float(t[0]) + __uint_as_float(t[1]);
}
__device__ __forceinline__ float xrow_max(float x) {
  auto s = __builtin_amdgcn_permlane16_swap(__float_as_uint(x), __float_as_uint(x), false, false);
  x = fmx(__uint_as_float(s[0]), __uint_as_float(s[1]));
  auto t = __builtin_amdgcn_permlane32_swap(__float_as_uint(x), __float_as_uint(x), false, false);
  return fmx(__uint_as_float(t[0]), __uint_as_float(t[1]));
}
__device__ __forceinline__ float wave_sum(float x) {
  x += dpp<XOR1>(x); x += dpp<XOR2>(x); x += dpp<ROR4>(x); x += dpp<ROR8>(x);
  return xrow_sum(x);
}
__device__ __forceinline__ f32x4 mfma32(v4u a, v4u b, f32x4 c) {
  return __builtin_amdgcn_mfma_f32_16x16x32_bf16(__builtin_bit_cast(bf16x8, a), __builtin_bit_cast(bf16x8, b), c, 0, 0, 0);
}
__device__ __forceinline__ v4u tcol8r(const v4u* r, int e) {
  const unsigned sel = (e & 1) ? 0x07060302u : 0x05040100u;
  v4u o;
  o.x = __builtin_amdgcn_perm(r[1][e >> 1], r[0][e >> 1], sel);
  o.y = __builtin_amdgcn_perm(r[3][e >> 1], r[2][e >> 1], sel);
  o.z = __builtin_amdgcn_perm(r[5][e >> 1], r[4][e >> 1], sel);
  o.w = __builtin_amdgcn_perm(r[7][e >> 1], r[6][e >> 1], sel);
  return o;
}
#define FOR8(X) X(0) X(1) X(2) X(3) X(4) X(5) X(6) X(7)
__device__ __forceinline__ v2u lds_tr(LAS unsigned char* p) { return __builtin_bit_cast(v2u, __builtin_amdgcn_ds_read_tr16_b64_v4i16((LAS s16x4*)p)); }
__device__ __forceinline__ rsrc_t mkrsrc(const void* p, int bytes) { return __builtin_amdgcn_make_buffer_rsrc((void*)p, 0, bytes, 0x00020000); }
template <int AUX> __device__ __forceinline__ v4u bld(rsrc_t r, int voff, int soff) { return __builtin_amdgcn_raw_buffer_load_b128(r, voff, soff, AUX); }
template <bool NT> __device__ __forceinline__ void dma16(const void* base, int voff, unsigned dst) {
  unsigned keep;
  if (NT) asm volatile("s_mov_b32 %0, m0\n\ts_mov_b32 m0, %3\n\ts_nop 0\n\tglobal_load_lds_dwordx4 %1, %2 nt\n\ts_mov_b32 m0, %0" : "=&s"(keep) : "v"(voff), "s"(base), "s"(dst) : "memory");
  else asm volatile("s_mov_b32 %0, m0\n\ts_mov_b32 m0, %3\n\ts_nop 0\n\tglobal_load_lds_dwordx4 %1, %2\n\ts_mov_b32 m0, %0" : "=&s"(keep) : "v"(voff), "s"(base), "s"(dst) : "memory");
}
__device__ __forceinline__ void dma16v(const void* lane_ptr, unsigned dst) {
  unsigned keep;
  asm volatile("s_mov_b32 %0, m0\n\ts_mov_b32 m0, %2\n\ts_nop 0\n\tglobal_load_lds_dwordx4 %1, off\n\ts_mov_b32 m0, %0" : "=&s"(keep) : "v"(lane_ptr), "s"(dst) : "memory");
}
#define VMCNT(n) asm volatile("s_waitcnt vmcnt(%0)" :: "i"(n) : "memory")
__device__ __forceinline__ unsigned put1(float v, unsigned t) { return (__float_as_uint(v) & ~15u) | (t & 15u); }
__device__ __forceinline__ v4u put4(f32x4 v, unsigned tag) { return v4u{put1(v.x, tag), put1(v.y, tag >> 8), put1(v.z, tag >> 16), put1(v.w, tag >> 24)}; }
__device__ __forceinline__ v4u put4r(f32x4 v, unsigned tag) {
  return put4(__builtin_bit_cast(f32x4, __builtin_bit_cast(v4u, v) + 8u), tag);
}
__device__ __forceinline__ unsigned tagdiff(v4u v, unsigned tag) {
  return (__builtin_amdgcn_perm(v.y, v.x, 0x0c0c0400u) | __builtin_amdgcn_perm(v.w, v.z, 0x04000c0cu)) ^ tag;
}
__device__ __forceinline__ bool all_ok(unsigned diff) { return __all((diff & 0x0F0F0F0Fu) == 0u); }
__device__ __forceinline__ f32x4 get4(v4u v) { return __builtin_bit_cast(f32x4, v & ~15u); }
__device__ __forceinline__ void st16(rsrc_t r, int off, v4u v) { __builtin_amdgcn_raw_buffer_store_b128(v, r, off, 0, AUX_SC1); }
#define CBAR() asm volatile("" ::: "memory")
#define LGKM0() asm volatile("s_waitcnt lgkmcnt(0)" ::: "memory")
#define SB() __builtin_amdgcn_sched_barrier(0)
#define PIN(x) asm volatile("" : "+v"(x))
__device__ __forceinline__ void lds_wait(volatile LAS unsigned* f, unsigned want = 1u) {
#pragma unroll 1
  for (unsigned i = 0; i < SPIN_L; ++i) { if (*f >= want) break; __builtin_amdgcn_s_sleep(1); }
  CBAR();
}
__device__ __forceinline__ void lds_set(volatile LAS unsigned* f) { LGKM0(); *f = 1u; }
__device__ __forceinline__ void lds_arrive(LAS unsigned* f, int lane) {
  LGKM0();
  if (lane == 0) __hip_atomic_fetch_add(f, 1u, __ATOMIC_RELAXED, __HIP_MEMORY_SCOPE_WORKGROUP);
}
__device__ __forceinline__ void wg_barrier() { LGKM0(); __builtin_amdgcn_s_barrier(); CBAR(); }
template <int N, int SLEEP> __device__ __forceinline__ void sweep_raw(rsrc_t rs, const int (&off)[N], v4u (&v)[N], unsigned tag) {
  unsigned spin = 0;
  unsigned long long more;
#pragma unroll 1
  do {
    if (SLEEP && spin) __builtin_amdgcn_s_sleep(SLEEP);
    CBAR();
#pragma unroll
    for (int i = 0; i < N; ++i) v[i] = bld<AUX_SC1>(rs, off[i], 0);
    ++spin;
    unsigned mask = 0x0F0F0F0Fu & (unsigned)((int)(spin - SPIN_G) >> 31);
    asm volatile("" : "+s"(mask));
    unsigned bad = 0;
#pragma unroll
    for (int i = 0; i < N; ++i) bad |= tagdiff(v[i], tag);
    more = __builtin_amdgcn_ballot_w64((bad & mask) != 0u);
  } while (more);
}
template <int N, int SLEEP> __device__ __forceinline__ void sweep(rsrc_t rs, const int (&off)[N], f32x4 (&o)[N], unsigned tag) {
  v4u v[N];
  sweep_raw<N, SLEEP>(rs, off, v, tag);
#pragma unroll
  for (int i = 0; i < N; ++i) o[i] = get4(v[i]);
}
__device__ __forceinline__ float fexp(float x) { return __builtin_amdgcn_exp2f(x * 1.4426950408889634f); }
__device__ __forceinline__ float silu_f(float x) { return x * __builtin_amdgcn_rcpf(1.f + fexp(-x)); }
__device__ __forceinline__ float gelu_f(float x) {
  const float t = x * fmaf(x * x, -0.10294324f, -2.3022082f);
  return x * __builtin_amdgcn_rcpf(1.f + __builtin_amdgcn_exp2f(t));
}
__device__ __forceinline__ void tcol4x8(const v2u* r, v4u* o) {
#pragma unroll
  for (int e = 0; e < 4; ++e) {
    const unsigned sel = (e & 1) ? 0x07060302u : 0x05040100u;
#pragma unroll
    for (int jp = 0; jp < 4; ++jp) o[e][jp] = __builtin_amdgcn_perm(r[2 * jp + 1][e >> 1], r[2 * jp][e >> 1], sel);
    PIN(o[e]);
  }
}
__device__ __forceinline__ f32x4 gemv32x64(v4u a, const v4u* bt) {
  f32x4 c[4];
#pragma unroll
  for (int e = 0; e < 4; ++e) c[e] = mfma32(a, bt[e], f32x4{0.f, 0.f, 0.f, 0.f});
  return f32x4{c[0][0], c[1][0], c[2][0], c[3][0]};
}
__device__ __forceinline__ void wait_until(unsigned long long t0, unsigned ticks) {
#pragma unroll 1
  for (int i = 0; i < 4096 && __builtin_amdgcn_s_memrealtime() - t0 < ticks; ++i) __builtin_amdgcn_s_sleep(1);
}
__device__ __forceinline__ float sel4(bool b0, bool b1, float c0, float c1, float c2, float c3) {
  const float lo = b0 ? c1 : c0, hi = b0 ? c3 : c2;
  return b1 ? hi : lo;
}
__device__ __forceinline__ float part_sum(f32x4 v) {
  auto p = __builtin_amdgcn_permlane32_swap(__float_as_uint(v[0]), __float_as_uint(v[2]), false, false);
  auto q = __builtin_amdgcn_permlane32_swap(__float_as_uint(v[1]), __float_as_uint(v[3]), false, false);
  const float A = __uint_as_float(p[0]) + __uint_as_float(p[1]), B = __uint_as_float(q[0]) + __uint_as_float(q[1]);
  auto r = __builtin_amdgcn_permlane16_swap(__float_as_uint(A), __float_as_uint(B), false, false);
  const float t = __uint_as_float(r[0]) + __uint_as_float(r[1]);
  return t + dpp<ROR8>(t);
}
__device__ __forceinline__ float split_weight(f32x4 m4, f32x4 l4, int hh) {
  const bool h0 = hh & 1, h1 = hh & 2;
  const float m = sel4(h0, h1, m4[0], m4[1], m4[2], m4[3]), l = sel4(h0, h1, l4[0], l4[1], l4[2], l4[3]);
  const float M = xrow_max(fmx(m, dpp<ROR8>(m)));
  const float e = fexp(m - M);
  float Ls = l * e; Ls += dpp<ROR8>(Ls);
  return e * __builtin_amdgcn_rcpf(xrow_sum(Ls));
}
__device__ __forceinline__ f32x2 rbf2(f32x2 v) {
  const unsigned p = pack2(v.x, v.y);
  return f32x2{bflo(p), bfhi(p)};
}
template <bool MAX> __device__ __forceinline__ float red2(float a, float b) { return MAX ? fmx(a, b) : a + b; }
template <bool MAX> __device__ __forceinline__ f32x4 wave_red4(f32x4 s, int lane) {
  const bool o1 = lane & 1, o2 = lane & 2;
  const float a = red2<MAX>(o1 ? s[1] : s[0], dpp<XOR1>(o1 ? s[0] : s[1])), c = red2<MAX>(o1 ? s[3] : s[2], dpp<XOR1>(o1 ? s[2] : s[3]));
  float m = red2<MAX>(o2 ? c : a, dpp<XOR2>(o2 ? a : c));
  m = red2<MAX>(m, dpp<ROR4>(m)); m = red2<MAX>(m, dpp<ROR8>(m));
  m = MAX ? xrow_max(m) : xrow_sum(m);
  return f32x4{dpp<0x00>(m), dpp<0x55>(m), dpp<0xAA>(m), dpp<0xFF>(m)};
}
#define STAMP(i) do {} while (0)
#define STAMP_OUT() do {} while (0)
}

static __device__ __attribute__((noinline, used)) void code_begin_marker() { __builtin_amdgcn_s_sleep(0); }
static __device__ __attribute__((noinline, used)) void code_end_marker();

__global__ void __launch_bounds__(512, 2)
adaln_block_kernel(const bf16_t* __restrict__ x, const bf16_t* __restrict__ kc, const bf16_t* __restrict__ vc,
                   const bf16_t* __restrict__ wq, const bf16_t* __restrict__ wo, const bf16_t* __restrict__ wg,
                   const bf16_t* __restrict__ wu, const bf16_t* __restrict__ wd, const bf16_t* __restrict__ cc,
                   const bf16_t* __restrict__ wmod, bf16_t* __restrict__ out, unsigned char* ws) {
  __shared__ __attribute__((aligned(16))) unsigned char lds_raw[L_END];
  LAS unsigned char* L = (LAS unsigned char*)lds_raw;
  volatile LAS unsigned* FL = (volatile LAS unsigned*)(L + L_FLAG);
  LAS unsigned* FC = (LAS unsigned*)(L + L_FLAG);
  const unsigned long long tstart = __builtin_amdgcn_s_memrealtime();
  const int tid = threadIdx.x, lane = tid & 63, wv = __builtin_amdgcn_readfirstlane(tid >> 6);
  const int g = blockIdx.x, grp = (g & 7) * 4 + (g >> 6), b = grp >> 3, kvh = grp & 7, s = (g >> 3) & 7;
  const int n = lane & 15, kg = lane >> 4;
  if (wv == 3) {
    const unsigned long long c0 = (unsigned long long)(uintptr_t)&code_begin_marker, c1 = (unsigned long long)(uintptr_t)&code_end_marker;
    const int len = (int)(c1 - c0);
    const v4u rc = {(unsigned)c0, (unsigned)(c0 >> 32) & 0xFFFFu, (unsigned)(len > 0 ? len : 0), 0x00020000u};
    unsigned keep;
    asm volatile("s_mov_b32 %0, m0\n\ts_mov_b32 m0, %3\n\ts_nop 0\n\tbuffer_load_dwordx4 %1, %2, 0 offen lds\n\ts_mov_b32 m0, %0"
                 : "=&s"(keep) : "v"((g >> 3) * 1024 + lane * 16), "s"(rc), "s"((unsigned)(uintptr_t)(L + L_HB)) : "memory");
  }
  if (tid < 80) *(LAS v4u*)(L + L_ZERO + tid * 16) = v4u{0u, 0u, 0u, 0u};
  asm volatile("" :: "s"(x), "s"(kc), "s"(vc), "s"(wq), "s"(cc), "s"(wmod), "s"(ws));
  wg_barrier();
  const rsrc_t rws = mkrsrc(ws, WS_END);
  unsigned tag;
  v4u stv[2];
#define W_HEAD ((g & 7) + 8 * ((g - 128) >> 5))
#define W_CQ ((g >> 3) & 3)
#define ST_OFF(row) (WS_ST + (((row) * 8 + (W_HEAD >> 2)) * 8 + (lane >> 3)) * 128)

  if (__builtin_expect(wv >= 4, 1)) {
    const int w = wv - 4;
    const size_t kvbase = ((size_t)grp * SK + (SK - WIN) + s * 512) * 128;
    const int rot = (g * 37) & 127;
    const unsigned char* kbase = (const unsigned char*)(kc + kvbase);
    const unsigned ldsk = (unsigned)(uintptr_t)(L + L_K);
    const int klane = kg * 256 + ((n ^ kg) << 4);
    unsigned due0 = (unsigned)tstart + SCH_A;
#define PACE_WAIT(now, i) do { \
      const int late_ = (int)((now) - due0) - (((i) * SCH_T16) >> 4); \
      VMCNT(PHI); \
      if (late_ < LV2) { VMCNT(PMID); if (late_ < LV1) { VMCNT(PLO); if (late_ < LV0) VMCNT(PMIN); } } } while (0)
    STAMP(7);
#pragma unroll 4
    for (int i = 0; i < 32; ++i) {
      dma16<true>(kbase + ((4 * i + w + rot) & 127) * 1024, klane ^ ((i & 3) << 6), ldsk + (4 * i + w) * 1024);
      if (i < PLO) continue;
      const unsigned now = (unsigned)__builtin_amdgcn_s_memrealtime();
      if (i == PLO + 1 && (int)(now - SCH_F - due0) > 0) due0 = now - SCH_F;
      PACE_WAIT(now, i);
    }
    STAMP(1);
    { const unsigned anc = __builtin_amdgcn_readfirstlane(FL[F_ANC]); if (anc) due0 = anc + SCH_G; }
    const unsigned epoch = __builtin_amdgcn_raw_buffer_load_b32(rws, WS_EPOCH, 0, AUX_SC1);
    const rsrc_t rv = mkrsrc(vc + kvbase, 131072);
    v4u V[32], VT[24], VL[8];
    constexpr int TR0 = 8 + PHI;
#pragma unroll
    for (int i = 0; i < 32; ++i) {
      V[i] = bld<AUX_NT>(rv, lane * 16, ((4 * i + w + rot) & 127) * 1024);
      const unsigned now = (unsigned)__builtin_amdgcn_s_memrealtime();
      if (i >= TR0) { const int k = i - TR0; VT[k] = tcol8r(V + 8 * (k >> 3), k & 7); PIN(VT[k]); }
      if (i == PLO - 1) { VMCNT(PLO); FL[F_K + w] = 1u; STAMP(2); }
      else if (i < 31) PACE_WAIT(now, 32 + i);
      SB();
    }
    FL[F_VI + w] = 1u;
    tag = __builtin_amdgcn_readfirstlane(epoch) + 0x01010101u;
    STAMP(3);
#pragma unroll
    for (int k = 32 - TR0; k < 24; ++k) { VT[k] = tcol8r(V + 8 * (k >> 3), k & 7); PIN(VT[k]); }
    lds_wait(FL + F_PCNT, 4u);
    STAMP(4);
    f32x4 f;
    {
      const LAS f32x4* sp = (const LAS f32x4*)(L + L_F);
      const f32x4 m0 = sp[0], m1 = sp[2], m2 = sp[4], m3 = sp[6], mo = sp[2 * w];
#pragma unroll
      for (int h = 0; h < 4; ++h) f[h] = fexp(mo[h] - fmx(fmx(m0[h], m1[h]), fmx(m2[h], m3[h])));
    }
    f32x4 acc[2] = {f32x4{0.f, 0.f, 0.f, 0.f}, f32x4{0.f, 0.f, 0.f, 0.f}};
    int paddr[4];
#pragma unroll
    for (int ep = 0; ep < 4; ++ep) paddr[ep] = (n >> 2) == ep ? L_P + w * 1024 + (((n & 3) * 4 + kg) << 4) : L_ZERO;
    int mra = L_MRG + (w * 512 + 8 * n + 2 * kg) * 4;
#pragma unroll
    for (int G = 0; G < 4; ++G) {
      v4u pa[4];
#pragma unroll
      for (int ep = 0; ep < 4; ++ep) pa[ep] = *(const LAS v4u*)(L + paddr[ep] + G * 256);
      if (G == 3) {
        PIN(pa[0]); PIN(pa[1]); PIN(pa[2]); PIN(pa[3]); PIN(f); PIN(mra); PIN(acc[0]); PIN(acc[1]);
        STAMP(5);
#pragma unroll
        for (int jp = 0; jp < 4; ++jp) {
#pragma unroll
          for (int e = 0; e < 8; ++e) {
            unsigned t = __builtin_amdgcn_perm(V[25 + 2 * jp][e >> 1], V[24 + 2 * jp][e >> 1], (e & 1) ? 0x07060302u : 0x05040100u);
            if (jp < 3) PIN(t);
            VL[e][jp] = t;
          }
          if (jp < 3) SB();
        }
      }
#define PV(E) acc[E & 1] = mfma32(pa[E >> 1], G < 3 ? VT[8 * G + E] : VL[E], acc[E & 1]);
      FOR8(PV)
#undef PV
      SB();
    }
    STAMP(6);
#pragma unroll
    for (int h = 0; h < 4; ++h) *(LAS f32x2*)(L + mra + h * 512) = f32x2{acc[0][h], acc[1][h]} * f[h];
    wg_barrier();
    SB();
  } else {
    const int cw = wv;
    __builtin_amdgcn_s_setprio(2);
    const int ks = g >> 5, ns = g & 31, u5 = lane & 31, h5 = lane >> 5;
    const int mkq = g < 64 ? g >> 4 : (g - 64) >> 5, mcb = g < 64 ? g & 15 : 16 + ((g - 64) & 31);
    v2u wm[8], wr[8];
    const rsrc_t rm = mkrsrc(wmod + (size_t)(mkq * 128 + cw * 32) * 3072, g < 192 ? 32 * 6144 : 0);
    const rsrc_t rq = mkrsrc(wq + (size_t)(ks * 64 + (cw & 1) * 32) * 4096, 32 * 8192);
    const v4u cf = *(const v4u*)(cc + (n >> 2) * 512 + ((128 * mkq) & 511) + 32 * cw + 8 * kg);
#pragma unroll
    for (int j = 0; j < 8; ++j) wm[j] = __builtin_amdgcn_raw_buffer_load_b64(rm, 8 * kg * 6144 + mcb * 128 + n * 8, j * 6144, AUX_NT);
    const unsigned epoch = __builtin_amdgcn_raw_buffer_load_b32(rws, WS_EPOCH, 0, AUX_SC1);
    const v4u xs = *(const v4u*)(x + cw * 512 + lane * 8);
    const v2u xo = *(const v2u*)(x + cw * 512 + 64 * ks + 4 * n);
#pragma unroll
    for (int j = 0; j < 8; ++j) wr[j] = __builtin_amdgcn_raw_buffer_load_b64(rq, 8 * kg * 8192 + ns * 256 + (cw >> 1) * 128 + n * 8, j * 8192, AUX_NT);
    SB();
    STAMP(1);
    v4u bq[4];
    unsigned long long tfirst;
    {
      v4u a, bt[4];
#pragma unroll
      for (int i = 0; i < 4; ++i) a[i] = pack2(silu_f(bflo(cf[i])), silu_f(bfhi(cf[i])));
      PIN(a);
      tcol4x8(wm, bt);
      tfirst = __builtin_amdgcn_s_memrealtime();
      *(LAS f32x4*)(L + L_MRG + cw * 1024 + lane * 16) = gemv32x64(a, bt);
      lds_arrive(FC + F_MCNT, lane);
      tag = __builtin_amdgcn_readfirstlane(epoch) + 0x01010101u;
      if (cw == 0 && g < 192) {
        lds_wait(FL + F_MCNT, 4u);
        const LAS f32x4* mp = (const LAS f32x4*)(L + L_MRG + lane * 16);
        st16(rws, WS_MOD + ((mkq * 4 + kg) * 3072 + 64 * mcb + 4 * n) * 4, put4r((mp[0] + mp[64]) + (mp[128] + mp[192]), tag));
        if (g < 64 && lane == 0) { const float t = __uint_as_float((unsigned)__builtin_amdgcn_s_memrealtime() << 4); st16(rws, WS_MT + g * 16, put4(f32x4{t, t, t, t}, tag)); }
        STAMP(11);
      }
    }
    float ln[4];
    {
      float sum = 0.f;
#pragma unroll
      for (int i = 0; i < 4; ++i) sum += bflo(xs[i]) + bfhi(xs[i]);
      const float mean = wave_sum(sum) * (1.f / 512.f);
      float sq = 0.f;
#pragma unroll
      for (int i = 0; i < 4; ++i) { const float d0 = bflo(xs[i]) - mean, d1 = bfhi(xs[i]) - mean; sq = fmaf(d0, d0, sq); sq = fmaf(d1, d1, sq); }
      sq = wave_sum(sq);
      const float mb = rbf(mean), den = rbf(sqrtf(rbf(rbf(sq * (1.f / 512.f)) + 1e-5f)));
#pragma unroll
      for (int i = 0; i < 2; ++i) { ln[2 * i] = rbf(rbf(bflo(xo[i]) - mb) / den); ln[2 * i + 1] = rbf(rbf(bfhi(xo[i]) - mb) / den); }
    }
    tcol4x8(wr, bq);
    STAMP(2);
    wait_until(tstart, T_MOD);
    wait_until(tfirst, T_MODF);
    {
      const int mo = WS_MOD + ((h5 * 4 + cw) * 3072 + (kg & 1) * 512 + 64 * ks + 4 * n) * 4;
      const int off[2] = {mo, mo + 2 * 4 * 3072 * 4};
      f32x4 v[2];
      v4u mt;
      if (cw == 3) mt = bld<AUX_SC1>(rws, WS_MT + lane * 16, 0);
      sweep<2, 8>(rws, off, v, tag);
      STAMP(3);
      if (cw == 3) {
        const unsigned now = (unsigned)__builtin_amdgcn_s_memrealtime();
        const int d = (int)(mt.x - (now << 4)) >> 4;
        const bool ok = (tagdiff(mt, tag) & 0x0F0F0F0Fu) == 0u && d <= 0 && d > -2000;
        const float cnt = wave_sum(ok ? 1.f : 0.f), sum = wave_sum(ok ? (float)d : 0.f);
        FL[F_ANC] = (now + (cnt > 0.f ? (int)(sum / cnt) : -300)) | 1u;
      }
      const f32x4 t = v[0] + v[1];
      float h[4];
#pragma unroll
      for (int e = 0; e < 4; ++e) {
        auto p = __builtin_amdgcn_permlane32_swap(__float_as_uint(t[e]), __float_as_uint(t[e]), false, false);
        const float u = rbf(__uint_as_float(p[0]) + __uint_as_float(p[1]));
        auto r = __builtin_amdgcn_permlane16_swap(__float_as_uint(u), __float_as_uint(u), false, false);
        h[e] = rbf(ln[e] * rbf(1.f + __uint_as_float(r[1]))) + __uint_as_float(r[0]);
      }
      if (lane < 16) *(LAS v2u*)(L + L_HQ + cw * 128 + n * 8) = v2u{pack2(h[0], h[1]), pack2(h[2], h[3])};
      lds_arrive(FC + F_HCNT, lane);
      lds_wait(FL + F_HCNT, 4u);
      const v4u a = *(const LAS v4u*)(L + L_HQ + (n >> 2) * 128 + (cw & 1) * 64 + kg * 16);
      const f32x4 qp = gemv32x64(a, bq);
      if (cw & 1) {
        *(LAS f32x4*)(L + L_MRG + 4096 + (cw >> 1) * 1024 + lane * 16) = qp;
        lds_set(FL + F_G + (cw >> 1));
      } else {
        lds_wait(FL + F_G + (cw >> 1));
        st16(rws, WS_QP + ((kg * 8 + ks) * 4096 + 128 * ns + 64 * (cw >> 1) + 4 * n) * 4, put4r(qp + *(const LAS f32x4*)(L + L_MRG + 4096 + (cw >> 1) * 1024 + lane * 16), tag));
      }
    }
    STAMP(4);
    if (cw == 3 && g >= 192) {
      const int rr = g - 192;
      const int mo = WS_MOD + ((((lane >> 3) & 3) * 4 + (rr >> 4)) * 3072 + 1024 + h5 * 512 + 32 * (rr & 15) + 4 * (lane & 7)) * 4;
      const int off[2] = {mo, mo + 4096};
      f32x4 v[2];
      const unsigned short xin = x[rr * 32 + u5];
      sweep<2, 8>(rws, off, v, tag);
#pragma unroll
      for (int i = 0; i < 2; ++i)
#pragma unroll
        for (int e = 0; e < 4; ++e) {
          float u = v[i][e]; u += dpp<ROR8>(u);
          auto p = __builtin_amdgcn_permlane16_swap(__float_as_uint(u), __float_as_uint(u), false, false);
          v[i][e] = rbf(__uint_as_float(p[0]) + __uint_as_float(p[1]));
        }
      *(LAS float*)(L + L_GG + 256 + u5 * 4) = bflo(xin);
      if (u5 < 8) {
        if (lane < 32) {
          *(LAS f32x4*)(L + L_GG + lane * 16) = v[0];
          st16(rws, WS_CB + rr * 128 + lane * 16, put4(f32x4{rbf(1.f + v[1][0]), rbf(1.f + v[1][1]), rbf(1.f + v[1][2]), rbf(1.f + v[1][3])}, tag));
        } else {
          *(LAS f32x4*)(L + L_GG + 128 + u5 * 16) = v[1];
          st16(rws, WS_SB + rr * 128 + u5 * 16, put4(v[0], tag));
        }
      }
    }
    const int qo = WS_QP + ((b * 8 + h5) * 4096 + kvh * 512 + cw * 128 + 4 * u5) * 4;
    {
      unsigned tq = __builtin_amdgcn_readfirstlane(FL[F_ANC]) + T_Q;
      { const int late = (int)(tq - (unsigned)tstart) - T_Q - QSL; if (late > 0) tq += late; }
#pragma unroll 1
      for (int i = 0; i < 4096 && (int)((unsigned)__builtin_amdgcn_s_memrealtime() - tq) < 0; ++i) __builtin_amdgcn_s_sleep(1);
    }
    lds_wait(FL + F_K + cw);
    v4u qv[4];
#pragma unroll
    for (int i = 0; i < 4; ++i) qv[i] = bld<AUX_SC1>(rws, qo, i * 32768);
    SB();
    STAMP(5);
    v4u kf[32];
#pragma unroll
    for (int t = 0; t < 8; ++t)
#pragma unroll
      for (int ds = 0; ds < 4; ++ds)
        kf[4 * t + ds] = *(const LAS v4u*)(L + L_K + (16 * (4 * t + (n >> 2)) + 4 * cw + (n & 3)) * 256 + (((4 * ds + kg) ^ n) << 4));
    SB();
    {
#pragma unroll 1
      for (unsigned spin = 0; spin < SPIN_G; ++spin) {
        if (all_ok(tagdiff(qv[0], tag) | tagdiff(qv[1], tag) | tagdiff(qv[2], tag) | tagdiff(qv[3], tag))) break;
        __builtin_amdgcn_s_sleep(8);
#pragma unroll
        for (int i = 0; i < 4; ++i) if (!all_ok(tagdiff(qv[i], tag))) qv[i] = bld<AUX_SC1>(rws, qo, i * 32768);
      }
      const f32x4 t = (get4(qv[0]) + get4(qv[1])) + (get4(qv[2]) + get4(qv[3]));
      float q[4];
#pragma unroll
      for (int e = 0; e < 4; ++e) {
        auto p = __builtin_amdgcn_permlane32_swap(__float_as_uint(t[e]), __float_as_uint(t[e]), false, false);
        q[e] = __uint_as_float(p[0]) + __uint_as_float(p[1]);
      }
      if (lane < 32) *(LAS v2u*)(L + L_QB + cw * 320 + u5 * 8) = v2u{pack2(q[0], q[1]), pack2(q[2], q[3])};
      lds_arrive(FC + F_QCNT, lane);
      lds_wait(FL + F_QCNT, 4u);
    }
    STAMP(6);
    {
      f32x4 sc[2];
      v4u qf[4];
#pragma unroll
      for (int ds = 0; ds < 4; ++ds) qf[ds] = *(const LAS v4u*)(L + L_QB + (n & 3) * 320 + kg * 16 + ds * 64);
      f32x4 c[8];
#pragma unroll
      for (int t = 0; t < 8; ++t) {
        c[t] = f32x4{0.f, 0.f, 0.f, 0.f};
#pragma unroll
        for (int ds = 0; ds < 4; ++ds) c[t] = mfma32(qf[ds], kf[4 * t + ds], c[t]);
      }
#pragma unroll
      for (int sg = 0; sg < 2; ++sg)
#pragma unroll
        for (int h = 0; h < 4; ++h) sc[sg][h] = sel4(lane & 16, lane & 32, c[4 * sg][h], c[4 * sg + 1][h], c[4 * sg + 2][h], c[4 * sg + 3][h]);
      f32x4 e0, e1, mx;
#pragma unroll
      for (int h = 0; h < 4; ++h) {
        const f32x2 r = rbf2(rbf2(f32x2{sc[0][h], sc[1][h]}) * 0.08838834764831845f);
        e0[h] = r.x; e1[h] = r.y; mx[h] = fmx(r.x, r.y);
      }
      const f32x4 mw = wave_red4<true>(mx, lane);
#pragma unroll
      for (int h = 0; h < 4; ++h) {
        e0[h] = fexp(e0[h] - mw[h]); e1[h] = fexp(e1[h] - mw[h]);
        const unsigned pp = pack2(e0[h], e1[h]);
        LAS unsigned short* pd = (LAS unsigned short*)(L + L_P) + ((((cw * 4 + (kg >> 1)) * 4 + h) * 4 + (n & 3)) * 8 + 4 * (kg & 1) + (n >> 2));
        pd[0] = (unsigned short)(pp & 0xFFFFu);
        pd[2 * 128] = (unsigned short)(pp >> 16);
      }
      const f32x4 lw = wave_red4<false>(e0 + e1, lane);
      if (lane < 2) *(LAS f32x4*)(L + L_F + cw * 32 + lane * 16) = lane == 0 ? mw : lw;
      lds_arrive(FC + F_PCNT, lane);
    }
    STAMP(7);
    lds_wait(FL + F_PCNT, 4u);
    if (cw == 3) {
      const LAS f32x4* sp = (const LAS f32x4*)(L + L_F);
      f32x4 M, Ls;
#pragma unroll
      for (int h = 0; h < 4; ++h) {
        M[h] = fmx(fmx(sp[0][h], sp[2][h]), fmx(sp[4][h], sp[6][h]));
        Ls[h] = (sp[1][h] * fexp(sp[0][h] - M[h]) + sp[3][h] * fexp(sp[2][h] - M[h])) + (sp[5][h] * fexp(sp[4][h] - M[h]) + sp[7][h] * fexp(sp[6][h] - M[h]));
      }
      if (lane < 2) st16(rws, WS_ST + (grp * 8 + s) * 128 + lane * 16, put4(lane == 0 ? M : Ls, tag));
    }
    if (g < 128 && ((g >> 5) << 3) + (g & 7) < 28) {
      const int mo = WS_SB + cw * 2048 + lane * 16;
      const int off[4] = {mo, mo + 1024, mo + 8192, mo + 8192 + 1024};
      f32x4 pre[4];
      sweep<4, 8>(rws, off, pre, tag);
#pragma unroll
      for (int i = 0; i < 4; ++i) *(LAS f32x4*)(L + L_SC + (i >> 1) * 8192 + cw * 2048 + (i & 1) * 1024 + lane * 16) = pre[i];
    }
    STAMP(8);
    if (g >= 128) {
      const unsigned char* wsrc = (const unsigned char*)(wo + (size_t)(W_HEAD * 128 + cw * 32) * 512 + W_CQ * 128);
      const int vo = kg * 1024 + ((n ^ (kg << 1)) << 4), sto = ST_OFF(cw);
      lds_wait(FL + F_VI + cw);
      __builtin_amdgcn_s_sleep(WO_DLY);
      stv[0] = bld<AUX_SC1>(rws, sto, 0); stv[1] = bld<AUX_SC1>(rws, sto + 16, 0);
#pragma unroll
      for (int j = 0; j < 8; ++j) dma16<true>(wsrc + j * 4096, vo ^ ((j & 2) << 6), (unsigned)(uintptr_t)(L + L_WO) + cw * 8192 + j * 1024);
      SB();
    }
    int h1o = WS_H1 + (grp * 8 + s) * 2048 + tid * 16;
    PIN(h1o);
    STAMP(9);
    wg_barrier();
    if (cw < 2) {
      const LAS f32x4* mp = (const LAS f32x4*)(L + L_MRG + tid * 16);
      st16(rws, h1o, put4((mp[0] + mp[128]) + (mp[256] + mp[384]), tag));
    }
    SB();
  }
  STAMP(10);
  if (g >= 128) {
    const unsigned long long tp = __builtin_amdgcn_s_memrealtime();
    const int whead = W_HEAD, wcq = W_CQ;
    int csa = L_CS + (wv >> 1) * 32 + (lane >> 3) * 4;
    int h2o = WS_H2 + ((kg * 16 + wcq * 4 + wv) * 32 + whead) * 128 + n * 8;
    int oar = L_OA + (n >> 2) * 320 + kg * 16, oaw = L_OA + (wv >> 1) * 320 + (wv & 1) * 128 + n * 8 + kg * 2;
    int wtr = L_WO + (8 * kg + (n >> 2)) * 256 + (((4 * wv + ((n >> 1) & 1)) ^ (((n >> 2) | ((kg & 1) << 2)) << 1)) << 4) + (n & 1) * 8, wtr1 = wtr ^ 32;
    unsigned tg2 = tag >> ((n & 1) * 16);
    PIN(h2o); PIN(oar); PIN(oaw); PIN(csa); PIN(tg2); PIN(wtr); PIN(wtr1);
    SB();
    if (wv < 4) {
      if (!all_ok(tagdiff(stv[0], tag) | tagdiff(stv[1], tag))) {
        const int so[2] = {ST_OFF(wv), ST_OFF(wv) + 16};
        sweep_raw<2, 0>(rws, so, stv, tag);
      }
      *(LAS float*)(L + L_CS + wv * 32 + (lane >> 3) * 4) = split_weight(get4(stv[0]), get4(stv[1]), whead);
      CBAR();
      FL[F_CS + wv] = 1u;
    }
    {
      const int ho = WS_H1 + (((wv >> 1) * 8 + (whead >> 2)) * 8 + (lane >> 3)) * 2048 + (whead & 3) * 512 + (wv & 1) * 256 + (lane & 7) * 16;
      const int off[2] = {ho, ho + 128};
      f32x4 v[2];
      wait_until(tp, H1DLY);
      sweep<2, 2>(rws, off, v, tag);
      float cs;
#pragma unroll 1
      for (unsigned i = 0; i < SPIN_L; ++i) {
        const unsigned fl = FL[F_CS + (wv >> 1)];
        cs = *(const volatile LAS float*)(L + csa);
        if (fl) break;
      }
      const float o0 = part_sum(v[0] * cs), o1 = part_sum(v[1] * cs);
      *(LAS unsigned short*)(L + oaw) = (unsigned short)pack2((lane & 8) ? o1 : o0, 0.f);
    }
    VMCNT(0);
    wg_barrier();
    STAMP(11);
    if (wv < 4) {
      v4u a[4]; v2u bl[8], bh[8];
#pragma unroll
      for (int k = 0; k < 4; ++k) {
        a[k] = *(const LAS v4u*)(L + oar + k * 64);
        bl[2 * k] = lds_tr(L + wtr + k * 8192); bh[2 * k] = lds_tr(L + wtr + k * 8192 + 1024);
        bl[2 * k + 1] = lds_tr(L + wtr1 + k * 8192); bh[2 * k + 1] = lds_tr(L + wtr1 + k * 8192 + 1024);
      }
      SB();
      f32x4 c0 = {0.f, 0.f, 0.f, 0.f}, c1 = {0.f, 0.f, 0.f, 0.f};
#pragma unroll
      for (int k = 0; k < 4; ++k) {
        c0 = mfma32(a[k], v4u{bl[2 * k].x, bl[2 * k].y, bh[2 * k].x, bh[2 * k].y}, c0);
        c1 = mfma32(a[k], v4u{bl[2 * k + 1].x, bl[2 * k + 1].y, bh[2 * k + 1].x, bh[2 * k + 1].y}, c1);
      }
      __builtin_amdgcn_raw_buffer_store_b64(v2u{put1(c0[0], tg2), put1(c1[0], tg2 >> 8)}, rws, h2o, 0, AUX_SC1);
    }
    SB();
    STAMP(12);
    if (g >= 192) {
      const int rr = g - 192, col = 16 * ((lane >> 2) & 1) + 4 * kg + (lane & 3);
      float x1 = 0.f, gb = 0.f;
      int oo = (rr * 32 + col) * 2, rda = L_RED + col * 4;
      PIN(oo); PIN(rda);
      if (wv == 0) {
        const int pc = 2 * (lane & 7) + (kg >> 1) + 16 * (kg & 1);
        float ga = *(const LAS float*)(L + L_GG + pc * 4), xin = *(const LAS float*)(L + L_GG + 256 + pc * 4);
        gb = *(const LAS float*)(L + L_GG + 128 + col * 4);
        int x1o = WS_X1 + (n >> 3) * 8192 + rr * 128 + pc * 4;
        unsigned tg1 = tag >> ((pc & 3) * 8);
        PIN(ga); PIN(xin); PIN(x1o); PIN(gb); PIN(tg1);
        const int ho = WS_H2 + rr * 4096 + lane * 16;
        const int off[4] = {ho, ho + 1024, ho + 2048, ho + 3072};
        f32x4 v[4];
        __builtin_amdgcn_s_sleep(H2DLY);
        sweep<4, 0>(rws, off, v, tag);
        STAMP(13);
        x1 = rbf(xin + rbf(ga * rbf(part_sum((v[0] + v[1]) + (v[2] + v[3])))));
        __builtin_amdgcn_raw_buffer_store_b32(put1(x1, tg1), rws, x1o, 0, AUX_SC1);
        SB();
        *(LAS float*)(L + L_GG + 384 + pc * 4) = x1;
        LGKM0();
        x1 = *(const LAS float*)(L + L_GG + 384 + col * 4);
        PIN(x1);
      }
      STAMP(14);
      if (wv == 7) {
        const int off[1] = {WS_X1 + (g & 1) * 8192 + lane * 128};
        v4u v[1];
        sweep_raw<1, 0>(rws, off, v, tag);
        STAMP(13);
        __builtin_amdgcn_s_sleep(DLY_H4);
        FL[F_H4] = 1u;
      } else {
        const int off[2] = {WS_H4 + rr * 14336 + wv * 2048 + lane * 16, WS_H4 + rr * 14336 + wv * 2048 + 1024 + lane * 16};
        f32x4 v[2];
        lds_wait(FL + F_H4);
        sweep<2, 0>(rws, off, v, tag);
        *(LAS float*)(L + rda + wv * 128) = part_sum(v[0] + v[1]);
      }
      wg_barrier();
      if (wv == 0) {
        const LAS float* rp = (const LAS float*)(L + rda);
        const float a = ((rp[0] + rp[32]) + (rp[64] + rp[96])) + ((rp[128] + rp[160]) + rp[192]);
        const unsigned ob = pack2(x1 + rbf(gb * rbf(a)), 0.f);
        if (n < 8) __builtin_amdgcn_raw_buffer_store_b16((unsigned short)ob, mkrsrc(out, 4096), oo, 0, AUX_SC1);
        if (g == 192 && lane == 0) __builtin_amdgcn_raw_buffer_store_b32(tag, rws, WS_EPOCH, 0, AUX_SC1);
      }
      STAMP(15);
    }
  } else if (((g >> 5) << 3) + (g & 7) < 28) {
    const int jsl = 4 * (((g >> 5) << 3) + (g & 7)) + ((g >> 3) & 3);
    __builtin_amdgcn_s_barrier();
    __builtin_amdgcn_s_sleep(GDLY);
    {
      const unsigned char* gsrc = (const unsigned char*)(((lane & 2) ? wu : wg) + (size_t)(32 * ((lane >> 2) & 1) + 8 * (lane >> 5) + ((lane >> 3) & 3)) * FF + 16 * jsl + 8 * ((lane ^ (lane >> 4)) & 1));
#pragma unroll
      for (int j = 0; j < 4; ++j) {
        const int k = (8 * j + wv + 8 * ((g >> 3) & 3)) & 31;
        dma16v(gsrc + (64 * (k >> 2) + 4 * ((k >> 1) & 1) + 16 * (k & 1)) * (FF * 2), (unsigned)(uintptr_t)(L + L_FB) + k * 1024);
      }
#pragma unroll
      for (int j = 0; j < 2; ++j)
        dma16<true>(wd + (size_t)(16 * jsl + 2 * wv + j) * 512, (lane ^ (j | ((wv & 1) << 3))) << 4, (unsigned)(uintptr_t)(L + L_DN) + (2 * wv + j) * 1024);
    }
    v4u bg[8], bd[2], a[8];
    const int rb = wv >> 1, hf = wv & 1;
    f32x4 shv = *(const LAS f32x4*)(L + L_SC + rb * 2048 + hf * 1024 + lane * 16), scv = *(const LAS f32x4*)(L + L_SC + 8192 + rb * 2048 + hf * 1024 + lane * 16);
    PIN(shv); PIN(scv);
    int hbw = L_HB + rb * 1024 + (rb >> 1) * 32 + hf * 512 + lane * 8;
    int hbr = L_HB + (n >> 2) * 1024 + (n >> 3) * 32 + (32 * (n & 1) + 8 * kg) * 2;
    int acr = ((n & 1) == (kg >> 1)) ? L_ACT + (n >> 2) * 32 + (kg & 1) * 16 : L_ZERO, acw = L_ACT + kg * 32 + wv * 8 + (n & 3) * 2;
    int h4o = WS_H4 + ((kg * 16 + 2 * wv + (n >> 3)) * 112 + jsl) * 128 + (n & 7) * 16;
    int dtr = L_DN + (8 * (kg & 1) + (n >> 2)) * 1024 + (((8 * wv + 2 * (n & 3)) ^ (((n >> 2) & 1) | (n & 8))) << 4) + (kg >> 1) * 8;
    PIN(hbw); PIN(hbr); PIN(acr); PIN(acw); PIN(h4o); PIN(dtr);
    VMCNT(2);
    wg_barrier();
    STAMP(12);
    if (wv < 4) {
      LAS unsigned char* gt = L + L_FB + (kg >> 1) * 1024 + (lane & 31) * 32 + (((wv >> 1) ^ (n >> 3)) << 4) + (wv & 1) * 8;
#pragma unroll
      for (int t = 0; t < 8; ++t) {
        const v2u lo = lds_tr(gt + t * 4096), hi = lds_tr(gt + t * 4096 + 2048);
        bg[t] = v4u{lo.x, lo.y, hi.x, hi.y};
      }
    }
    SB();
    {
      const int xr = WS_X1 + (g & 1) * 8192 + rb * 2048 + lane * 16;
      const int off[2] = {xr + hf * 1024, xr + (hf ^ 1) * 1024};
      v4u r[2];
      sweep_raw<2, 0>(rws, off, r, tag);
      STAMP(13);
      const unsigned p0 = __builtin_amdgcn_perm(r[0].y, r[0].x, 0x07060302u), p1 = __builtin_amdgcn_perm(r[0].w, r[0].z, 0x07060302u);
      const unsigned p2 = __builtin_amdgcn_perm(r[1].y, r[1].x, 0x07060302u), p3 = __builtin_amdgcn_perm(r[1].w, r[1].z, 0x07060302u);
      const float s1 = dot2(p1, 0x3F803F80u, dot2(p0, 0x3F803F80u, 0.f)) + dot2(p3, 0x3F803F80u, dot2(p2, 0x3F803F80u, 0.f));
      const float s2 = dot2(p1, p1, dot2(p0, p0, 0.f)) + dot2(p3, p3, dot2(p2, p2, 0.f));
      auto sw = __builtin_amdgcn_permlane32_swap(__float_as_uint(s1), __float_as_uint(s2), false, false);
      float t = __uint_as_float(sw[0]) + __uint_as_float(sw[1]);
      t += dpp<XOR1>(t); t += dpp<XOR2>(t); t += dpp<ROR4>(t); t += dpp<ROR8>(t);
      const float mean = (__uint_as_float(__builtin_amdgcn_readlane(__float_as_uint(t), 0)) + __uint_as_float(__builtin_amdgcn_readlane(__float_as_uint(t), 16))) * (1.f / 512.f);
      const float msq = (__uint_as_float(__builtin_amdgcn_readlane(__float_as_uint(t), 32)) + __uint_as_float(__builtin_amdgcn_readlane(__float_as_uint(t), 48))) * (1.f / 512.f);
      const float mb = rbf(mean), rden = __builtin_amdgcn_rcpf(rbf(__builtin_amdgcn_sqrtf(rbf(rbf(fmx(fmaf(-mean, mean, msq), 0.f)) + 1e-5f))));
      const f32x2 da = rbf2(f32x2{bflo(p0), bfhi(p0)} - mb), db = rbf2(f32x2{bflo(p1), bfhi(p1)} - mb);
      const f32x2 na = rbf2(da * rden), nb = rbf2(db * rden);
      const f32x2 ha = rbf2(na * f32x2{scv[0], scv[1]}) + f32x2{shv[0], shv[1]}, hb = rbf2(nb * f32x2{scv[2], scv[3]}) + f32x2{shv[2], shv[3]};
      *(LAS v2u*)(L + hbw) = v2u{pack2(ha.x, ha.y), pack2(hb.x, hb.y)};
    }
    VMCNT(0);
    wg_barrier();
    if (wv < 4) {
#pragma unroll
      for (int t = 0; t < 8; ++t) a[t] = *(const LAS v4u*)(L + hbr + t * 128);
    }
#pragma unroll
    for (int m2 = 0; m2 < 2; ++m2) {
      const v2u lo = lds_tr(L + (dtr ^ (m2 << 4))), hi = lds_tr(L + (dtr ^ (m2 << 4)) + 4096);
      bd[m2] = v4u{lo.x, lo.y, hi.x, hi.y};
    }
    SB();
    if (wv < 4) {
      f32x4 c = {0.f, 0.f, 0.f, 0.f};
#pragma unroll
      for (int t = 0; t < 8; ++t) c = mfma32(a[t], bg[t], c);
      float gs = (lane & 8) ? c[1] : c[0];
      gs += dpp<ROR8>(gs);
      gs = rbf(gs);
      const float act = rbf(gelu_f(gs)) * dpp<ROR4>(gs);
      if (n < 4) *(LAS unsigned short*)(L + acw) = (unsigned short)pack2(act, 0.f);
    }
    wg_barrier();
    STAMP(14);
    {
      const v4u aa = *(const LAS v4u*)(L + acr);
      const f32x4 c0 = mfma32(aa, bd[0], f32x4{0.f, 0.f, 0.f, 0.f}), c1 = mfma32(aa, bd[1], f32x4{0.f, 0.f, 0.f, 0.f});
      st16(rws, h4o, put4(f32x4{c0[0], c0[1], c1[0], c1[1]}, tag));
    }
    STAMP(15);
  }
  STAMP_OUT();
}

static __device__ __attribute__((noinline, used)) void code_end_marker() { __builtin_amdgcn_s_sleep(1); }

extern "C" void kernel_launch(void* const* d_in, const int* in_sizes, int n_in, void* d_out, int out_size, void* d_ws, size_t ws_size, hipStream_t stream) {
  adaln_block_kernel<<<256, 512, 0, stream>>>((const bf16_t*)d_in[0], (const bf16_t*)d_in[1], (const bf16_t*)d_in[2], (const bf16_t*)d_in[3],
                                              (const bf16_t*)d_in[4], (const bf16_t*)d_in[6], (const bf16_t*)d_in[7], (const bf16_t*)d_in[8],
                                              (const bf16_t*)d_in[9], (const bf16_t*)d_in[10], (bf16_t*)d_out, (unsigned char*)d_ws);
}
```

```cpp
#include <hip/hip_runtime.h>
#include <hip/hip_bf16.h>
#include <stdint.h>

#define LAS __attribute__((address_space(3)))
typedef unsigned short bf16_t;
typedef unsigned v4u __attribute__((ext_vector_type(4)));
typedef unsigned v2u __attribute__((ext_vector_type(2)));
typedef float f32x4 __attribute__((ext_vector_type(4)));
typedef float f32x2 __attribute__((ext_vector_type(2)));
typedef __bf16 bf16x8 __attribute__((ext_vector_type(8)));
typedef __bf16 bf16x2 __attribute__((ext_vector_type(2)));
typedef short s16x4 __attribute__((ext_vector_type(4)));
typedef __amdgpu_buffer_rsrc_t rsrc_t;

#define PMIN 3
#define PLO 4
#define PMID 6
#define PHI 10
#define LV0 (-40)
#define LV1 0
#define LV2 30
#define SCH_T16 247
#define SCH_A 110
#define SCH_F 60
#define SCH_G (-100)
#define T_MOD 380
#define T_MODF 190
#define T_Q 440
#define QSL 260
#define WO_DLY 7
#define H1DLY 25
#define H2DLY 14
#define DLY_H4 34

namespace {
constexpr int SK = 16384, WIN = 4096, FF = 1792;
constexpr int AUX_SC1 = 16, AUX_NT = 2;
constexpr int WS_EPOCH = 0;
constexpr int WS_MT = 2048;
constexpr int WS_X1 = 4096;
constexpr int WS_SB = WS_X1 + 16384;
constexpr int WS_CB = WS_SB + 8192;
constexpr int WS_ST = WS_CB + 8192;
constexpr int WS_MOD = WS_ST + 32768;
constexpr int WS_QP = WS_MOD + 196608;
constexpr int WS_H1 = WS_QP + 524288;
constexpr int WS_H2 = WS_H1 + 524288;
constexpr int WS_H4 = WS_H2 + 262144;
constexpr int WS_END = WS_H4 + 917504;
constexpr int L_K = 0;
constexpr int L_FB = 0;
constexpr int L_WO = 0;
constexpr int L_DN = 32768;
constexpr int L_P = 131072;
constexpr int L_QB = L_P + 4096;
constexpr int L_HQ = L_QB + 1280;
constexpr int L_ZERO = L_HQ + 1024;
constexpr int L_FLAG = L_ZERO + 1024;
constexpr int L_GG = L_FLAG + 256;
constexpr int L_OA = L_GG + 512;
constexpr int L_CS = L_OA + 1280;
constexpr int L_F = L_CS + 128;
constexpr int L_ACT = L_F + 128;
constexpr int L_RED = L_ACT + 128;
constexpr int L_MRG = L_RED + 2048;
constexpr int L_HB = L_MRG + 8192;
constexpr int L_END = L_HB + 4096 + 64;
enum { F_MCNT = 0, F_HCNT = 1, F_QCNT = 2, F_K = 4, F_PCNT = 8, F_G = 10, F_H4 = 12, F_VI = 16, F_CS = 24, F_ANC = 28 };
constexpr unsigned SPIN_G = 1u << 16, SPIN_L = 1u << 20;

__device__ __forceinline__ float bflo(unsigned u) { return __uint_as_float(u << 16); }
__device__ __forceinline__ float bfhi(unsigned u) { return __uint_as_float(u & 0xFFFF0000u); }
__device__ __forceinline__ unsigned pack2(float lo, float hi) {
  f32x2 v = {lo, hi};
  return __builtin_bit_cast(unsigned, __builtin_convertvector(v, bf16x2));
}
__device__ __forceinline__ float dot2(unsigned a, unsigned b, float c) {
  return __builtin_amdgcn_fdot2_f32_bf16(__builtin_bit_cast(bf16x2, a), __builtin_bit_cast(bf16x2, b), c, false);
}
__device__ __forceinline__ float rbf(float x) { return bflo(pack2(x, x) & 0xFFFFu); }
__device__ __forceinline__ float fmx(float a, float b) { return __builtin_amdgcn_fmed3f(a, b, __builtin_inff()); }
template <int CTRL> __device__ __forceinline__ float dpp(float x) {
  return __builtin_bit_cast(float, __builtin_amdgcn_mov_dpp(__builtin_bit_cast(int, x), CTRL, 0xf, 0xf, true));
}
constexpr int XOR1 = 0xB1, XOR2 = 0x4E, ROR4 = 0x124, ROR8 = 0x128;
__device__ __forceinline__ float xrow_sum(float x) {
  auto s = __builtin_amdgcn_permlane16_swap(__float_as_uint(x), __float_as_uint(x), false, false);
  x = __uint_as_float(s[0]) + __uint_as_float(s[1]);
  auto t = __builtin_amdgcn_permlane32_swap(__float_as_uint(x), __float_as_uint(x), false, false);
  return __uint_as_float(t[0]) + __uint_as_float(t[1]);
}
__device__ __forceinline__ float xrow_max(float x) {
  auto s = __builtin_amdgcn_permlane16_swap(__float_as_uint(x), __float_as_uint(x), false, false);
  x = fmx(__uint_as_float(s[0]), __uint_as_float(s[1]));
  auto t = __builtin_amdgcn_permlane32_swap(__float_as_uint(x), __float_as_uint(x), false, false);
  return fmx(__uint_as_float(t[0]), __uint_as_float(t[1]));
}
__device__ __forceinline__ float wave_sum(float x) {
  x += dpp<XOR1>(x); x += dpp<XOR2>(x); x += dpp<ROR4>(x); x += dpp<ROR8>(x);
  return xrow_sum(x);
}
__device__ __forceinline__ f32x4 mfma32(v4u a, v4u b, f32x4 c) {
  return __builtin_amdgcn_mfma_f32_16x16x32_bf16(__builtin_bit_cast(bf16x8, a), __builtin_bit_cast(bf16x8, b), c, 0, 0, 0);
}
__device__ __forceinline__ v4u tcol8r(const v4u* r, int e) {
  const unsigned sel = (e & 1) ? 0x07060302u : 0x05040100u;
  v4u o;
  o.x = __builtin_amdgcn_perm(r[1][e >> 1], r[0][e >> 1], sel);
  o.y = __builtin_amdgcn_perm(r[3][e >> 1], r[2][e >> 1], sel);
  o.z = __builtin_amdgcn_perm(r[5][e >> 1], r[4][e >> 1], sel);
  o.w = __builtin_amdgcn_perm(r[7][e >> 1], r[6][e >> 1], sel);
  return o;
}
#define FOR8(X) X(0) X(1) X(2) X(3) X(4) X(5) X(6) X(7)
__device__ __forceinline__ v2u lds_tr(LAS unsigned char* p) { return __builtin_bit_cast(v2u, __builtin_amdgcn_ds_read_tr16_b64_v4i16((LAS s16x4*)p)); }
__device__ __forceinline__ rsrc_t mkrsrc(const void* p, int bytes) { return __builtin_amdgcn_make_buffer_rsrc((void*)p, 0, bytes, 0x00020000); }
template <int AUX> __device__ __forceinline__ v4u bld(rsrc_t r, int voff, int soff) { return __builtin_amdgcn_raw_buffer_load_b128(r, voff, soff, AUX); }
template <bool NT> __device__ __forceinline__ void dma16(const void* base, int voff, unsigned dst) {
  unsigned keep;
  if (NT) asm volatile("s_mov_b32 %0, m0\n\ts_mov_b32 m0, %3\n\ts_nop 0\n\tglobal_load_lds_dwordx4 %1, %2 nt\n\ts_mov_b32 m0, %0" : "=&s"(keep) : "v"(voff), "s"(base), "s"(dst) : "memory");
  else asm volatile("s_mov_b32 %0, m0\n\ts_mov_b32 m0, %3\n\ts_nop 0\n\tglobal_load_lds_dwordx4 %1, %2\n\ts_mov_b32 m0, %0" : "=&s"(keep) : "v"(voff), "s"(base), "s"(dst) : "memory");
}
__device__ __forceinline__ void dma16v(const void* lane_ptr, unsigned dst) {
  unsigned keep;
  asm volatile("s_mov_b32 %0, m0\n\ts_mov_b32 m0, %2\n\ts_nop 0\n\tglobal_load_lds_dwordx4 %1, off\n\ts_mov_b32 m0, %0" : "=&s"(keep) : "v"(lane_ptr), "s"(dst) : "memory");
}
#define VMCNT(n) asm volatile("s_waitcnt vmcnt(%0)" :: "i"(n) : "memory")
__device__ __forceinline__ unsigned put1(float v, unsigned t) { return (__float_as_uint(v) & ~15u) | (t & 15u); }
__device__ __forceinline__ v4u put4(f32x4 v, unsigned tag) { return v4u{put1(v.x, tag), put1(v.y, tag >> 8), put1(v.z, tag >> 16), put1(v.w, tag >> 24)}; }
__device__ __forceinline__ v4u put4r(f32x4 v, unsigned tag) {
  return put4(__builtin_bit_cast(f32x4, __builtin_bit_cast(v4u, v) + 8u), tag);
}
__device__ __forceinline__ unsigned tagdiff(v4u v, unsigned tag) {
  return (__builtin_amdgcn_perm(v.y, v.x, 0x0c0c0400u) | __builtin_amdgcn_perm(v.w, v.z, 0x04000c0cu)) ^ tag;
}
__device__ __forceinline__ bool all_ok(unsigned diff) { return __all((diff & 0x0F0F0F0Fu) == 0u); }
__device__ __forceinline__ f32x4 get4(v4u v) { return __builtin_bit_cast(f32x4, v & ~15u); }
__device__ __forceinline__ void st16(rsrc_t r, int off, v4u v) { __builtin_amdgcn_raw_buffer_store_b128(v, r, off, 0, AUX_SC1); }
#define CBAR() asm volatile("" ::: "memory")
#define LGKM0() asm volatile("s_waitcnt lgkmcnt(0)" ::: "memory")
#define SB() __builtin_amdgcn_sched_barrier(0)
#define PIN(x) asm volatile("" : "+v"(x))
__device__ __forceinline__ void lds_wait(volatile LAS unsigned* f, unsigned want = 1u) {
#pragma unroll 1
  for (unsigned i = 0; i < SPIN_L; ++i) { if (*f >= want) break; __builtin_amdgcn_s_sleep(1); }
  CBAR();
}
__device__ __forceinline__ void lds_set(volatile LAS unsigned* f) { LGKM0(); *f = 1u; }
__device__ __forceinline__ void lds_arrive(LAS unsigned* f, int lane) {
  LGKM0();
  if (lane == 0) __hip_atomic_fetch_add(f, 1u, __ATOMIC_RELAXED, __HIP_MEMORY_SCOPE_WORKGROUP);
}
__device__ __forceinline__ void wg_barrier() { LGKM0(); __builtin_amdgcn_s_barrier(); CBAR(); }
template <int N, int SLEEP> __device__ __forceinline__ void sweep_raw(rsrc_t rs, const int (&off)[N], v4u (&v)[N], unsigned tag) {
  unsigned spin = 0;
  unsigned long long more;
#pragma unroll 1
  do {
    if (SLEEP && spin) __builtin_amdgcn_s_sleep(SLEEP);
    CBAR();
#pragma unroll
    for (int i = 0; i < N; ++i) v[i] = bld<AUX_SC1>(rs, off[i], 0);
    ++spin;
    unsigned mask = 0x0F0F0F0Fu & (unsigned)((int)(spin - SPIN_G) >> 31);
    asm volatile("" : "+s"(mask));
    unsigned bad = 0;
#pragma unroll
    for (int i = 0; i < N; ++i) bad |= tagdiff(v[i], tag);
    more = __builtin_amdgcn_ballot_w64((bad & mask) != 0u);
  } while (more);
}
template <int N, int SLEEP> __device__ __forceinline__ void sweep(rsrc_t rs, const int (&off)[N], f32x4 (&o)[N], unsigned tag) {
  v4u v[N];
  sweep_raw<N, SLEEP>(rs, off, v, tag);
#pragma unroll
  for (int i = 0; i < N; ++i) o[i] = get4(v[i]);
}
__device__ __forceinline__ float fexp(float x) { return __builtin_amdgcn_exp2f(x * 1.4426950408889634f); }
__device__ __forceinline__ float silu_f(float x) { return x * __builtin_amdgcn_rcpf(1.f + fexp(-x)); }
__device__ __forceinline__ float gelu_f(float x) {
  const float t = x * fmaf(x * x, -0.10294324f, -2.3022082f);
  return x * __builtin_amdgcn_rcpf(1.f + __builtin_amdgcn_exp2f(t));
}
__device__ __forceinline__ void tcol4x8(const v2u* r, v4u* o) {
#pragma unroll
  for (int e = 0; e < 4; ++e) {
    const unsigned sel = (e & 1) ? 0x07060302u : 0x05040100u;
#pragma unroll
    for (int jp = 0; jp < 4; ++jp) o[e][jp] = __builtin_amdgcn_perm(r[2 * jp + 1][e >> 1], r[2 * jp][e >> 1], sel);
    PIN(o[e]);
  }
}
__device__ __forceinline__ f32x4 gemv32x64(v4u a, const v4u* bt) {
  f32x4 c[4];
#pragma unroll
  for (int e = 0; e < 4; ++e) c[e] = mfma32(a, bt[e], f32x4{0.f, 0.f, 0.f, 0.f});
  return f32x4{c[0][0], c[1][0], c[2][0], c[3][0]};
}
__device__ __forceinline__ void wait_until(unsigned long long t0, unsigned ticks) {
#pragma unroll 1
  for (int i = 0; i < 4096 && __builtin_amdgcn_s_memrealtime() - t0 < ticks; ++i) __builtin_amdgcn_s_sleep(1);
}
__device__ __forceinline__ float sel4(bool b0, bool b1, float c0, float c1, float c2, float c3) {
  const float lo = b0 ? c1 : c0, hi = b0 ? c3 : c2;
  return b1 ? hi : lo;
}
__device__ __forceinline__ float part_sum(f32x4 v) {
  auto p = __builtin_amdgcn_permlane32_swap(__float_as_uint(v[0]), __float_as_uint(v[2]), false, false);
  auto q = __builtin_amdgcn_permlane32_swap(__float_as_uint(v[1]), __float_as_uint(v[3]), false, false);
  const float A = __uint_as_float(p[0]) + __uint_as_float(p[1]), B = __uint_as_float(q[0]) + __uint_as_float(q[1]);
  auto r = __builtin_amdgcn_permlane16_swap(__float_as_uint(A), __float_as_uint(B), false, false);
  const float t = __uint_as_float(r[0]) + __uint_as_float(r[1]);
  return t + dpp<ROR8>(t);
}
__device__ __forceinline__ float split_weight(f32x4 m4, f32x4 l4, int hh) {
  const bool h0 = hh & 1, h1 = hh & 2;
  const float m = sel4(h0, h1, m4[0], m4[1], m4[2], m4[3]), l = sel4(h0, h1, l4[0], l4[1], l4[2], l4[3]);
  const float M = xrow_max(fmx(m, dpp<ROR8>(m)));
  const float e = fexp(m - M);
  float Ls = l * e; Ls += dpp<ROR8>(Ls);
  return e * __builtin_amdgcn_rcpf(xrow_sum(Ls));
}
__device__ __forceinline__ f32x2 rbf2(f32x2 v) {
  const unsigned p = pack2(v.x, v.y);
  return f32x2{bflo(p), bfhi(p)};
}
template <bool MAX> __device__ __forceinline__ float red2(float a, float b) { return MAX ? fmx(a, b) : a + b; }
template <bool MAX> __device__ __forceinline__ f32x4 wave_red4(f32x4 s, int lane) {
  const bool o1 = lane & 1, o2 = lane & 2;
  const float a = red2<MAX>(o1 ? s[1] : s[0], dpp<XOR1>(o1 ? s[0] : s[1])), c = red2<MAX>(o1 ? s[3] : s[2], dpp<XOR1>(o1 ? s[2] : s[3]));
  float m = red2<MAX>(o2 ? c : a, dpp<XOR2>(o2 ? a : c));
  m = red2<MAX>(m, dpp<ROR4>(m)); m = red2<MAX>(m, dpp<ROR8>(m));
  m = MAX ? xrow_max(m) : xrow_sum(m);
  return f32x4{dpp<0x00>(m), dpp<0x55>(m), dpp<0xAA>(m), dpp<0xFF>(m)};
}
#define STAMP(i) do {} while (0)
#define STAMP_OUT() do {} while (0)
}

static __device__ __attribute__((noinline, used)) void code_begin_marker() { __builtin_amdgcn_s_sleep(0); }
static __device__ __attribute__((noinline, used)) void code_end_marker();

__global__ void __launch_bounds__(512, 2)
adaln_block_kernel(const bf16_t* __restrict__ x, const bf16_t* __restrict__ kc, const bf16_t* __restrict__ vc,
                   const bf16_t* __restrict__ wq, const bf16_t* __restrict__ wo, const bf16_t* __restrict__ wg,
                   const bf16_t* __restrict__ wu, const bf16_t* __restrict__ wd, const bf16_t* __restrict__ cc,
                   const bf16_t* __restrict__ wmod, bf16_t* __restrict__ out, unsigned char* ws) {
  __shared__ __attribute__((aligned(16))) unsigned char lds_raw[L_END];
  LAS unsigned char* L = (LAS unsigned char*)lds_raw;
  volatile LAS unsigned* FL = (volatile LAS unsigned*)(L + L_FLAG);
  LAS unsigned* FC = (LAS unsigned*)(L + L_FLAG);
  const unsigned long long tstart = __builtin_amdgcn_s_memrealtime();
  const int tid = threadIdx.x, lane = tid & 63, wv = __builtin_amdgcn_readfirstlane(tid >> 6);
  const int g = blockIdx.x, grp = (g & 7) * 4 + (g >> 6), b = grp >> 3, kvh = grp & 7, s = (g >> 3) & 7;
  const int n = lane & 15, kg = lane >> 4;
  if (wv == 3) {
    const unsigned long long c0 = (unsigned long long)(uintptr_t)&code_begin_marker, c1 = (unsigned long long)(uintptr_t)&code_end_marker;
    const int len = (int)(c1 - c0);
    const v4u rc = {(unsigned)c0, (unsigned)(c0 >> 32) & 0xFFFFu, (unsigned)(len > 0 ? len : 0), 0x00020000u};
    unsigned keep;
    asm volatile("s_mov_b32 %0, m0\n\ts_mov_b32 m0, %3\n\ts_nop 0\n\tbuffer_load_dwordx4 %1, %2, 0 offen lds\n\ts_mov_b32 m0, %0"
                 : "=&s"(keep) : "v"((g >> 3) * 1024 + lane * 16), "s"(rc), "s"((unsigned)(uintptr_t)(L + L_HB)) : "memory");
  }
  if (tid < 80) *(LAS v4u*)(L + L_ZERO + tid * 16) = v4u{0u, 0u, 0u, 0u};
  asm volatile("" :: "s"(x), "s"(kc), "s"(vc), "s"(wq), "s"(cc), "s"(wmod), "s"(ws));
  wg_barrier();
  const rsrc_t rws = mkrsrc(ws, WS_END);
  unsigned tag;
  v4u stv[2];
#define W_HEAD ((g & 7) + 8 * ((g - 128) >> 5))
#define W_CQ ((g >> 3) & 3)
#define ST_OFF(row) (WS_ST + (((row) * 8 + (W_HEAD >> 2)) * 8 + (lane >> 3)) * 128)

  if (__builtin_expect(wv >= 4, 1)) {
    const int w = wv - 4;
    const size_t kvbase = ((size_t)grp * SK + (SK - WIN) + s * 512) * 128;
    const int rot = (g * 37) & 127;
    const unsigned char* kbase = (const unsigned char*)(kc + kvbase);
    const unsigned ldsk = (unsigned)(uintptr_t)(L + L_K);
    const int klane = kg * 256 + ((n ^ kg) << 4);
    unsigned due0 = (unsigned)tstart + SCH_A;
#define PACE_WAIT(now, i) do { \
      const int late_ = (int)((now) - due0) - (((i) * SCH_T16) >> 4); \
      VMCNT(PHI); \
      if (late_ < LV2) { VMCNT(PMID); if (late_ < LV1) { VMCNT(PLO); if (late_ < LV0) VMCNT(PMIN); } } } while (0)
    STAMP(7);
#pragma unroll 4
    for (int i = 0; i < 32; ++i) {
      dma16<true>(kbase + ((4 * i + w + rot) & 127) * 1024, klane ^ ((i & 3) << 6), ldsk + (4 * i + w) * 1024);
      if (i < PLO) continue;
      const unsigned now = (unsigned)__builtin_amdgcn_s_memrealtime();
      if (i == PLO + 1 && (int)(now - SCH_F - due0) > 0) due0 = now - SCH_F;
      PACE_WAIT(now, i);
    }
    STAMP(1);
    { const unsigned anc = __builtin_amdgcn_readfirstlane(FL[F_ANC]); if (anc) due0 = anc + SCH_G; }
    const unsigned epoch = __builtin_amdgcn_raw_buffer_load_b32(rws, WS_EPOCH, 0, AUX_SC1);
    const rsrc_t rv = mkrsrc(vc + kvbase, 131072);
    v4u V[32], VT[24], VL[8];
    constexpr int TR0 = 8 + PHI;
#pragma unroll
    for (int i = 0; i < 32; ++i) {
      V[i] = bld<AUX_NT>(rv, lane * 16, ((4 * i + w + rot) & 127) * 1024);
      const unsigned now = (unsigned)__builtin_amdgcn_s_memrealtime();
      if (i >= TR0) { const int k = i - TR0; VT[k] = tcol8r(V + 8 * (k >> 3), k & 7); PIN(VT[k]); }
      if (i == PLO - 1) { VMCNT(PLO); FL[F_K + w] = 1u; STAMP(2); }
      else if (i < 31) PACE_WAIT(now, 32 + i);
      SB();
    }
    FL[F_VI + w] = 1u;
    tag = __builtin_amdgcn_readfirstlane(epoch) + 0x01010101u;
    STAMP(3);
#pragma unroll
    for (int k = 32 - TR0; k < 24; ++k) { VT[k] = tcol8r(V + 8 * (k >> 3), k & 7); PIN(VT[k]); }
    lds_wait(FL + F_PCNT, 4u);
    STAMP(4);
    f32x4 f;
    {
      const LAS f32x4* sp = (const LAS f32x4*)(L + L_F);
      const f32x4 m0 = sp[0], m1 = sp[2], m2 = sp[4], m3 = sp[6], mo = sp[2 * w];
#pragma unroll
      for (int h = 0; h < 4; ++h) f[h] = fexp(mo[h] - fmx(fmx(m0[h], m1[h]), fmx(m2[h], m3[h])));
    }
    f32x4 acc[2] = {f32x4{0.f, 0.f, 0.f, 0.f}, f32x4{0.f, 0.f, 0.f, 0.f}};
    int paddr[4];
#pragma unroll
    for (int ep = 0; ep < 4; ++ep) paddr[ep] = (n >> 2) == ep ? L_P + w * 1024 + (((n & 3) * 4 + kg) << 4) : L_ZERO;
    int mra = L_MRG + (w * 512 + 8 * n + 2 * kg) * 4;
#pragma unroll
    for (int G = 0; G < 4; ++G) {
      v4u pa[4];
#pragma unroll
      for (int ep = 0; ep < 4; ++ep) pa[ep] = *(const LAS v4u*)(L + paddr[ep] + G * 256);
      if (G == 3) {
        PIN(pa[0]); PIN(pa[1]); PIN(pa[2]); PIN(pa[3]); PIN(f); PIN(mra); PIN(acc[0]); PIN(acc[1]);
        STAMP(5);
#pragma unroll
        for (int jp = 0; jp < 4; ++jp) {
#pragma unroll
          for (int e = 0; e < 8; ++e) {
            unsigned t = __builtin_amdgcn_perm(V[25 + 2 * jp][e >> 1], V[24 + 2 * jp][e >> 1], (e & 1) ? 0x07060302u : 0x05040100u);
            if (jp < 3) PIN(t);
            VL[e][jp] = t;
          }
          if (jp < 3) SB();
        }
      }
#define PV(E) acc[E & 1] = mfma32(pa[E >> 1], G < 3 ? VT[8 * G + E] : VL[E], acc[E & 1]);
      FOR8(PV)
#undef PV
      SB();
    }
    STAMP(6);
#pragma unroll
    for (int h = 0; h < 4; ++h) *(LAS f32x2*)(L + mra + h * 512) = f32x2{acc[0][h], acc[1][h]} * f[h];
    wg_barrier();
    SB();
  } else {
    const int cw = wv;
    __builtin_amdgcn_s_setprio(2);
    const int ks = g >> 5, ns = g & 31, u5 = lane & 31, h5 = lane >> 5;
    const int mkq = g < 64 ? g >> 4 : (g - 64) >> 5, mcb = g < 64 ? g & 15 : 16 + ((g - 64) & 31);
    v2u wm[8], wr[8];
    const rsrc_t rm = mkrsrc(wmod + (size_t)(mkq * 128 + cw * 32) * 3072, g < 192 ? 32 * 6144 : 0);
    const rsrc_t rq = mkrsrc(wq + (size_t)(ks * 64 + (cw & 1) * 32) * 4096, 32 * 8192);
    const v4u cf = *(const v4u*)(cc + (n >> 2) * 512 + ((128 * mkq) & 511) + 32 * cw + 8 * kg);
#pragma unroll
    for (int j = 0; j < 8; ++j) wm[j] = __builtin_amdgcn_raw_buffer_load_b64(rm, 8 * kg * 6144 + mcb * 128 + n * 8, j * 6144, AUX_NT);
    const unsigned epoch = __builtin_amdgcn_raw_buffer_load_b32(rws, WS_EPOCH, 0, AUX_SC1);
    const v4u xs = *(const v4u*)(x + cw * 512 + lane * 8);
    const v2u xo = *(const v2u*)(x + cw * 512 + 64 * ks + 4 * n);
#pragma unroll
    for (int j = 0; j < 8; ++j) wr[j] = __builtin_amdgcn_raw_buffer_load_b64(rq, 8 * kg * 8192 + ns * 256 + (cw >> 1) * 128 + n * 8, j * 8192, AUX_NT);
    SB();
    STAMP(1);
    v4u bq[4];
    unsigned long long tfirst;
    {
      v4u a, bt[4];
#pragma unroll
      for (int i = 0; i < 4; ++i) a[i] = pack2(silu_f(bflo(cf[i])), silu_f(bfhi(cf[i])));
      PIN(a);
      tcol4x8(wm, bt);
      tfirst = __builtin_amdgcn_s_memrealtime();
      *(LAS f32x4*)(L + L_MRG + cw * 1024 + lane * 16) = gemv32x64(a, bt);
      lds_arrive(FC + F_MCNT, lane);
      tag = __builtin_amdgcn_readfirstlane(epoch) + 0x01010101u;
      if (cw == 0 && g < 192) {
        lds_wait(FL + F_MCNT, 4u);
        const LAS f32x4* mp = (const LAS f32x4*)(L + L_MRG + lane * 16);
        st16(rws, WS_MOD + ((mkq * 4 + kg) * 3072 + 64 * mcb + 4 * n) * 4, put4r((mp[0] + mp[64]) + (mp[128] + mp[192]), tag));
        if (g < 64 && lane == 0) { const float t = __uint_as_float((unsigned)__builtin_amdgcn_s_memrealtime() << 4); st16(rws, WS_MT + g * 16, put4(f32x4{t, t, t, t}, tag)); }
        STAMP(11);
      }
    }
    float ln[4];
    {
      float sum = 0.f;
#pragma unroll
      for (int i = 0; i < 4; ++i) sum += bflo(xs[i]) + bfhi(xs[i]);
      const float mean = wave_sum(sum) * (1.f / 512.f);
      float sq = 0.f;
#pragma unroll
      for (int i = 0; i < 4; ++i) { const float d0 = bflo(xs[i]) - mean, d1 = bfhi(xs[i]) - mean; sq = fmaf(d0, d0, sq); sq = fmaf(d1, d1, sq); }
      sq = wave_sum(sq);
      const float mb = rbf(mean), den = rbf(sqrtf(rbf(rbf(sq * (1.f / 512.f)) + 1e-5f)));
#pragma unroll
      for (int i = 0; i < 2; ++i) { ln[2 * i] = rbf(rbf(bflo(xo[i]) - mb) / den); ln[2 * i + 1] = rbf(rbf(bfhi(xo[i]) - mb) / den); }
    }
    tcol4x8(wr, bq);
    STAMP(2);
    wait_until(tstart, T_MOD);
    wait_until(tfirst, T_MODF);
    {
      const int mo = WS_MOD + ((h5 * 4 + cw) * 3072 + (kg & 1) * 512 + 64 * ks + 4 * n) * 4;
      const int off[2] = {mo, mo + 2 * 4 * 3072 * 4};
      f32x4 v[2];
      v4u mt;
      if (cw == 3) mt = bld<AUX_SC1>(rws, WS_MT + lane * 16, 0);
      sweep<2, 8>(rws, off, v, tag);
      STAMP(3);
      if (cw == 3) {
        const unsigned now = (unsigned)__builtin_amdgcn_s_memrealtime();
        const int d = (int)(mt.x - (now << 4)) >> 4;
        const bool ok = (tagdiff(mt, tag) & 0x0F0F0F0Fu) == 0u && d <= 0 && d > -2000;
        const float cnt = wave_sum(ok ? 1.f : 0.f), sum = wave_sum(ok ? (float)d : 0.f);
        FL[F_ANC] = (now + (cnt > 0.f ? (int)(sum / cnt) : -300)) | 1u;
      }
      const f32x4 t = v[0] + v[1];
      float h[4];
#pragma unroll
      for (int e = 0; e < 4; ++e) {
        auto p = __builtin_amdgcn_permlane32_swap(__float_as_uint(t[e]), __float_as_uint(t[e]), false, false);
        const float u = rbf(__uint_as_float(p[0]) + __uint_as_float(p[1]));
        auto r = __builtin_amdgcn_permlane16_swap(__float_as_uint(u), __float_as_uint(u), false, false);
        h[e] = rbf(ln[e] * rbf(1.f + __uint_as_float(r[1]))) + __uint_as_float(r[0]);
      }
      if (lane < 16) *(LAS v2u*)(L + L_HQ + cw * 128 + n * 8) = v2u{pack2(h[0], h[1]), pack2(h[2], h[3])};
      lds_arrive(FC + F_HCNT, lane);
      lds_wait(FL + F_HCNT, 4u);
      const v4u a = *(const LAS v4u*)(L + L_HQ + (n >> 2) * 128 + (cw & 1) * 64 + kg * 16);
      const f32x4 qp = gemv32x64(a, bq);
      if (cw & 1) {
        *(LAS f32x4*)(L + L_MRG + 4096 + (cw >> 1) * 1024 + lane * 16) = qp;
        lds_set(FL + F_G + (cw >> 1));
      } else {
        lds_wait(FL + F_G + (cw >> 1));
        st16(rws, WS_QP + ((kg * 8 + ks) * 4096 + 128 * ns + 64 * (cw >> 1) + 4 * n) * 4, put4r(qp + *(const LAS f32x4*)(L + L_MRG + 4096 + (cw >> 1) * 1024 + lane * 16), tag));
      }
    }
    STAMP(4);
    if (cw == 3 && g >= 192) {
      const int rr = g - 192;
      const int mo = WS_MOD + ((((lane >> 3) & 3) * 4 + (rr >> 4)) * 3072 + 1024 + h5 * 512 + 32 * (rr & 15) + 4 * (lane & 7)) * 4;
      const int off[2] = {mo, mo + 4096};
      f32x4 v[2];
      const unsigned short xin = x[rr * 32 + u5];
      sweep<2, 8>(rws, off, v, tag);
#pragma unroll
      for (int i = 0; i < 2; ++i)
#pragma unroll
        for (int e = 0; e < 4; ++e) {
          float u = v[i][e]; u += dpp<ROR8>(u);
          auto p = __builtin_amdgcn_permlane16_swap(__float_as_uint(u), __float_as_uint(u), false, false);
          v[i][e] = rbf(__uint_as_float(p[0]) + __uint_as_float(p[1]));
        }
      *(LAS float*)(L + L_GG + 256 + u5 * 4) = bflo(xin);
      if (u5 < 8) {
        if (lane < 32) {
          *(LAS f32x4*)(L + L_GG + lane * 16) = v[0];
          st16(rws, WS_CB + rr * 128 + lane * 16, put4(f32x4{rbf(1.f + v[1][0]), rbf(1.f + v[1][1]), rbf(1.f + v[1][2]), rbf(1.f + v[1][3])}, tag));
        } else {
          *(LAS f32x4*)(L + L_GG + 128 + u5 * 16) = v[1];
          st16(rws, WS_SB + rr * 128 + u5 * 16, put4(v[0], tag));
        }
      }
    }
    const int qo = WS_QP + ((b * 8 + h5) * 4096 + kvh * 512 + cw * 128 + 4 * u5) * 4;
    {
      unsigned tq = __builtin_amdgcn_readfirstlane(FL[F_ANC]) + T_Q;
      { const int late = (int)(tq - (unsigned)tstart) - T_Q - QSL; if (late > 0) tq += late; }
#pragma unroll 1
      for (int i = 0; i < 4096 && (int)((unsigned)__builtin_amdgcn_s_memrealtime() - tq) < 0; ++i) __builtin_amdgcn_s_sleep(1);
    }
    lds_wait(FL + F_K + cw);
    v4u qv[4];
#pragma unroll
    for (int i = 0; i < 4; ++i) qv[i] = bld<AUX_SC1>(rws, qo, i * 32768);
    SB();
    STAMP(5);
    v4u kf[32];
#pragma unroll
    for (int t = 0; t < 8; ++t)
#pragma unroll
      for (int ds = 0; ds < 4; ++ds)
        kf[4 * t + ds] = *(const LAS v4u*)(L + L_K + (16 * (4 * t + (n >> 2)) + 4 * cw + (n & 3)) * 256 + (((4 * ds + kg) ^ n) << 4));
    SB();
    {
#pragma unroll 1
      for (unsigned spin = 0; spin < SPIN_G; ++spin) {
        if (all_ok(tagdiff(qv[0], tag) | tagdiff(qv[1], tag) | tagdiff(qv[2], tag) | tagdiff(qv[3], tag))) break;
        __builtin_amdgcn_s_sleep(8);
#pragma unroll
        for (int i = 0; i < 4; ++i) if (!all_ok(tagdiff(qv[i], tag))) qv[i] = bld<AUX_SC1>(rws, qo, i * 32768);
      }
      const f32x4 t = (get4(qv[0]) + get4(qv[1])) + (get4(qv[2]) + get4(qv[3]));
      float q[4];
#pragma unroll
      for (int e = 0; e < 4; ++e) {
        auto p = __builtin_amdgcn_permlane32_swap(__float_as_uint(t[e]), __float_as_uint(t[e]), false, false);
        q[e] = __uint_as_float(p[0]) + __uint_as_float(p[1]);
      }
      if (lane < 32) *(LAS v2u*)(L + L_QB + cw * 320 + u5 * 8) = v2u{pack2(q[0], q[1]), pack2(q[2], q[3])};
      lds_arrive(FC + F_QCNT, lane);
      lds_wait(FL + F_QCNT, 4u);
    }
    STAMP(6);
    {
      f32x4 sc[2];
      v4u qf[4];
#pragma unroll
      for (int ds = 0; ds < 4; ++ds) qf[ds] = *(const LAS v4u*)(L + L_QB + (n & 3) * 320 + kg * 16 + ds * 64);
      f32x4 c[8];
#pragma unroll
      for (int t = 0; t < 8; ++t) {
        c[t] = f32x4{0.f, 0.f, 0.f, 0.f};
#pragma unroll
        for (int ds = 0; ds < 4; ++ds) c[t] = mfma32(qf[ds], kf[4 * t + ds], c[t]);
      }
#pragma unroll
      for (int sg = 0; sg < 2; ++sg)
#pragma unroll
        for (int h = 0; h < 4; ++h) sc[sg][h] = sel4(lane & 16, lane & 32, c[4 * sg][h], c[4 * sg + 1][h], c[4 * sg + 2][h], c[4 * sg + 3][h]);
      f32x4 e0, e1, mx;
#pragma unroll
      for (int h = 0; h < 4; ++h) {
        const f32x2 r = rbf2(rbf2(f32x2{sc[0][h], sc[1][h]}) * 0.08838834764831845f);
        e0[h] = r.x; e1[h] = r.y; mx[h] = fmx(r.x, r.y);
      }
      const f32x4 mw = wave_red4<true>(mx, lane);
#pragma unroll
      for (int h = 0; h < 4; ++h) {
        e0[h] = fexp(e0[h] - mw[h]); e1[h] = fexp(e1[h] - mw[h]);
        const unsigned pp = pack2(e0[h], e1[h]);
        LAS unsigned short* pd = (LAS unsigned short*)(L + L_P) + ((((cw * 4 + (kg >> 1)) * 4 + h) * 4 + (n & 3)) * 8 + 4 * (kg & 1) + (n >> 2));
        pd[0] = (unsigned short)(pp & 0xFFFFu);
        pd[2 * 128] = (unsigned short)(pp >> 16);
      }
      const f32x4 lw = wave_red4<false>(e0 + e1, lane);
      if (lane < 2) *(LAS f32x4*)(L + L_F + cw * 32 + lane * 16) = lane == 0 ? mw : lw;
      lds_arrive(FC + F_PCNT, lane);
    }
    STAMP(7);
    lds_wait(FL + F_PCNT, 4u);
    if (cw == 3) {
      const LAS f32x4* sp = (const LAS f32x4*)(L + L_F);
      f32x4 M, Ls;
#pragma unroll
      for (int h = 0; h < 4; ++h) {
        M[h] = fmx(fmx(sp[0][h], sp[2][h]), fmx(sp[4][h], sp[6][h]));
        Ls[h] = (sp[1][h] * fexp(sp[0][h] - M[h]) + sp[3][h] * fexp(sp[2][h] - M[h])) + (sp[5][h] * fexp(sp[4][h] - M[h]) + sp[7][h] * fexp(sp[6][h] - M[h]));
      }
      if (lane < 2) st16(rws, WS_ST + (grp * 8 + s) * 128 + lane * 16, put4(lane == 0 ? M : Ls, tag));
    }
    STAMP(8);
    if (g >= 128) {
      const unsigned char* wsrc = (const unsigned char*)(wo + (size_t)(W_HEAD * 128 + cw * 32) * 512 + W_CQ * 128);
      const int vo = kg * 1024 + ((n ^ (kg << 1)) << 4), sto = ST_OFF(cw);
      lds_wait(FL + F_VI + cw);
      __builtin_amdgcn_s_sleep(WO_DLY);
      stv[0] = bld<AUX_SC1>(rws, sto, 0); stv[1] = bld<AUX_SC1>(rws, sto + 16, 0);
#pragma unroll
      for (int j = 0; j < 8; ++j) dma16<true>(wsrc + j * 4096, vo ^ ((j & 2) << 6), (unsigned)(uintptr_t)(L + L_WO) + cw * 8192 + j * 1024);
      SB();
    }
    int h1o = WS_H1 + (grp * 8 + s) * 2048 + tid * 16;
    PIN(h1o);
    STAMP(9);
    wg_barrier();
    if (cw < 2) {
      const LAS f32x4* mp = (const LAS f32x4*)(L + L_MRG + tid * 16);
      st16(rws, h1o, put4((mp[0] + mp[128]) + (mp[256] + mp[384]), tag));
    }
    SB();
  }
  STAMP(10);
  if (g >= 128) {
    const unsigned long long tp = __builtin_amdgcn_s_memrealtime();
    const int whead = W_HEAD, wcq = W_CQ;
    int csa = L_CS + (wv >> 1) * 32 + (lane >> 3) * 4;
    int h2o = WS_H2 + ((kg * 16 + wcq * 4 + wv) * 32 + whead) * 128 + n * 8;
    int oar = L_OA + (n >> 2) * 320 + kg * 16, oaw = L_OA + (wv >> 1) * 320 + (wv & 1) * 128 + n * 8 + kg * 2;
    int wtr = L_WO + (8 * kg + (n >> 2)) * 256 + (((4 * wv + ((n >> 1) & 1)) ^ (((n >> 2) | ((kg & 1) << 2)) << 1)) << 4) + (n & 1) * 8, wtr1 = wtr ^ 32;
    unsigned tg2 = tag >> ((n & 1) * 16);
    PIN(h2o); PIN(oar); PIN(oaw); PIN(csa); PIN(tg2); PIN(wtr); PIN(wtr1);
    SB();
    if (wv < 4) {
      if (!all_ok(tagdiff(stv[0], tag) | tagdiff(stv[1], tag))) {
        const int so[2] = {ST_OFF(wv), ST_OFF(wv) + 16};
        sweep_raw<2, 0>(rws, so, stv, tag);
      }
      *(LAS float*)(L + L_CS + wv * 32 + (lane >> 3) * 4) = split_weight(get4(stv[0]), get4(stv[1]), whead);
      CBAR();
      FL[F_CS + wv] = 1u;
    }
    {
      const int ho = WS_H1 + (((wv >> 1) * 8 + (whead >> 2)) * 8 + (lane >> 3)) * 2048 + (whead & 3) * 512 + (wv & 1) * 256 + (lane & 7) * 16;
      const int off[2] = {ho, ho + 128};
      f32x4 v[2];
      wait_until(tp, H1DLY);
      sweep<2, 2>(rws, off, v, tag);
      float cs;
#pragma unroll 1
      for (unsigned i = 0; i < SPIN_L; ++i) {
        const unsigned fl = FL[F_CS + (wv >> 1)];
        cs = *(const volatile LAS float*)(L + csa);
        if (fl) break;
      }
      const float o0 = part_sum(v[0] * cs), o1 = part_sum(v[1] * cs);
      *(LAS unsigned short*)(L + oaw) = (unsigned short)pack2((lane & 8) ? o1 : o0, 0.f);
    }
    VMCNT(0);
    wg_barrier();
    STAMP(11);
    if (wv < 4) {
      v4u a[4]; v2u bl[8], bh[8];
#pragma unroll
      for (int k = 0; k < 4; ++k) {
        a[k] = *(const LAS v4u*)(L + oar + k * 64);
        bl[2 * k] = lds_tr(L + wtr + k * 8192); bh[2 * k] = lds_tr(L + wtr + k * 8192 + 1024);
        bl[2 * k + 1] = lds_tr(L + wtr1 + k * 8192); bh[2 * k + 1] = lds_tr(L + wtr1 + k * 8192 + 1024);
      }
      SB();
      f32x4 c0 = {0.f, 0.f, 0.f, 0.f}, c1 = {0.f, 0.f, 0.f, 0.f};
#pragma unroll
      for (int k = 0; k < 4; ++k) {
        c0 = mfma32(a[k], v4u{bl[2 * k].x, bl[2 * k].y, bh[2 * k].x, bh[2 * k].y}, c0);
        c1 = mfma32(a[k], v4u{bl[2 * k + 1].x, bl[2 * k + 1].y, bh[2 * k + 1].x, bh[2 * k + 1].y}, c1);
      }
      __builtin_amdgcn_raw_buffer_store_b64(v2u{put1(c0[0], tg2), put1(c1[0], tg2 >> 8)}, rws, h2o, 0, AUX_SC1);
    }
    SB();
    STAMP(12);
    if (g >= 192) {
      const int rr = g - 192, col = 16 * ((lane >> 2) & 1) + 4 * kg + (lane & 3);
      float x1 = 0.f, gb = 0.f;
      int oo = (rr * 32 + col) * 2, rda = L_RED + col * 4;
      PIN(oo); PIN(rda);
      if (wv == 0) {
        const int pc = 2 * (lane & 7) + (kg >> 1) + 16 * (kg & 1);
        float ga = *(const LAS float*)(L + L_GG + pc * 4), xin = *(const LAS float*)(L + L_GG + 256 + pc * 4);
        gb = *(const LAS float*)(L + L_GG + 128 + col * 4);
        int x1o = WS_X1 + (n >> 3) * 8192 + rr * 128 + pc * 4;
        unsigned tg1 = tag >> ((pc & 3) * 8);
        PIN(ga); PIN(xin); PIN(x1o); PIN(gb); PIN(tg1);
        const int ho = WS_H2 + rr * 4096 + lane * 16;
        const int off[4] = {ho, ho + 1024, ho + 2048, ho + 3072};
        f32x4 v[4];
        __builtin_amdgcn_s_sleep(H2DLY);
        sweep<4, 0>(rws, off, v, tag);
        STAMP(13);
        x1 = rbf(xin + rbf(ga * rbf(part_sum((v[0] + v[1]) + (v[2] + v[3])))));
        __builtin_amdgcn_raw_buffer_store_b32(put1(x1, tg1), rws, x1o, 0, AUX_SC1);
        SB();
        *(LAS float*)(L + L_GG + 384 + pc * 4) = x1;
        LGKM0();
        x1 = *(const LAS float*)(L + L_GG + 384 + col * 4);
        PIN(x1);
      }
      STAMP(14);
      if (wv == 7) {
        const int off[1] = {WS_X1 + (g & 1) * 8192 + lane * 128};
        v4u v[1];
        sweep_raw<1, 0>(rws, off, v, tag);
        STAMP(13);
        __builtin_amdgcn_s_sleep(DLY_H4);
        FL[F_H4] = 1u;
      } else {
        const int off[2] = {WS_H4 + rr * 14336 + wv * 2048 + lane * 16, WS_H4 + rr * 14336 + wv * 2048 + 1024 + lane * 16};
        f32x4 v[2];
        lds_wait(FL + F_H4);
        sweep<2, 0>(rws, off, v, tag);
        *(LAS float*)(L + rda + wv * 128) = part_sum(v[0] + v[1]);
      }
      wg_barrier();
      if (wv == 0) {
        const LAS float* rp = (const LAS float*)(L + rda);
        const float a = ((rp[0] + rp[32]) + (rp[64] + rp[96])) + ((rp[128] + rp[160]) + rp[192]);
        const unsigned ob = pack2(x1 + rbf(gb * rbf(a)), 0.f);
        if (n < 8) __builtin_amdgcn_raw_buffer_store_b16((unsigned short)ob, mkrsrc(out, 4096), oo, 0, AUX_SC1);
        if (g == 192 && lane == 0) __builtin_amdgcn_raw_buffer_store_b32(tag, rws, WS_EPOCH, 0, AUX_SC1);
      }
      STAMP(15);
    }
  } else if (((g >> 5) << 3) + (g & 7) < 28) {
    const int jsl = 4 * (((g >> 5) << 3) + (g & 7)) + ((g >> 3) & 3);
    __builtin_amdgcn_s_barrier();
    const int sco[2] = {WS_SB + wv * 1024 + lane * 16, WS_CB + wv * 1024 + lane * 16};
    v4u scr[2];
    scr[0] = bld<AUX_SC1>(rws, sco[0], 0); scr[1] = bld<AUX_SC1>(rws, sco[1], 0);
    SB();
    {
      const unsigned char* gsrc = (const unsigned char*)(((lane & 2) ? wu : wg) + (size_t)(32 * ((lane >> 2) & 1) + 8 * (lane >> 5) + ((lane >> 3) & 3)) * FF + 16 * jsl + 8 * ((lane ^ (lane >> 4)) & 1));
#pragma unroll
      for (int j = 0; j < 4; ++j) {
        const int k = (8 * j + wv + 8 * ((g >> 3) & 3)) & 31;
        dma16v(gsrc + (64 * (k >> 2) + 4 * ((k >> 1) & 1) + 16 * (k & 1)) * (FF * 2), (unsigned)(uintptr_t)(L + L_FB) + k * 1024);
      }
#pragma unroll
      for (int j = 0; j < 2; ++j)
        dma16<true>(wd + (size_t)(16 * jsl + 2 * wv + j) * 512, (lane ^ (j | ((wv & 1) << 3))) << 4, (unsigned)(uintptr_t)(L + L_DN) + (2 * wv + j) * 1024);
    }
    v4u bg[8], bd[2], a[8];
    const int rb = wv >> 1, hf = wv & 1;
    int hbw = L_HB + rb * 1024 + (rb >> 1) * 32 + hf * 512 + lane * 8;
    int hbr = L_HB + (n >> 2) * 1024 + (n >> 3) * 32 + (32 * (n & 1) + 8 * kg) * 2;
    int acr = ((n & 1) == (kg >> 1)) ? L_ACT + (n >> 2) * 32 + (kg & 1) * 16 : L_ZERO, acw = L_ACT + kg * 32 + wv * 8 + (n & 3) * 2;
    int h4o = WS_H4 + ((kg * 16 + 2 * wv + (n >> 3)) * 112 + jsl) * 128 + (n & 7) * 16;
    int dtr = L_DN + (8 * (kg & 1) + (n >> 2)) * 1024 + (((8 * wv + 2 * (n & 3)) ^ (((n >> 2) & 1) | (n & 8))) << 4) + (kg >> 1) * 8;
    PIN(hbw); PIN(hbr); PIN(acr); PIN(acw); PIN(h4o); PIN(dtr);
    VMCNT(2);
    wg_barrier();
    STAMP(12);
    if (wv < 4) {
      LAS unsigned char* gt = L + L_FB + (kg >> 1) * 1024 + (lane & 31) * 32 + (((wv >> 1) ^ (n >> 3)) << 4) + (wv & 1) * 8;
#pragma unroll
      for (int t = 0; t < 8; ++t) {
        const v2u lo = lds_tr(gt + t * 4096), hi = lds_tr(gt + t * 4096 + 2048);
        bg[t] = v4u{lo.x, lo.y, hi.x, hi.y};
      }
    }
    if (!all_ok(tagdiff(scr[0], tag) | tagdiff(scr[1], tag))) sweep_raw<2, 0>(rws, sco, scr, tag);
    f32x4 shv = get4(scr[0]), scv = get4(scr[1]);
    PIN(shv); PIN(scv);
    SB();
    {
      const int xr = WS_X1 + (g & 1) * 8192 + rb * 2048 + lane * 16;
      const int off[2] = {xr + hf * 1024, xr + (hf ^ 1) * 1024};
      v4u r[2];
      sweep_raw<2, 0>(rws, off, r, tag);
      STAMP(13);
      const unsigned p0 = __builtin_amdgcn_perm(r[0].y, r[0].x, 0x07060302u), p1 = __builtin_amdgcn_perm(r[0].w, r[0].z, 0x07060302u);
      const unsigned p2 = __builtin_amdgcn_perm(r[1].y, r[1].x, 0x07060302u), p3 = __builtin_amdgcn_perm(r[1].w, r[1].z, 0x07060302u);
      const float s1 = dot2(p1, 0x3F803F80u, dot2(p0, 0x3F803F80u, 0.f)) + dot2(p3, 0x3F803F80u, dot2(p2, 0x3F803F80u, 0.f));
      const float s2 = dot2(p1, p1, dot2(p0, p0, 0.f)) + dot2(p3, p3, dot2(p2, p2, 0.f));
      auto sw = __builtin_amdgcn_permlane32_swap(__float_as_uint(s1), __float_as_uint(s2), false, false);
      float t = __uint_as_float(sw[0]) + __uint_as_float(sw[1]);
      t += dpp<XOR1>(t); t += dpp<XOR2>(t); t += dpp<ROR4>(t); t += dpp<ROR8>(t);
      const float mean = (__uint_as_float(__builtin_amdgcn_readlane(__float_as_uint(t), 0)) + __uint_as_float(__builtin_amdgcn_readlane(__float_as_uint(t), 16))) * (1.f / 512.f);
      const float msq = (__uint_as_float(__builtin_amdgcn_readlane(__float_as_uint(t), 32)) + __uint_as_float(__builtin_amdgcn_readlane(__float_as_uint(t), 48))) * (1.f / 512.f);
      const float mb = rbf(mean), rden = __builtin_amdgcn_rcpf(rbf(__builtin_amdgcn_sqrtf(rbf(rbf(fmx(fmaf(-mean, mean, msq), 0.f)) + 1e-5f))));
      const f32x2 da = rbf2(f32x2{bflo(p0), bfhi(p0)} - mb), db = rbf2(f32x2{bflo(p1), bfhi(p1)} - mb);
      const f32x2 na = rbf2(da * rden), nb = rbf2(db * rden);
      const f32x2 ha = rbf2(na * f32x2{scv[0], scv[1]}) + f32x2{shv[0], shv[1]}, hb = rbf2(nb * f32x2{scv[2], scv[3]}) + f32x2{shv[2], shv[3]};
      *(LAS v2u*)(L + hbw) = v2u{pack2(ha.x, ha.y), pack2(hb.x, hb.y)};
    }
    VMCNT(0);
    wg_barrier();
    if (wv < 4) {
#pragma unroll
      for (int t = 0; t < 8; ++t) a[t] = *(const LAS v4u*)(L + hbr + t * 128);
    }
#pragma unroll
    for (int m2 = 0; m2 < 2; ++m2) {
      const v2u lo = lds_tr(L + (dtr ^ (m2 << 4))), hi = lds_tr(L + (dtr ^ (m2 << 4)) + 4096);
      bd[m2] = v4u{lo.x, lo.y, hi.x, hi.y};
    }
    SB();
    if (wv < 4) {
      f32x4 c = {0.f, 0.f, 0.f, 0.f};
#pragma unroll
      for (int t = 0; t < 8; ++t) c = mfma32(a[t], bg[t], c);
      float gs = (lane & 8) ? c[1] : c[0];
      gs += dpp<ROR8>(gs);
      gs = rbf(gs);
      const float act = rbf(gelu_f(gs)) * dpp<ROR4>(gs);
      if (n < 4) *(LAS unsigned short*)(L + acw) = (unsigned short)pack2(act, 0.f);
    }
    wg_barrier();
    STAMP(14);
    {
      const v4u aa = *(const LAS v4u*)(L + acr);
      const f32x4 c0 = mfma32(aa, bd[0], f32x4{0.f, 0.f, 0.f, 0.f}), c1 = mfma32(aa, bd[1], f32x4{0.f, 0.f, 0.f, 0.f});
      st16(rws, h4o, put4(f32x4{c0[0], c0[1], c1[0], c1[1]}, tag));
    }
    STAMP(15);
  }
  STAMP_OUT();
}

static __device__ __attribute__((noinline, used)) void code_end_marker() { __builtin_amdgcn_s_sleep(1); }

extern "C" void kernel_launch(void* const* d_in, const int* in_sizes, int n_in, void* d_out, int out_size, void* d_ws, size_t ws_size, hipStream_t stream) {
  adaln_block_kernel<<<256, 512, 0, stream>>>((const bf16_t*)d_in[0], (const bf16_t*)d_in[1], (const bf16_t*)d_in[2], (const bf16_t*)d_in[3],
                                              (const bf16_t*)d_in[4], (const bf16_t*)d_in[6], (const bf16_t*)d_in[7], (const bf16_t*)d_in[8],
                                              (const bf16_t*)d_in[9], (const bf16_t*)d_in[10], (bf16_t*)d_out, (unsigned char*)d_ws);
}
```

```cpp
#include <hip/hip_runtime.h>
#include <hip/hip_bf16.h>
#include <stdint.h>

#define LAS __attribute__((address_space(3)))
typedef unsigned short bf16_t;
typedef unsigned v4u __attribute__((ext_vector_type(4)));
typedef unsigned v2u __attribute__((ext_vector_type(2)));
typedef float f32x4 __attribute__((ext_vector_type(4)));
typedef float f32x2 __attribute__((ext_vector_type(2)));
typedef __bf16 bf16x8 __attribute__((ext_vector_type(8)));
typedef __bf16 bf16x2 __attribute__((ext_vector_type(2)));
typedef short s16x4 __attribute__((ext_vector_type(4)));
typedef __amdgpu_buffer_rsrc_t rsrc_t;

#define PMIN 3
#define PLO 4
#define PMID 6
#define PHI 10
#define LV0 (-40)
#define LV1 0
#define LV2 30
#define SCH_T16 247
#define SCH_A 110
#define SCH_F 60
#define SCH_G (-100)
#define T_MOD 380
#define T_MODF 190
#define T_Q 440
#define QSL 260
#define WO_DLY 7
#define H1DLY 25
#define H2DLY 14
#define DLY_H4 34

namespace {
constexpr int SK = 16384, WIN = 4096, FF = 1792;
constexpr int AUX_SC1 = 16, AUX_NT = 2;
constexpr int WS_EPOCH = 0;
constexpr int WS_MT = 2048;
constexpr int WS_X1 = 4096;
constexpr int WS_SB = WS_X1 + 16384;
constexpr int WS_CB = WS_SB + 8192;
constexpr int WS_ST = WS_CB + 8192;
constexpr int WS_MOD = WS_ST + 32768;
constexpr int WS_QP = WS_MOD + 196608;
constexpr int WS_H1 = WS_QP + 524288;
constexpr int WS_H2 = WS_H1 + 524288;
constexpr int WS_H4 = WS_H2 + 262144;
constexpr int WS_END = WS_H4 + 917504;
constexpr int L_K = 0;
constexpr int L_FB = 0;
constexpr int L_WO = 0;
constexpr int L_DN = 32768;
constexpr int L_P = 131072;
constexpr int L_QB = L_P + 4096;
constexpr int L_HQ = L_QB + 1280;
constexpr int L_ZERO = L_HQ + 1024;
constexpr int L_FLAG = L_ZERO + 1024;
constexpr int L_GG = L_FLAG + 256;
constexpr int L_OA = L_GG + 512;
constexpr int L_CS = L_OA + 1280;
constexpr int L_F = L_CS + 128;
constexpr int L_ACT = L_F + 128;
constexpr int L_RED = L_ACT + 128;
constexpr int L_MRG = L_RED + 2048;
constexpr int L_HB = L_MRG + 8192;
constexpr int L_END = L_HB + 4096 + 64;
enum { F_MCNT = 0, F_HCNT = 1, F_QCNT = 2, F_K = 4, F_PCNT = 8, F_G = 10, F_H4 = 12, F_VI = 16, F_CS = 24, F_ANC = 28 };
constexpr unsigned SPIN_G = 1u << 16, SPIN_L = 1u << 20;

__device__ __forceinline__ float bflo(unsigned u) { return __uint_as_float(u << 16); }
__device__ __forceinline__ float bfhi(unsigned u) { return __uint_as_float(u & 0xFFFF0000u); }
__device__ __forceinline__ unsigned pack2(float lo, float hi) {
  f32x2 v = {lo, hi};
  return __builtin_bit_cast(unsigned, __builtin_convertvector(v, bf16x2));
}
__device__ __forceinline__ float dot2(unsigned a, unsigned b, float c) {
  return __builtin_amdgcn_fdot2_f32_bf16(__builtin_bit_cast(bf16x2, a), __builtin_bit_cast(bf16x2, b), c, false);
}
__device__ __forceinline__ float rbf(float x) { return bflo(pack2(x, x) & 0xFFFFu); }
__device__ __forceinline__ float fmx(float a, float b) { return __builtin_amdgcn_fmed3f(a, b, __builtin_inff()); }
template <int CTRL> __device__ __forceinline__ float dpp(float x) {
  return __builtin_bit_cast(float, __builtin_amdgcn_mov_dpp(__builtin_bit_cast(int, x), CTRL, 0xf, 0xf, true));
}
constexpr int XOR1 = 0xB1, XOR2 = 0x4E, ROR4 = 0x124, ROR8 = 0x128;
__device__ __forceinline__ float xrow_sum(float x) {
  auto s = __builtin_amdgcn_permlane16_swap(__float_as_uint(x), __float_as_uint(x), false, false);
  x = __uint_as_float(s[0]) + __uint_as_float(s[1]);
  auto t = __builtin_amdgcn_permlane32_swap(__float_as_uint(x), __float_as_uint(x), false, false);
  return __uint_as_float(t[0]) + __uint_as_float(t[1]);
}
__device__ __forceinline__ float xrow_max(float x) {
  auto s = __builtin_amdgcn_permlane16_swap(__float_as_uint(x), __float_as_uint(x), false, false);
  x = fmx(__uint_as_float(s[0]), __uint_as_float(s[1]));
  auto t = __builtin_amdgcn_permlane32_swap(__float_as_uint(x), __float_as_uint(x), false, false);
  return fmx(__uint_as_float(t[0]), __uint_as_float(t[1]));
}
__device__ __forceinline__ float wave_sum(float x) {
  x += dpp<XOR1>(x); x += dpp<XOR2>(x); x += dpp<ROR4>(x); x += dpp<ROR8>(x);
  return xrow_sum(x);
}
__device__ __forceinline__ f32x4 mfma32(v4u a, v4u b, f32x4 c) {
  return __builtin_amdgcn_mfma_f32_16x16x32_bf16(__builtin_bit_cast(bf16x8, a), __builtin_bit_cast(bf16x8, b), c, 0, 0, 0);
}
__device__ __forceinline__ v4u tcol8r(const v4u* r, int e) {
  const unsigned sel = (e & 1) ? 0x07060302u : 0x05040100u;
  v4u o;
  o.x = __builtin_amdgcn_perm(r[1][e >> 1], r[0][e >> 1], sel);
  o.y = __builtin_amdgcn_perm(r[3][e >> 1], r[2][e >> 1], sel);
  o.z = __builtin_amdgcn_perm(r[5][e >> 1], r[4][e >> 1], sel);
  o.w = __builtin_amdgcn_perm(r[7][e >> 1], r[6][e >> 1], sel);
  return o;
}
#define FOR8(X) X(0) X(1) X(2) X(3) X(4) X(5) X(6) X(7)
__device__ __forceinline__ v2u lds_tr(LAS unsigned char* p) { return __builtin_bit_cast(v2u, __builtin_amdgcn_ds_read_tr16_b64_v4i16((LAS s16x4*)p)); }
__device__ __forceinline__ rsrc_t mkrsrc(const void* p, int bytes) { return __builtin_amdgcn_make_buffer_rsrc((void*)p, 0, bytes, 0x00020000); }
template <int AUX> __device__ __forceinline__ v4u bld(rsrc_t r, int voff, int soff) { return __builtin_amdgcn_raw_buffer_load_b128(r, voff, soff, AUX); }
template <bool NT> __device__ __forceinline__ void dma16(const void* base, int voff, unsigned dst) {
  unsigned keep;
  if (NT) asm volatile("s_mov_b32 %0, m0\n\ts_mov_b32 m0, %3\n\ts_nop 0\n\tglobal_load_lds_dwordx4 %1, %2 nt\n\ts_mov_b32 m0, %0" : "=&s"(keep) : "v"(voff), "s"(base), "s"(dst) : "memory");
  else asm volatile("s_mov_b32 %0, m0\n\ts_mov_b32 m0, %3\n\ts_nop 0\n\tglobal_load_lds_dwordx4 %1, %2\n\ts_mov_b32 m0, %0" : "=&s"(keep) : "v"(voff), "s"(base), "s"(dst) : "memory");
}
__device__ __forceinline__ void dma16v(const void* lane_ptr, unsigned dst) {
  unsigned keep;
  asm volatile("s_mov_b32 %0, m0\n\ts_mov_b32 m0, %2\n\ts_nop 0\n\tglobal_load_lds_dwordx4 %1, off\n\ts_mov_b32 m0, %0" : "=&s"(keep) : "v"(lane_ptr), "s"(dst) : "memory");
}
#define VMCNT(n) asm volatile("s_waitcnt vmcnt(%0)" :: "i"(n) : "memory")
__device__ __forceinline__ unsigned put1(float v, unsigned t) { return (__float_as_uint(v) & ~15u) | (t & 15u); }
__device__ __forceinline__ v4u put4(f32x4 v, unsigned tag) { return v4u{put1(v.x, tag), put1(v.y, tag >> 8), put1(v.z, tag >> 16), put1(v.w, tag >> 24)}; }
__device__ __forceinline__ v4u put4r(f32x4 v, unsigned tag) {
  return put4(__builtin_bit_cast(f32x4, __builtin_bit_cast(v4u, v) + 8u), tag);
}
__device__ __forceinline__ unsigned tagdiff(v4u v, unsigned tag) {
  return (__builtin_amdgcn_perm(v.y, v.x, 0x0c0c0400u) | __builtin_amdgcn_perm(v.w, v.z, 0x04000c0cu)) ^ tag;
}
__device__ __forceinline__ bool all_ok(unsigned diff) { return __all((diff & 0x0F0F0F0Fu) == 0u); }
__device__ __forceinline__ f32x4 get4(v4u v) { return __builtin_bit_cast(f32x4, v & ~15u); }
__device__ __forceinline__ void st16(rsrc_t r, int off, v4u v) { __builtin_amdgcn_raw_buffer_store_b128(v, r, off, 0, AUX_SC1); }
#define CBAR() asm volatile("" ::: "memory")
#define LGKM0() asm volatile("s_waitcnt lgkmcnt(0)" ::: "memory")
#define SB() __builtin_amdgcn_sched_barrier(0)
#define PIN(x) asm volatile("" : "+v"(x))
__device__ __forceinline__ void lds_wait(volatile LAS unsigned* f, unsigned want = 1u) {
#pragma unroll 1
  for (unsigned i = 0; i < SPIN_L; ++i) { if (*f >= want) break; __builtin_amdgcn_s_sleep(1); }
  CBAR();
}
__device__ __forceinline__ void lds_set(volatile LAS unsigned* f) { LGKM0(); *f = 1u; }
__device__ __forceinline__ void lds_arrive(LAS unsigned* f, int lane) {
  LGKM0();
  if (lane == 0) __hip_atomic_fetch_add(f, 1u, __ATOMIC_RELAXED, __HIP_MEMORY_SCOPE_WORKGROUP);
}
__device__ __forceinline__ void wg_barrier() { LGKM0(); __builtin_amdgcn_s_barrier(); CBAR(); }
template <int N, int SLEEP> __device__ __forceinline__ void sweep_raw(rsrc_t rs, const int (&off)[N], v4u (&v)[N], unsigned tag) {
  unsigned spin = 0;
  unsigned long long more;
#pragma unroll 1
  do {
    if (SLEEP && spin) __builtin_amdgcn_s_sleep(SLEEP);
    CBAR();
#pragma unroll
    for (int i = 0; i < N; ++i) v[i] = bld<AUX_SC1>(rs, off[i], 0);
    ++spin;
    unsigned mask = 0x0F0F0F0Fu & (unsigned)((int)(spin - SPIN_G) >> 31);
    asm volatile("" : "+s"(mask));
    unsigned bad = 0;
#pragma unroll
    for (int i = 0; i < N; ++i) bad |= tagdiff(v[i], tag);
    more = __builtin_amdgcn_ballot_w64((bad & mask) != 0u);
  } while (more);
}
template <int N, int SLEEP> __device__ __forceinline__ void sweep(rsrc_t rs, const int (&off)[N], f32x4 (&o)[N], unsigned tag) {
  v4u v[N];
  sweep_raw<N, SLEEP>(rs, off, v, tag);
#pragma unroll
  for (int i = 0; i < N; ++i) o[i] = get4(v[i]);
}
__device__ __forceinline__ float fexp(float x) { return __builtin_amdgcn_exp2f(x * 1.4426950408889634f); }
__device__ __forceinline__ float silu_f(float x) { return x * __builtin_amdgcn_rcpf(1.f + fexp(-x)); }
__device__ __forceinline__ float gelu_f(float x) {
  const float t = x * fmaf(x * x, -0.10294324f, -2.3022082f);
  return x * __builtin_amdgcn_rcpf(1.f + __builtin_amdgcn_exp2f(t));
}
__device__ __forceinline__ void tcol4x8(const v2u* r, v4u* o) {
#pragma unroll
  for (int e = 0; e < 4; ++e) {
    const unsigned sel = (e & 1) ? 0x07060302u : 0x05040100u;
#pragma unroll
    for (int jp = 0; jp < 4; ++jp) o[e][jp] = __builtin_amdgcn_perm(r[2 * jp + 1][e >> 1], r[2 * jp][e >> 1], sel);
    PIN(o[e]);
  }
}
__device__ __forceinline__ f32x4 gemv32x64(v4u a, const v4u* bt) {
  f32x4 c[4];
#pragma unroll
  for (int e = 0; e < 4; ++e) c[e] = mfma32(a, bt[e], f32x4{0.f, 0.f, 0.f, 0.f});
  return f32x4{c[0][0], c[1][0], c[2][0], c[3][0]};
}
__device__ __forceinline__ void wait_until(unsigned long long t0, unsigned ticks) {
#pragma unroll 1
  for (int i = 0; i < 4096 && __builtin_amdgcn_s_memrealtime() - t0 < ticks; ++i) __builtin_amdgcn_s_sleep(1);
}
__device__ __forceinline__ float sel4(bool b0, bool b1, float c0, float c1, float c2, float c3) {
  const float lo = b0 ? c1 : c0, hi = b0 ? c3 : c2;
  return b1 ? hi : lo;
}
__device__ __forceinline__ float part_sum(f32x4 v) {
  auto p = __builtin_amdgcn_permlane32_swap(__float_as_uint(v[0]), __float_as_uint(v[2]), false, false);
  auto q = __builtin_amdgcn_permlane32_swap(__float_as_uint(v[1]), __float_as_uint(v[3]), false, false);
  const float A = __uint_as_float(p[0]) + __uint_as_float(p[1]), B = __uint_as_float(q[0]) + __uint_as_float(q[1]);
  auto r = __builtin_amdgcn_permlane16_swap(__float_as_uint(A), __float_as_uint(B), false, false);
  const float t = __uint_as_float(r[0]) + __uint_as_float(r[1]);
  return t + dpp<ROR8>(t);
}
__device__ __forceinline__ float split_weight(f32x4 m4, f32x4 l4, int hh) {
  const bool h0 = hh & 1, h1 = hh & 2;
  const float m = sel4(h0, h1, m4[0], m4[1], m4[2], m4[3]), l = sel4(h0, h1, l4[0], l4[1], l4[2], l4[3]);
  const float M = xrow_max(fmx(m, dpp<ROR8>(m)));
  const float e = fexp(m - M);
  float Ls = l * e; Ls += dpp<ROR8>(Ls);
  return e * __builtin_amdgcn_rcpf(xrow_sum(Ls));
}
__device__ __forceinline__ f32x2 rbf2(f32x2 v) {
  const unsigned p = pack2(v.x, v.y);
  return f32x2{bflo(p), bfhi(p)};
}
template <bool MAX> __device__ __forceinline__ float red2(float a, float b) { return MAX ? fmx(a, b) : a + b; }
template <bool MAX> __device__ __forceinline__ f32x4 wave_red4(f32x4 s, int lane) {
  const bool o1 = lane & 1, o2 = lane & 2;
  const float a = red2<MAX>(o1 ? s[1] : s[0], dpp<XOR1>(o1 ? s[0] : s[1])), c = red2<MAX>(o1 ? s[3] : s[2], dpp<XOR1>(o1 ? s[2] : s[3]));
  float m = red2<MAX>(o2 ? c : a, dpp<XOR2>(o2 ? a : c));
  m = red2<MAX>(m, dpp<ROR4>(m)); m = red2<MAX>(m, dpp<ROR8>(m));
  m = MAX ? xrow_max(m) : xrow_sum(m);
  return f32x4{dpp<0x00>(m), dpp<0x55>(m), dpp<0xAA>(m), dpp<0xFF>(m)};
}
}

static __device__ __attribute__((noinline, used)) void code_begin_marker() { __builtin_amdgcn_s_sleep(0); }
static __device__ __attribute__((noinline, used)) void code_end_marker();

__global__ void __launch_bounds__(512, 2)
adaln_block_kernel(const bf16_t* __restrict__ x, const bf16_t* __restrict__ kc, const bf16_t* __restrict__ vc,
                   const bf16_t* __restrict__ wq, const bf16_t* __restrict__ wo, const bf16_t* __restrict__ wg,
                   const bf16_t* __restrict__ wu, const bf16_t* __restrict__ wd, const bf16_t* __restrict__ cc,
                   const bf16_t* __restrict__ wmod, bf16_t* __restrict__ out, unsigned char* ws) {
  __shared__ __attribute__((aligned(16))) unsigned char lds_raw[L_END];
  LAS unsigned char* L = (LAS unsigned char*)lds_raw;
  volatile LAS unsigned* FL = (volatile LAS unsigned*)(L + L_FLAG);
  LAS unsigned* FC = (LAS unsigned*)(L + L_FLAG);
  const unsigned long long tstart = __builtin_amdgcn_s_memrealtime();
  const int tid = threadIdx.x, lane = tid & 63, wv = __builtin_amdgcn_readfirstlane(tid >> 6);
  const int g = blockIdx.x, grp = (g & 7) * 4 + (g >> 6), b = grp >> 3, kvh = grp & 7, s = (g >> 3) & 7;
  const int n = lane & 15, kg = lane >> 4;
  if (wv == 3) {
    const unsigned long long c0 = (unsigned long long)(uintptr_t)&code_begin_marker, c1 = (unsigned long long)(uintptr_t)&code_end_marker;
    const int len = (int)(c1 - c0);
    const v4u rc = {(unsigned)c0, (unsigned)(c0 >> 32) & 0xFFFFu, (unsigned)(len > 0 ? len : 0), 0x00020000u};
    unsigned keep;
    asm volatile("s_mov_b32 %0, m0\n\ts_mov_b32 m0, %3\n\ts_nop 0\n\tbuffer_load_dwordx4 %1, %2, 0 offen lds\n\ts_mov_b32 m0, %0"
                 : "=&s"(keep) : "v"((g >> 3) * 1024 + lane * 16), "s"(rc), "s"((unsigned)(uintptr_t)(L + L_HB)) : "memory");
  }
  if (tid < 80) *(LAS v4u*)(L + L_ZERO + tid * 16) = v4u{0u, 0u, 0u, 0u};
  asm volatile("" :: "s"(x), "s"(kc), "s"(vc), "s"(wq), "s"(cc), "s"(wmod), "s"(ws));
  wg_barrier();
  const rsrc_t rws = mkrsrc(ws, WS_END);
  unsigned tag;
  v4u stv[2];
#define W_HEAD ((g & 7) + 8 * ((g - 128) >> 5))
#define W_CQ ((g >> 3) & 3)
#define ST_OFF(row) (WS_ST + (((row) * 8 + (W_HEAD >> 2)) * 8 + (lane >> 3)) * 128 + (lane & 3) * 32)

  if (__builtin_expect(wv >= 4, 1)) {
    const int w = wv - 4;
    const size_t kvbase = ((size_t)grp * SK + (SK - WIN) + s * 512) * 128;
    const int rot = (g * 37) & 127;
    const unsigned char* kbase = (const unsigned char*)(kc + kvbase);
    const unsigned ldsk = (unsigned)(uintptr_t)(L + L_K);
    const int klane = kg * 256 + ((n ^ kg) << 4);
    unsigned due0 = (unsigned)tstart + SCH_A;
#define PACE_WAIT(now, i) do { \
      const int late_ = (int)((now) - due0) - (((i) * SCH_T16) >> 4); \
      VMCNT(PHI); \
      if (late_ < LV2) { VMCNT(PMID); if (late_ < LV1) { VMCNT(PLO); if (late_ < LV0) VMCNT(PMIN); } } } while (0)
#pragma unroll 4
    for (int i = 0; i < 32; ++i) {
      dma16<true>(kbase + ((4 * i + w + rot) & 127) * 1024, klane ^ ((i & 3) << 6), ldsk + (4 * i + w) * 1024);
      if (i < PLO) continue;
      const unsigned now = (unsigned)__builtin_amdgcn_s_memrealtime();
      if (i == PLO + 1 && (int)(now - SCH_F - due0) > 0) due0 = now - SCH_F;
      PACE_WAIT(now, i);
    }
    { const unsigned anc = __builtin_amdgcn_readfirstlane(FL[F_ANC]); if (anc) due0 = anc + SCH_G; }
    const unsigned epoch = __builtin_amdgcn_raw_buffer_load_b32(rws, WS_EPOCH, 0, AUX_SC1);
    const rsrc_t rv = mkrsrc(vc + kvbase, 131072);
    v4u V[32], VT[24], VL[8];
    constexpr int TR0 = 8 + PHI;
#pragma unroll
    for (int i = 0; i < 32; ++i) {
      V[i] = bld<AUX_NT>(rv, lane * 16, ((4 * i + w + rot) & 127) * 1024);
      const unsigned now = (unsigned)__builtin_amdgcn_s_memrealtime();
      if (i >= TR0) { const int k = i - TR0; VT[k] = tcol8r(V + 8 * (k >> 3), k & 7); PIN(VT[k]); }
      if (i == PLO - 1) { VMCNT(PLO); FL[F_K + w] = 1u; }
      else if (i < 31) PACE_WAIT(now, 32 + i);
      SB();
    }
    FL[F_VI + w] = 1u;
    tag = __builtin_amdgcn_readfirstlane(epoch) + 0x01010101u;
#pragma unroll
    for (int k = 32 - TR0; k < 24; ++k) { VT[k] = tcol8r(V + 8 * (k >> 3), k & 7); PIN(VT[k]); }
    lds_wait(FL + F_PCNT, 4u);
    f32x4 f;
    {
      const LAS f32x4* sp = (const LAS f32x4*)(L + L_F);
      const f32x4 m0 = sp[0], m1 = sp[2], m2 = sp[4], m3 = sp[6], mo = sp[2 * w];
#pragma unroll
      for (int h = 0; h < 4; ++h) f[h] = fexp(mo[h] - fmx(fmx(m0[h], m1[h]), fmx(m2[h], m3[h])));
    }
    f32x4 acc[2] = {f32x4{0.f, 0.f, 0.f, 0.f}, f32x4{0.f, 0.f, 0.f, 0.f}};
    int paddr[4];
#pragma unroll
    for (int ep = 0; ep < 4; ++ep) paddr[ep] = (n >> 2) == ep ? L_P + w * 1024 + (((n & 3) * 4 + kg) << 4) : L_ZERO;
    int mra = L_MRG + (w * 512 + 8 * n + 2 * kg) * 4;
#pragma unroll
    for (int G = 0; G < 4; ++G) {
      v4u pa[4];
#pragma unroll
      for (int ep = 0; ep < 4; ++ep) pa[ep] = *(const LAS v4u*)(L + paddr[ep] + G * 256);
      if (G == 3) {
        PIN(pa[0]); PIN(pa[1]); PIN(pa[2]); PIN(pa[3]); PIN(f); PIN(mra); PIN(acc[0]); PIN(acc[1]);
#pragma unroll
        for (int jp = 0; jp < 4; ++jp) {
#pragma unroll
          for (int e = 0; e < 8; ++e) {
            unsigned t = __builtin_amdgcn_perm(V[25 + 2 * jp][e >> 1], V[24 + 2 * jp][e >> 1], (e & 1) ? 0x07060302u : 0x05040100u);
            if (jp < 3) PIN(t);
            VL[e][jp] = t;
          }
          if (jp < 3) SB();
        }
      }
#define PV(E) acc[E & 1] = mfma32(pa[E >> 1], G < 3 ? VT[8 * G + E] : VL[E], acc[E & 1]);
      FOR8(PV)
#undef PV
      SB();
    }
#pragma unroll
    for (int h = 0; h < 4; ++h) *(LAS f32x2*)(L + mra + h * 512) = f32x2{acc[0][h], acc[1][h]} * f[h];
    wg_barrier();
    SB();
  } else {
    const int cw = wv;
    __builtin_amdgcn_s_setprio(2);
    const int ks = g >> 5, ns = g & 31, u5 = lane & 31, h5 = lane >> 5;
    const int mkq = (g < 64 ? g >> 4 : (g - 64) >> 5) & 3, mcb = g < 64 ? g & 15 : 16 + ((g - 64) & 31);
    v2u wm[8], wr[8];
    const rsrc_t rm = mkrsrc(wmod + (size_t)(mkq * 128 + cw * 32) * 3072, g < 192 ? 32 * 6144 : 0);
    const rsrc_t rq = mkrsrc(wq + (size_t)(ks * 64 + (cw & 1) * 32) * 4096, 32 * 8192);
    const v4u cf = *(const v4u*)(cc + (n >> 2) * 512 + 128 * mkq + 32 * cw + 8 * kg);
#pragma unroll
    for (int j = 0; j < 8; ++j) wm[j] = __builtin_amdgcn_raw_buffer_load_b64(rm, 8 * kg * 6144 + mcb * 128 + n * 8, j * 6144, AUX_NT);
    const unsigned epoch = __builtin_amdgcn_raw_buffer_load_b32(rws, WS_EPOCH, 0, AUX_SC1);
    const v4u xs = *(const v4u*)(x + cw * 512 + lane * 8);
    const v2u xo = *(const v2u*)(x + cw * 512 + 64 * ks + 4 * n);
#pragma unroll
    for (int j = 0; j < 8; ++j) wr[j] = __builtin_amdgcn_raw_buffer_load_b64(rq, 8 * kg * 8192 + ns * 256 + (cw >> 1) * 128 + n * 8, j * 8192, AUX_NT);
    SB();
    v4u bq[4];
    unsigned long long tfirst;
    {
      v4u a, bt[4];
#pragma unroll
      for (int i = 0; i < 4; ++i) a[i] = pack2(silu_f(bflo(cf[i])), silu_f(bfhi(cf[i])));
      PIN(a);
      tcol4x8(wm, bt);
      tfirst = __builtin_amdgcn_s_memrealtime();
      *(LAS f32x4*)(L + L_MRG + cw * 1024 + lane * 16) = gemv32x64(a, bt);
      lds_arrive(FC + F_MCNT, lane);
      tag = __builtin_amdgcn_readfirstlane(epoch) + 0x01010101u;
      if (cw == 0 && g < 192) {
        lds_wait(FL + F_MCNT, 4u);
        const LAS f32x4* mp = (const LAS f32x4*)(L + L_MRG + lane * 16);
        st16(rws, WS_MOD + ((mkq * 4 + kg) * 3072 + 64 * mcb + 4 * n) * 4, put4r((mp[0] + mp[64]) + (mp[128] + mp[192]), tag));
        if (g < 64 && lane == 0) { const float t = __uint_as_float((unsigned)__builtin_amdgcn_s_memrealtime() << 4); st16(rws, WS_MT + g * 16, put4(f32x4{t, t, t, t}, tag)); }
      }
    }
    float ln[4];
    {
      float sum = 0.f;
#pragma unroll
      for (int i = 0; i < 4; ++i) sum += bflo(xs[i]) + bfhi(xs[i]);
      const float mean = wave_sum(sum) * (1.f / 512.f);
      float sq = 0.f;
#pragma unroll
      for (int i = 0; i < 4; ++i) { const float d0 = bflo(xs[i]) - mean, d1 = bfhi(xs[i]) - mean; sq = fmaf(d0, d0, sq); sq = fmaf(d1, d1, sq); }
      sq = wave_sum(sq);
      const float mb = rbf(mean), den = rbf(sqrtf(rbf(rbf(sq * (1.f / 512.f)) + 1e-5f)));
#pragma unroll
      for (int i = 0; i < 2; ++i) { ln[2 * i] = rbf(rbf(bflo(xo[i]) - mb) / den); ln[2 * i + 1] = rbf(rbf(bfhi(xo[i]) - mb) / den); }
    }
    tcol4x8(wr, bq);
    wait_until(tstart, T_MOD);
    wait_until(tfirst, T_MODF);
    {
      const int mo = WS_MOD + ((h5 * 4 + cw) * 3072 + (kg & 1) * 512 + 64 * ks + 4 * n) * 4;
      const int off[2] = {mo, mo + 2 * 4 * 3072 * 4};
      f32x4 v[2];
      v4u mt;
      if (cw == 3) mt = bld<AUX_SC1>(rws, WS_MT + lane * 16, 0);
      sweep<2, 8>(rws, off, v, tag);
      if (cw == 3) {
        const unsigned now = (unsigned)__builtin_amdgcn_s_memrealtime();
        const int d = (int)(mt.x - (now << 4)) >> 4;
        const bool ok = (tagdiff(mt, tag) & 0x0F0F0F0Fu) == 0u && d <= 0 && d > -2000;
        const float cnt = wave_sum(ok ? 1.f : 0.f), sum = wave_sum(ok ? (float)d : 0.f);
        FL[F_ANC] = (now + (cnt > 0.f ? (int)(sum / cnt) : -300)) | 1u;
      }
      const f32x4 t = v[0] + v[1];
      float h[4];
#pragma unroll
      for (int e = 0; e < 4; ++e) {
        auto p = __builtin_amdgcn_permlane32_swap(__float_as_uint(t[e]), __float_as_uint(t[e]), false, false);
        const float u = rbf(__uint_as_float(p[0]) + __uint_as_float(p[1]));
        auto r = __builtin_amdgcn_permlane16_swap(__float_as_uint(u), __float_as_uint(u), false, false);
        h[e] = rbf(ln[e] * rbf(1.f + __uint_as_float(r[1]))) + __uint_as_float(r[0]);
      }
      if (lane < 16) *(LAS v2u*)(L + L_HQ + cw * 128 + n * 8) = v2u{pack2(h[0], h[1]), pack2(h[2], h[3])};
      lds_arrive(FC + F_HCNT, lane);
      lds_wait(FL + F_HCNT, 4u);
      const v4u a = *(const LAS v4u*)(L + L_HQ + (n >> 2) * 128 + (cw & 1) * 64 + kg * 16);
      const f32x4 qp = gemv32x64(a, bq);
      if (cw & 1) {
        *(LAS f32x4*)(L + L_MRG + 4096 + (cw >> 1) * 1024 + lane * 16) = qp;
        lds_set(FL + F_G + (cw >> 1));
      } else {
        lds_wait(FL + F_G + (cw >> 1));
        st16(rws, WS_QP + ((kg * 8 + ks) * 4096 + 128 * ns + 64 * (cw >> 1) + 4 * n) * 4, put4r(qp + *(const LAS f32x4*)(L + L_MRG + 4096 + (cw >> 1) * 1024 + lane * 16), tag));
      }
    }
    if (cw == 3 && g >= 192) {
      const int rr = g - 192;
      const int mo = WS_MOD + ((((lane >> 3) & 3) * 4 + (rr >> 4)) * 3072 + 1024 + h5 * 512 + 32 * (rr & 15) + 4 * (lane & 7)) * 4;
      const int off[2] = {mo, mo + 4096};
      f32x4 v[2];
      const unsigned short xin = x[rr * 32 + u5];
      sweep<2, 8>(rws, off, v, tag);
#pragma unroll
      for (int i = 0; i < 2; ++i)
#pragma unroll
        for (int e = 0; e < 4; ++e) {
          float u = v[i][e]; u += dpp<ROR8>(u);
          auto p = __builtin_amdgcn_permlane16_swap(__float_as_uint(u), __float_as_uint(u), false, false);
          v[i][e] = rbf(__uint_as_float(p[0]) + __uint_as_float(p[1]));
        }
      *(LAS float*)(L + L_GG + 256 + u5 * 4) = bflo(xin);
      if (u5 < 8) {
        if (lane < 32) {
          *(LAS f32x4*)(L + L_GG + lane * 16) = v[0];
          st16(rws, WS_CB + rr * 128 + lane * 16, put4(f32x4{rbf(1.f + v[1][0]), rbf(1.f + v[1][1]), rbf(1.f + v[1][2]), rbf(1.f + v[1][3])}, tag));
        } else {
          *(LAS f32x4*)(L + L_GG + 128 + u5 * 16) = v[1];
          st16(rws, WS_SB + rr * 128 + u5 * 16, put4(v[0], tag));
        }
      }
    }
    const int qo = WS_QP + ((b * 8 + h5) * 4096 + kvh * 512 + cw * 128 + 4 * u5) * 4;
    {
      unsigned tq = __builtin_amdgcn_readfirstlane(FL[F_ANC]) + T_Q;
      { const int late = (int)(tq - (unsigned)tstart) - T_Q - QSL; if (late > 0) tq += late; }
#pragma unroll 1
      for (int i = 0; i < 4096 && (int)((unsigned)__builtin_amdgcn_s_memrealtime() - tq) < 0; ++i) __builtin_amdgcn_s_sleep(1);
    }
    lds_wait(FL + F_K + cw);
    v4u qv[4];
#pragma unroll
    for (int i = 0; i < 4; ++i) qv[i] = bld<AUX_SC1>(rws, qo, i * 32768);
    SB();
    v4u kf[32];
#pragma unroll
    for (int t = 0; t < 8; ++t)
#pragma unroll
      for (int ds = 0; ds < 4; ++ds)
        kf[4 * t + ds] = *(const LAS v4u*)(L + L_K + (16 * (4 * t + (n >> 2)) + 4 * cw + (n & 3)) * 256 + (((4 * ds + kg) ^ n) << 4));
    SB();
    {
#pragma unroll 1
      for (unsigned spin = 0; spin < SPIN_G; ++spin) {
        if (all_ok(tagdiff(qv[0], tag) | tagdiff(qv[1], tag) | tagdiff(qv[2], tag) | tagdiff(qv[3], tag))) break;
        __builtin_amdgcn_s_sleep(8);
#pragma unroll
        for (int i = 0; i < 4; ++i) if (!all_ok(tagdiff(qv[i], tag))) qv[i] = bld<AUX_SC1>(rws, qo, i * 32768);
      }
      const f32x4 t = (get4(qv[0]) + get4(qv[1])) + (get4(qv[2]) + get4(qv[3]));
      float q[4];
#pragma unroll
      for (int e = 0; e < 4; ++e) {
        auto p = __builtin_amdgcn_permlane32_swap(__float_as_uint(t[e]), __float_as_uint(t[e]), false, false);
        q[e] = __uint_as_float(p[0]) + __uint_as_float(p[1]);
      }
      if (lane < 32) *(LAS v2u*)(L + L_QB + cw * 320 + u5 * 8) = v2u{pack2(q[0], q[1]), pack2(q[2], q[3])};
      lds_arrive(FC + F_QCNT, lane);
      lds_wait(FL + F_QCNT, 4u);
    }
    {
      f32x4 sc[2];
      v4u qf[4];
#pragma unroll
      for (int ds = 0; ds < 4; ++ds) qf[ds] = *(const LAS v4u*)(L + L_QB + (n & 3) * 320 + kg * 16 + ds * 64);
      f32x4 c[8];
#pragma unroll
      for (int t = 0; t < 8; ++t) {
        c[t] = f32x4{0.f, 0.f, 0.f, 0.f};
#pragma unroll
        for (int ds = 0; ds < 4; ++ds) c[t] = mfma32(qf[ds], kf[4 * t + ds], c[t]);
      }
#pragma unroll
      for (int sg = 0; sg < 2; ++sg)
#pragma unroll
        for (int h = 0; h < 4; ++h) sc[sg][h] = sel4(lane & 16, lane & 32, c[4 * sg][h], c[4 * sg + 1][h], c[4 * sg + 2][h], c[4 * sg + 3][h]);
      f32x4 e0, e1, mx;
#pragma unroll
      for (int h = 0; h < 4; ++h) {
        const f32x2 r = rbf2(rbf2(f32x2{sc[0][h], sc[1][h]}) * 0.08838834764831845f);
        e0[h] = r.x; e1[h] = r.y; mx[h] = fmx(r.x, r.y);
      }
      const f32x4 mw = wave_red4<true>(mx, lane);
#pragma unroll
      for (int h = 0; h < 4; ++h) {
        e0[h] = fexp(e0[h] - mw[h]); e1[h] = fexp(e1[h] - mw[h]);
        const unsigned pp = pack2(e0[h], e1[h]);
        LAS unsigned short* pd = (LAS unsigned short*)(L + L_P) + ((((cw * 4 + (kg >> 1)) * 4 + h) * 4 + (n & 3)) * 8 + 4 * (kg & 1) + (n >> 2));
        pd[0] = (unsigned short)(pp & 0xFFFFu);
        pd[2 * 128] = (unsigned short)(pp >> 16);
      }
      const f32x4 lw = wave_red4<false>(e0 + e1, lane);
      if (lane < 2) *(LAS f32x4*)(L + L_F + cw * 32 + lane * 16) = lane == 0 ? mw : lw;
      lds_arrive(FC + F_PCNT, lane);
    }
    lds_wait(FL + F_PCNT, 4u);
    if (cw == 3) {
      const LAS f32x4* sp = (const LAS f32x4*)(L + L_F);
      f32x4 M, Ls;
#pragma unroll
      for (int h = 0; h < 4; ++h) {
        M[h] = fmx(fmx(sp[0][h], sp[2][h]), fmx(sp[4][h], sp[6][h]));
        Ls[h] = (sp[1][h] * fexp(sp[0][h] - M[h]) + sp[3][h] * fexp(sp[2][h] - M[h])) + (sp[5][h] * fexp(sp[4][h] - M[h]) + sp[7][h] * fexp(sp[6][h] - M[h]));
      }
      if (lane < 8) st16(rws, WS_ST + (grp * 8 + s) * 128 + lane * 16, put4((lane & 1) == 0 ? M : Ls, tag));
    }
    if (g >= 128) {
      const unsigned char* wsrc = (const unsigned char*)(wo + (size_t)(W_HEAD * 128 + cw * 32) * 512 + W_CQ * 128);
      const int vo = kg * 1024 + ((n ^ (kg << 1)) << 4), sto = ST_OFF(cw);
      lds_wait(FL + F_VI + cw);
      __builtin_amdgcn_s_sleep(WO_DLY);
      stv[0] = bld<AUX_SC1>(rws, sto, 0); stv[1] = bld<AUX_SC1>(rws, sto + 16, 0);
#pragma unroll
      for (int j = 0; j < 8; ++j) dma16<true>(wsrc + j * 4096, vo ^ ((j & 2) << 6), (unsigned)(uintptr_t)(L + L_WO) + cw * 8192 + j * 1024);
      SB();
    }
    int h1o = WS_H1 + (grp * 8 + s) * 2048 + tid * 16;
    PIN(h1o);
    wg_barrier();
    if (cw < 2) {
      const LAS f32x4* mp = (const LAS f32x4*)(L + L_MRG + tid * 16);
      st16(rws, h1o, put4((mp[0] + mp[128]) + (mp[256] + mp[384]), tag));
    }
    SB();
  }
  if (g >= 128) {
    const unsigned long long tp = __builtin_amdgcn_s_memrealtime();
    const int whead = W_HEAD, wcq = W_CQ;
    int csa = L_CS + (wv >> 1) * 32 + (lane >> 3) * 4;
    int h2o = WS_H2 + ((kg * 16 + wcq * 4 + wv) * 32 + whead) * 128 + n * 8;
    int oar = L_OA + (n >> 2) * 320 + kg * 16, oaw = L_OA + (wv >> 1) * 320 + (wv & 1) * 128 + n * 8 + kg * 2;
    int wtr = L_WO + (8 * kg + (n >> 2)) * 256 + (((4 * wv + ((n >> 1) & 1)) ^ (((n >> 2) | ((kg & 1) << 2)) << 1)) << 4) + (n & 1) * 8, wtr1 = wtr ^ 32;
    unsigned tg2 = tag >> ((n & 1) * 16);
    PIN(h2o); PIN(oar); PIN(oaw); PIN(csa); PIN(tg2); PIN(wtr); PIN(wtr1);
    SB();
    if (wv < 4) {
      if (!all_ok(tagdiff(stv[0], tag) | tagdiff(stv[1], tag))) {
        const int so[2] = {ST_OFF(wv), ST_OFF(wv) + 16};
        sweep_raw<2, 0>(rws, so, stv, tag);
      }
      *(LAS float*)(L + L_CS + wv * 32 + (lane >> 3) * 4) = split_weight(get4(stv[0]), get4(stv[1]), whead);
      CBAR();
      FL[F_CS + wv] = 1u;
    }
    {
      const int ho = WS_H1 + (((wv >> 1) * 8 + (whead >> 2)) * 8 + (lane >> 3)) * 2048 + (whead & 3) * 512 + (wv & 1) * 256 + (lane & 7) * 16;
      const int off[2] = {ho, ho + 128};
      f32x4 v[2];
      wait_until(tp, H1DLY);
      sweep<2, 2>(rws, off, v, tag);
      float cs;
#pragma unroll 1
      for (unsigned i = 0; i < SPIN_L; ++i) {
        const unsigned fl = FL[F_CS + (wv >> 1)];
        cs = *(const volatile LAS float*)(L + csa);
        if (fl) break;
      }
      const float o0 = part_sum(v[0] * cs), o1 = part_sum(v[1] * cs);
      *(LAS unsigned short*)(L + oaw) = (unsigned short)pack2((lane & 8) ? o1 : o0, 0.f);
    }
    VMCNT(0);
    wg_barrier();
    if (wv < 4) {
      v4u a[4]; v2u bl[8], bh[8];
#pragma unroll
      for (int k = 0; k < 4; ++k) {
        a[k] = *(const LAS v4u*)(L + oar + k * 64);
        bl[2 * k] = lds_tr(L + wtr + k * 8192); bh[2 * k] = lds_tr(L + wtr + k * 8192 + 1024);
        bl[2 * k + 1] = lds_tr(L + wtr1 + k * 8192); bh[2 * k + 1] = lds_tr(L + wtr1 + k * 8192 + 1024);
      }
      SB();
      f32x4 c0 = {0.f, 0.f, 0.f, 0.f}, c1 = {0.f, 0.f, 0.f, 0.f};
#pragma unroll
      for (int k = 0; k < 4; ++k) {
        c0 = mfma32(a[k], v4u{bl[2 * k].x, bl[2 * k].y, bh[2 * k].x, bh[2 * k].y}, c0);
        c1 = mfma32(a[k], v4u{bl[2 * k + 1].x, bl[2 * k + 1].y, bh[2 * k + 1].x, bh[2 * k + 1].y}, c1);
      }
      __builtin_amdgcn_raw_buffer_store_b64(v2u{put1(c0[0], tg2), put1(c1[0], tg2 >> 8)}, rws, h2o, 0, AUX_SC1);
    }
    SB();
    if (g >= 192) {
      const int rr = g - 192, col = 16 * ((lane >> 2) & 1) + 4 * kg + (lane & 3);
      float x1 = 0.f, gb = 0.f;
      int oo = (rr * 32 + col) * 2, rda = L_RED + col * 4;
      PIN(oo); PIN(rda);
      if (wv == 0) {
        const int pc = 2 * (lane & 7) + (kg >> 1) + 16 * (kg & 1);
        float ga = *(const LAS float*)(L + L_GG + pc * 4), xin = *(const LAS float*)(L + L_GG + 256 + pc * 4);
        gb = *(const LAS float*)(L + L_GG + 128 + col * 4);
        int x1o = WS_X1 + (n >> 3) * 8192 + rr * 128 + pc * 4;
        unsigned tg1 = tag >> ((pc & 3) * 8);
        PIN(ga); PIN(xin); PIN(x1o); PIN(gb); PIN(tg1);
        const int ho = WS_H2 + rr * 4096 + lane * 16;
        const int off[4] = {ho, ho + 1024, ho + 2048, ho + 3072};
        f32x4 v[4];
        __builtin_amdgcn_s_sleep(H2DLY);
        sweep<4, 0>(rws, off, v, tag);
        x1 = rbf(xin + rbf(ga * rbf(part_sum((v[0] + v[1]) + (v[2] + v[3])))));
        __builtin_amdgcn_raw_buffer_store_b32(put1(x1, tg1), rws, x1o, 0, AUX_SC1);
        SB();
        *(LAS float*)(L + L_GG + 384 + pc * 4) = x1;
        LGKM0();
        x1 = *(const LAS float*)(L + L_GG + 384 + col * 4);
        PIN(x1);
      }
      if (wv == 7) {
        const int off[1] = {WS_X1 + (g & 1) * 8192 + lane * 128};
        v4u v[1];
        sweep_raw<1, 0>(rws, off, v, tag);
        __builtin_amdgcn_s_sleep(DLY_H4);
        FL[F_H4] = 1u;
      } else {
        const int off[2] = {WS_H4 + rr * 14336 + wv * 2048 + lane * 16, WS_H4 + rr * 14336 + wv * 2048 + 1024 + lane * 16};
        f32x4 v[2];
        lds_wait(FL + F_H4);
        sweep<2, 0>(rws, off, v, tag);
        *(LAS float*)(L + rda + wv * 128) = part_sum(v[0] + v[1]);
      }
      wg_barrier();
      if (wv == 0) {
        const LAS float* rp = (const LAS float*)(L + rda);
        const float a = ((rp[0] + rp[32]) + (rp[64] + rp[96])) + ((rp[128] + rp[160]) + rp[192]);
        const unsigned ob = pack2(x1 + rbf(gb * rbf(a)), 0.f);
        if (n < 8) __builtin_amdgcn_raw_buffer_store_b16((unsigned short)ob, mkrsrc(out, 4096), oo, 0, AUX_SC1);
        if (g == 192 && lane == 0) __builtin_amdgcn_raw_buffer_store_b32(tag, rws, WS_EPOCH, 0, AUX_SC1);
      }
    }
  } else if (((g >> 5) << 3) + (g & 7) < 28) {
    const int jsl = 4 * (((g >> 5) << 3) + (g & 7)) + ((g >> 3) & 3);
    __builtin_amdgcn_s_barrier();
    const int sco[2] = {WS_SB + wv * 1024 + lane * 16, WS_CB + wv * 1024 + lane * 16};
    v4u scr[2];
    scr[0] = bld<AUX_SC1>(rws, sco[0], 0); scr[1] = bld<AUX_SC1>(rws, sco[1], 0);
    SB();
    {
      const unsigned char* gsrc = (const unsigned char*)(((lane & 2) ? wu : wg) + (size_t)(32 * ((lane >> 2) & 1) + 8 * (lane >> 5) + ((lane >> 3) & 3)) * FF + 16 * jsl + 8 * ((lane ^ (lane >> 4)) & 1));
#pragma unroll
      for (int j = 0; j < 4; ++j) {
        const int k = (8 * j + wv + 8 * ((g >> 3) & 3)) & 31;
        dma16v(gsrc + (64 * (k >> 2) + 4 * ((k >> 1) & 1) + 16 * (k & 1)) * (FF * 2), (unsigned)(uintptr_t)(L + L_FB) + k * 1024);
      }
#pragma unroll
      for (int j = 0; j < 2; ++j)
        dma16<true>(wd + (size_t)(16 * jsl + 2 * wv + j) * 512, (lane ^ (j | ((wv & 1) << 3))) << 4, (unsigned)(uintptr_t)(L + L_DN) + (2 * wv + j) * 1024);
    }
    v4u bg[8], bd[2], a[8];
    const int rb = wv >> 1, hf = wv & 1;
    int hbw = L_HB + rb * 1024 + (rb >> 1) * 32 + hf * 512 + lane * 8;
    int hbr = L_HB + (n >> 2) * 1024 + (n >> 3) * 32 + (32 * (n & 1) + 8 * kg) * 2;
    int acr = ((n & 1) == (kg >> 1)) ? L_ACT + (n >> 2) * 32 + (kg & 1) * 16 : L_ZERO, acw = L_ACT + kg * 32 + wv * 8 + (n & 3) * 2;
    int h4o = WS_H4 + ((kg * 16 + 2 * wv + (n >> 3)) * 112 + jsl) * 128 + (n & 7) * 16;
    int dtr = L_DN + (8 * (kg & 1) + (n >> 2)) * 1024 + (((8 * wv + 2 * (n & 3)) ^ (((n >> 2) & 1) | (n & 8))) << 4) + (kg >> 1) * 8;
    PIN(hbw); PIN(hbr); PIN(acr); PIN(acw); PIN(h4o); PIN(dtr);
    VMCNT(2);
    wg_barrier();
    if (wv < 4) {
      LAS unsigned char* gt = L + L_FB + (kg >> 1) * 1024 + (lane & 31) * 32 + (((wv >> 1) ^ (n >> 3)) << 4) + (wv & 1) * 8;
#pragma unroll
      for (int t = 0; t < 8; ++t) {
        const v2u lo = lds_tr(gt + t * 4096), hi = lds_tr(gt + t * 4096 + 2048);
        bg[t] = v4u{lo.x, lo.y, hi.x, hi.y};
      }
    }
    if (!all_ok(tagdiff(scr[0], tag) | tagdiff(scr[1], tag))) sweep_raw<2, 0>(rws, sco, scr, tag);
    f32x4 shv = get4(scr[0]), scv = get4(scr[1]);
    PIN(shv); PIN(scv);
    SB();
    {
      const int xr = WS_X1 + (g & 1) * 8192 + rb * 2048 + lane * 16;
      const int off[2] = {xr + hf * 1024, xr + (hf ^ 1) * 1024};
      v4u r[2];
      sweep_raw<2, 0>(rws, off, r, tag);
      const unsigned p0 = __builtin_amdgcn_perm(r[0].y, r[0].x, 0x07060302u), p1 = __builtin_amdgcn_perm(r[0].w, r[0].z, 0x07060302u);
      const unsigned p2 = __builtin_amdgcn_perm(r[1].y, r[1].x, 0x07060302u), p3 = __builtin_amdgcn_perm(r[1].w, r[1].z, 0x07060302u);
      const float s1 = dot2(p1, 0x3F803F80u, dot2(p0, 0x3F803F80u, 0.f)) + dot2(p3, 0x3F803F80u, dot2(p2, 0x3F803F80u, 0.f));
      const float s2 = dot2(p1, p1, dot2(p0, p0, 0.f)) + dot2(p3, p3, dot2(p2, p2, 0.f));
      auto sw = __builtin_amdgcn_permlane32_swap(__float_as_uint(s1), __float_as_uint(s2), false, false);
      float t = __uint_as_float(sw[0]) + __uint_as_float(sw[1]);
      t += dpp<XOR1>(t); t += dpp<XOR2>(t); t += dpp<ROR4>(t); t += dpp<ROR8>(t);
      const float mean = (__uint_as_float(__builtin_amdgcn_readlane(__float_as_uint(t), 0)) + __uint_as_float(__builtin_amdgcn_readlane(__float_as_uint(t), 16))) * (1.f / 512.f);
      const float msq = (__uint_as_float(__builtin_amdgcn_readlane(__float_as_uint(t), 32)) + __uint_as_float(__builtin_amdgcn_readlane(__float_as_uint(t), 48))) * (1.f / 512.f);
      const float mb = rbf(mean), rden = __builtin_amdgcn_rcpf(rbf(__builtin_amdgcn_sqrtf(rbf(rbf(fmx(fmaf(-mean, mean, msq), 0.f)) + 1e-5f))));
      const f32x2 da = rbf2(f32x2{bflo(p0), bfhi(p0)} - mb), db = rbf2(f32x2{bflo(p1), bfhi(p1)} - mb);
      const f32x2 na = rbf2(da * rden), nb = rbf2(db * rden);
      const f32x2 ha = rbf2(na * f32x2{scv[0], scv[1]}) + f32x2{shv[0], shv[1]}, hb = rbf2(nb * f32x2{scv[2], scv[3]}) + f32x2{shv[2], shv[3]};
      *(LAS v2u*)(L + hbw) = v2u{pack2(ha.x, ha.y), pack2(hb.x, hb.y)};
    }
    VMCNT(0);
    wg_barrier();
    if (wv < 4) {
#pragma unroll
      for (int t = 0; t < 8; ++t) a[t] = *(const LAS v4u*)(L + hbr + t * 128);
    }
#pragma unroll
    for (int m2 = 0; m2 < 2; ++m2) {
      const v2u lo = lds_tr(L + (dtr ^ (m2 << 4))), hi = lds_tr(L + (dtr ^ (m2 << 4)) + 4096);
      bd[m2] = v4u{lo.x, lo.y, hi.x, hi.y};
    }
    SB();
    if (wv < 4) {
      f32x4 c = {0.f, 0.f, 0.f, 0.f};
#pragma unroll
      for (int t = 0; t < 8; ++t) c = mfma32(a[t], bg[t], c);
      float gs = (lane & 8) ? c[1] : c[0];
      gs += dpp<ROR8>(gs);
      gs = rbf(gs);
      const float act = rbf(gelu_f(gs)) * dpp<ROR4>(gs);
      if (n < 4) *(LAS unsigned short*)(L + acw) = (unsigned short)pack2(act, 0.f);
    }
    wg_barrier();
    {
      const v4u aa = *(const LAS v4u*)(L + acr);
      const f32x4 c0 = mfma32(aa, bd[0], f32x4{0.f, 0.f, 0.f, 0.f}), c1 = mfma32(aa, bd[1], f32x4{0.f, 0.f, 0.f, 0.f});
      st16(rws, h4o, put4(f32x4{c0[0], c0[1], c1[0], c1[1]}, tag));
    }
  }
}

static __device__ __attribute__((noinline, used)) void code_end_marker() { __builtin_amdgcn_s_sleep(1); }

extern "C" void kernel_launch(void* const* d_in, const int* in_sizes, int n_in, void* d_out, int out_size, void* d_ws, size_t ws_size, hipStream_t stream) {
  adaln_block_kernel<<<256, 512, 0, stream>>>((const bf16_t*)d_in[0], (const bf16_t*)d_in[1], (const bf16_t*)d_in[2], (const bf16_t*)d_in[3],
                                              (const bf16_t*)d_in[4], (const bf16_t*)d_in[6], (const bf16_t*)d_in[7], (const bf16_t*)d_in[8],
                                              (const bf16_t*)d_in[9], (const bf16_t*)d_in[10], (bf16_t*)d_out, (unsigned char*)d_ws);
}
```
